```python
import torch

def _softmax(x, axis=-1):
    return torch.softmax(x, dim=axis)
def _gelu(x):
    return torch.nn.functional.gelu(x, approximate="tanh")
def _silu(x):
    return torch.nn.functional.silu(x)
def _sigmoid(x):
    return torch.sigmoid(x)
def _layernorm(x, axis=-1, eps=1e-5):
    m = x.mean(dim=axis, keepdim=True)
    v = x.var(dim=axis, keepdim=True, unbiased=False)
    return (x - m) / torch.sqrt(v + eps)
def _scatter_add(tgt, idx, src, axis=0):
    return tgt.clone().index_add_(axis, idx, src)
def _segment_sum(src, seg, n):
    out = torch.zeros((n,) + src.shape[1:], dtype=src.dtype)
    return out.index_add_(0, seg, src)
def _pad(x, pw):
    flat = tuple(v for pair in reversed(pw) for v in pair)
    return torch.nn.functional.pad(x, flat)

kernel_name = 'sequential_preLN__ssm_conv_s4d__glu_gated_bilinear__prompt'

def setup_inputs():

    _w = (lambda a: (lambda t: t.to(torch.bfloat16) if t.is_floating_point() else t)(torch.as_tensor(a)))
    def _A_setup():
        Bsz, L, D, N = 1, 2048, 576, 128
        return {
            "u": _w((lambda *s: torch.randn(*s, dtype=torch.bfloat16) * 0.05)(Bsz, L, D)),
            "log_A_real": _w((lambda *s: torch.randn(*s, dtype=torch.bfloat16) * 6.4)(D, N) * 0.5),
            "A_imag": _w((lambda *s: torch.randn(*s, dtype=torch.bfloat16) * 0.05)(D, N) * 3.14),
            "C_real": _w((lambda *s: torch.randn(*s, dtype=torch.bfloat16) * 0.05)(D, N) / N ** 0.5),
            "C_imag": _w((lambda *s: torch.randn(*s, dtype=torch.bfloat16) * 0.05)(D, N) / N ** 0.5),
            "log_dt": _w((lambda *s: torch.randn(*s, dtype=torch.bfloat16) * 0.05)(D) * 0.5 - 4.0),
            "D_skip": _w((lambda *s: torch.randn(*s, dtype=torch.bfloat16) * 0.05)(D)),
        }
    _A_in = _A_setup()

    def _B_setup():
        B, S, D, F = 1, 2048, 576, 4608
        return {
            "x": _w((lambda *s: torch.randn(*s, dtype=torch.bfloat16) * 0.05)(B, S, D)),
            "w_gate": _w((lambda *s: torch.randn(*s, dtype=torch.bfloat16) * 0.4)(D, F) * (D ** -0.5)),
            "w_up": _w((lambda *s: torch.randn(*s, dtype=torch.bfloat16) * 0.4)(D, F) * (D ** -0.5)),
            "w_down": _w((lambda *s: torch.randn(*s, dtype=torch.bfloat16) * 0.4)(F, D) * (F ** -0.5)),
        }
    _B_in = _B_setup()

    return {
        "A_u": _A_in["u"],
        "A_log_A_real": _A_in["log_A_real"],
        "A_A_imag": _A_in["A_imag"],
        "A_C_real": _A_in["C_real"],
        "A_C_imag": _A_in["C_imag"],
        "A_log_dt": _A_in["log_dt"],
        "A_D_skip": _A_in["D_skip"],
        "B_x": _B_in["x"],
        "B_w_gate": _B_in["w_gate"],
        "B_w_up": _B_in["w_up"],
        "B_w_down": _B_in["w_down"],
    }

def reference(A_u, A_log_A_real, A_A_imag, A_C_real, A_C_imag, A_log_dt, A_D_skip, B_x, B_w_gate, B_w_up, B_w_down):
    def _A_ref(u, log_A_real, A_imag, C_real, C_imag, log_dt, D_skip):
        L = u.shape[1]
        dt = torch.exp(log_dt)
        A = -torch.exp(log_A_real) + 1j * A_imag
        C = (C_real + 1j * C_imag) * dt[:, None]
        pos = (lambda x, dt: x.to(dt))(torch.arange(L), log_dt.dtype)
        P = torch.exp((A * dt[:, None])[:, :, None] * pos[None, None, :])
        K = 2.0 * torch.einsum("dn,dnl->dl", C, P).real
        L2 = 2 * L
        u_t = (lambda x, *d: x.permute(*d))(u, 0, 2, 1)
        Kf = (lambda x, *a, **k: torch.fft.rfft(x.float(), *a, **k))(K, L2)
        Uf = (lambda x, *a, **k: torch.fft.rfft(x.float(), *a, **k))(u_t, L2)
        y = (lambda x, *a, **k: torch.fft.irfft(x, *a, **k).to(torch.bfloat16))(Uf * Kf[None, :, :], L2)[:, :, :L]
        y = (lambda x, *d: x.permute(*d))(y, 0, 2, 1)
        return y + u * D_skip[None, None, :]

    def _B_ref(x, w_gate, w_up, w_down):
        g = torch.einsum("bsd,df->bsf", x, w_gate)
        u = torch.einsum("bsd,df->bsf", x, w_up)
        h = g * u
        return torch.einsum("bsf,fd->bsd", h, w_down)

    x = A_u
    h = x + _A_ref(_layernorm(x, axis=-1), A_log_A_real, A_A_imag, A_C_real, A_C_imag, A_log_dt, A_D_skip)
    h2 = h + _B_ref(_layernorm(h, axis=-1), B_w_gate, B_w_up, B_w_down)
    return h2
```

```cpp
#include <hip/hip_runtime.h>
#include <stdint.h>
typedef unsigned short u16;
typedef unsigned u32x4 __attribute__((ext_vector_type(4)));
typedef __attribute__((address_space(1))) unsigned gu32;
struct Args {
  const u16 *x, *lar, *aim, *cre, *cim, *ldt, *dsk, *wg, *wu, *wd;
  char* ws; u16* out;
};

__global__ __launch_bounds__(512, 2) void k_fused(Args A) {
  __shared__ __attribute__((aligned(1024))) char smem[163840];
  unsigned tid = threadIdx.x;

  asm volatile(
    "Lst1_%=:\n"
    "s_load_dwordx16 s[64:79], s[0:1], 0x0\n"
    "s_load_dwordx8 s[28:35], s[0:1], 0x40\n"
    "v_readfirstlane_b32 s80, v239\n"
    "v_and_b32 v239, 63, v239\n"
    "v_mov_b32 v253, 0\n"
    "s_lshr_b32 s80, s80, 6\n"
    "s_cmp_lt_u32 s81, 144\n"
    "s_cselect_b32 s82, 1, 0\n"
    "s_cbranch_scc1 Lcv6_%=\n"
    "s_cmp_lt_u32 s81, 208\n"
    "s_cbranch_scc1 Lw07_%=\n"
    "s_cmp_lt_u32 s81, 248\n"
    "s_cbranch_scc1 Lrp8_%=\n"
    "s_lshl_b32 s44, s80, 13\n"
    "v_lshlrev_b32 v47, 7, v239\n"
    "s_getpc_b64 s[40:41]\n"
    "Lpc9_%=:\n"
    "s_sub_u32 s40, s40, Lpc9_%=-Lst1_%=\n"
    "s_subb_u32 s41, s41, 0\n"
    "s_and_b32 s41, s41, 0xffff\n"
    "s_mov_b32 s42, Lend2_%=-Lst1_%=\n"
    "s_mov_b32 s43, 0x20000\n"
    "buffer_load_dword v48, v47, s[40:43], s44 offen\n"
    "Lrp8_%=:\n"
    "s_add_i32 s40, s81, 8\n"
    "s_and_b32 s40, s40, 24\n"
    "s_or_b32 s40, s40, s80\n"
    "s_movk_i32 s41, 0x480\n"
    "v_lshl_or_b32 v150, s40, 6, v239\n"
    "s_and_b32 s40, s81, 7\n"
    "s_mulk_i32 s40, 0x90\n"
    "v_mov_b32_e32 v151, s40\n"
    "v_mad_u32_u24 v254, v150, s41, v151\n"
    "s_waitcnt lgkmcnt(0)\n"
    "global_load_dword v252, v253, s[32:33] sc1\n"
    "s_sub_u32 s40, s81, 216\n"
    "s_cmp_ge_u32 s40, 32\n"
    "s_cbranch_scc1 Lrole4_%=\n"
    "global_load_dword v0, v254, s[64:65]\n"
    "global_load_dword v1, v254, s[64:65] offset:140\n"
    "s_branch Lrole4_%=\n"
    "Lw07_%=:\n"
    "s_lshr_b32 s41, s81, 3\n"
    "s_lshl_b32 s40, s81, 3\n"
    "s_add_i32 s41, s41, 6\n"
    "s_and_b32 s40, s40, 56\n"
    "s_and_b32 s41, s41, 7\n"
    "s_or_b32 s40, s41, s40\n"
    "s_lshl_b32 s40, s40, 5\n"
    "s_lshl_b32 s41, s80, 2\n"
    "s_or_b32 s40, s40, s41\n"
    "v_lshrrev_b32_e32 v150, 4, v239\n"
    "v_or_b32_e32 v150, s40, v150\n"
    "v_and_b32_e32 v151, 15, v239\n"
    "v_mul_u32_u24_e32 v152, 0x480, v150\n"
    "s_movk_i32 s41, 0x480\n"
    "v_lshlrev_b32_e32 v153, 4, v151\n"
    "v_lshl_or_b32 v255, v151, 3, v152\n"
    "v_mov_b32_e32 v151, 0x1000\n"
    "s_lshl_b32 s40, s40, 5\n"
    "v_mad_u32_u24 v254, v150, s41, v153\n"
    "v_lshl_add_u32 v251, v150, 3, v151\n"
    "s_and_b32 s40, s40, 0x7f80\n"
    "v_lshlrev_b32_e32 v150, 2, v239\n"
    "s_add_i32 s40, s40, 0x22000\n"
    "v_and_b32_e32 v150, 0x7c, v150\n"
    "v_or_b32_e32 v250, s40, v150\n"
    "s_waitcnt lgkmcnt(0)\n"
    "global_load_dword v252, v253, s[32:33] sc1\n"
    "s_bitcmp1_b32 s81, 2\n"
    "s_cbranch_scc1 Lz10_%=\n"
    "global_store_dword v250, v253, s[32:33] sc1\n"
    "Lz10_%=:\n"
    "global_load_dwordx4 v[0:3], v254, s[64:65] offset:0\n"
    "global_load_dwordx4 v[4:7], v254, s[64:65] offset:256\n"
    "global_load_dwordx4 v[8:11], v254, s[64:65] offset:512\n"
    "global_load_dwordx4 v[12:15], v254, s[64:65] offset:768\n"
    "global_load_dwordx2 v[16:17], v255, s[64:65] offset:1024\n"
    "global_load_dword v18, v251, s[32:33] sc1\n"
    "s_branch Lrole4_%=\n"
    "Lcv6_%=:\n"
    "s_and_b32 s41, s81, 7\n"
    "s_lshr_b32 s40, s81, 3\n"
    "s_mul_i32 s41, s41, 18\n"
    "s_add_i32 s41, s41, s40\n"
    "s_lshl_b32 s42, s41, 10\n"
    "s_lshl_b32 s43, s80, 7\n"
    "s_or_b32 s42, s42, s43\n"
    "s_add_i32 s40, s40, s80\n"
    "s_and_b32 s40, s40, 3\n"
    "v_lshl_or_b32 v254, v239, 1, s42\n"
    "s_and_b32 s42, s80, 4\n"
    "s_or_b32 s40, s40, s42\n"
    "s_lshl_b32 s40, s40, 6\n"
    "v_mov_b32_e32 v150, 0x100\n"
    "s_lshl_b32 s57, s41, 3\n"
    "v_bitop3_b32 v150, s40, v150, v239 bitop3:0x36\n"
    "s_movk_i32 s40, 0x1200\n"
    "v_mov_b32_e32 v151, s57\n"
    "v_mad_u32_u24 v255, v150, s40, v151\n"
    "s_waitcnt lgkmcnt(0)\n"
    "global_load_ushort v240, v254, s[66:67]\n"
    "global_load_ushort v241, v254, s[68:69]\n"
    "global_load_ushort v242, v254, s[70:71]\n"
    "global_load_ushort v243, v254, s[72:73]\n"
    "s_load_dwordx2 s[60:61], s[74:75], s57\n"
    "s_load_dwordx2 s[62:63], s[76:77], s57\n"
    "global_load_dword v252, v253, s[32:33] sc1\n"
    "s_barrier\n"
    "s_cmp_lt_u32 s80, 4\n"
    "s_cbranch_scc1 Lon11_%=\n"
    "global_load_dwordx2 v[244:245], v255, s[64:65] offset:0\n"
    "Lon11_%=:\n"
    "s_cmp_lt_u32 s80, 4\n"
    "s_cbranch_scc1 Lon12_%=\n"
    "global_load_dwordx2 v[246:247], v255, s[64:65] offset:1152\n"
    "Lon12_%=:\n"
    "s_mov_b64 s[84:85], s[34:35]\n"
    "s_add_u32 s34, s32, 6291455\n"
    "s_addc_u32 s35, s33, 0\n"
    "s_and_b32 s34, s34, 0xffe00000\n"
    "s_mov_b32 s36, s32\n"
    "s_and_b32 s37, s33, 0xffff\n"
    "s_mov_b32 s38, 524288\n"
    "s_mov_b32 s39, 0x20000\n"
    "s_add_u32 s24, s36, 524288\n"
    "s_addc_u32 s25, s37, 0\n"
    "s_mov_b32 s26, 2359296\n"
    "s_mov_b32 s27, 0x20000\n"
    "s_lshr_b32 s41, s81, 3\n"
    "s_add_i32 s47, s41, s80\n"
    "s_and_b32 s47, s47, 3\n"
    "s_and_b32 s48, s80, 4\n"
    "s_or_b32 s47, s47, s48\n"
    "v_bfe_u32 v152, v239, 2, 2\n"
    "v_and_b32_e32 v153, 3, v239\n"
    "s_lshl_b32 s47, s47, 6\n"
    "v_mov_b32_e32 v154, 0x100\n"
    "v_min_u32_e32 v153, 2, v153\n"
    "v_lshlrev_b32_e32 v158, 1, v152\n"
    "v_and_b32_e32 v151, 15, v239\n"
    "s_lshr_b32 s43, s80, 2\n"
    "s_and_b32 s44, s80, 1\n"
    "v_bitop3_b32 v154, s47, v154, v239 bitop3:0x36\n"
    "s_lshl_b32 s47, s80, 8\n"
    "v_and_b32_e32 v155, 48, v239\n"
    "v_add_u32_e32 v159, v158, v153\n"
    "v_lshrrev_b32_e32 v150, 4, v239\n"
    "s_xor_b32 s45, s44, s43\n"
    "s_lshr_b32 s46, s80, 1\n"
    "v_or_b32_e32 v156, s47, v155\n"
    "s_movk_i32 s48, 0x820\n"
    "v_or_b32_e32 v159, 32, v159\n"
    "v_lshlrev_b32_e32 v161, 4, v151\n"
    "s_and_b32 s40, s81, 7\n"
    "s_and_b32 s42, s80, 3\n"
    "v_mad_u32_u24 v221, v151, s48, v156\n"
    "v_mad_u32_u24 v159, v159, s48, v156\n"
    "s_sub_i32 s45, 0, s45\n"
    "v_mad_u32_u24 v161, v150, s48, v161\n"
    "s_mul_i32 s48, s46, 0x2100\n"
    "v_lshlrev_b32_e32 v151, 5, v151\n"
    "v_sub_u32_e32 v162, s48, v151\n"
    "s_cmp_eq_u32 s44, s43\n"
    "s_movk_i32 s43, 0xdf8\n"
    "v_add_u32_e32 v162, 0x14000, v162\n"
    "s_cselect_b32 s43, s43, 0xbf0\n"
    "v_add_u32_e32 v163, s43, v162\n"
    "v_lshrrev_b32_e32 v164, 5, v239\n"
    "s_cselect_b32 s43, 4, 9\n"
    "v_bfe_u32 v160, v239, 3, 1\n"
    "v_lshlrev_b32_e32 v164, s43, v164\n"
    "s_cselect_b32 s43, -8, -16\n"
    "v_mul_u32_u24_e32 v160, 5, v160\n"
    "v_and_b32_e32 v158, 2, v158\n"
    "v_mov_b32_e32 v165, s43\n"
    "s_movk_i32 s43, 0xff6\n"
    "v_add3_u32 v158, v153, v160, v158\n"
    "s_cselect_b32 s43, s43, 0x7e0\n"
    "v_mul_u32_u24_e32 v158, 0x820, v158\n"
    "s_mov_b32 s49, 0x14d20\n"
    "v_mad_u32_u24 v153, v152, 3, v153\n"
    "v_add_u32_e32 v162, s43, v162\n"
    "s_cselect_b32 s43, 3, 9\n"
    "v_add3_u32 v158, v158, v156, s49\n"
    "v_mul_u32_u24_e32 v153, 0x820, v153\n"
    "s_mov_b32 s49, 0x19e60\n"
    "v_lshlrev_b32_e32 v150, s43, v150\n"
    "s_mov_b32 s44, 0x1c400\n"
    "v_add_u32_e32 v157, 0x8200, v221\n"
    "v_add3_u32 v153, v153, v156, s49\n"
    "v_sub_u32_e32 v163, v163, v164\n"
    "v_and_b32_e32 v164, 16, v239\n"
    "v_sub_u32_e32 v229, v162, v150\n"
    "s_cselect_b32 s44, s44, 0x20ba0\n"
    "s_mulk_i32 s46, 0x11e0\n"
    "s_mul_i32 s48, s80, 0x1080\n"
    "v_mov_b32_e32 v150, 0x1000\n"
    "v_xor_b32_e32 v160, v153, v157\n"
    "v_cmp_ne_u32_e32 vcc, 0, v164\n"
    "s_add_i32 s44, s44, s46\n"
    "s_add_i32 s48, s48, 0x14e40\n"
    "v_lshl_add_u32 v220, v154, 5, v150\n"
    "v_mov_b32_e32 v150, 0x1c400\n"
    "v_xor_b32_e32 v156, v158, v159\n"
    "v_cndmask_b32_e32 v164, 0, v165, vcc\n"
    "v_bitop3_b32 v225, v153, v157, s45 bitop3:0xe4\n"
    "v_bitop3_b32 v227, v160, v153, s45 bitop3:0x6c\n"
    "v_lshl_add_u32 v230, v154, 3, v150\n"
    "v_mov_b32_e32 v150, s48\n"
    "v_mov_b32_e32 v153, s44\n"
    "v_cmp_gt_u32_e32 vcc, 56, v239\n"
    "v_bitop3_b32 v226, v156, v158, s45 bitop3:0x6c\n"
    "s_mul_i32 s44, s42, 0x2100\n"
    "v_cndmask_b32_e32 v150, v150, v153, vcc\n"
    "v_lshlrev_b32_e32 v153, 3, v239\n"
    "v_lshlrev_b32_e32 v156, 2, v239\n"
    "s_movk_i32 s46, 0x1080\n"
    "v_add_u32_e32 v231, v150, v153\n"
    "v_and_b32_e32 v150, 1, v239\n"
    "v_and_b32_e32 v156, 8, v156\n"
    "v_or_b32_e32 v157, s44, v155\n"
    "v_mad_u32_u24 v157, v150, s46, v157\n"
    "v_lshl_or_b32 v152, v152, 4, v156\n"
    "v_sub_u32_e32 v152, v157, v152\n"
    "v_add_u32_e32 v232, 0x14038, v152\n"
    "v_mul_i32_i24_e32 v152, 0xffffb860, v150\n"
    "v_and_b32_e32 v151, 0x1c0, v151\n"
    "v_lshlrev_b32_e32 v150, 2, v150\n"
    "s_mulk_i32 s42, 0x11e0\n"
    "v_or3_b32 v234, v151, v150, v155\n"
    "v_lshrrev_b32_e32 v150, 4, v154\n"
    "s_mul_i32 s40, s40, 18\n"
    "s_mov_b32 s43, 0x20ba0\n"
    "v_add_u32_e32 v222, s47, v161\n"
    "s_xor_b32 s47, s47, 0x100\n"
    "v_add3_u32 v152, v155, s42, v152\n"
    "v_mul_u32_u24_e32 v150, 0x12000, v150\n"
    "s_movk_i32 s42, 0x78\n"
    "s_add_i32 s40, s40, s41\n"
    "v_add_u32_e32 v228, v163, v164\n"
    "v_add_u32_e32 v223, s47, v161\n"
    "v_bitop3_b32 v224, v158, v159, s45 bitop3:0xe4\n"
    "v_add3_u32 v233, v152, v151, s43\n"
    "v_and_or_b32 v235, v153, s42, v150\n"
    "s_lshl_b32 s59, s40, 9\n"
    "s_waitcnt lgkmcnt(0)\n"
    "s_lshr_b32 s56, s80, 1\n"
    "s_lshl_b32 s56, s56, 4\n"
    "s_lshr_b64 s[60:61], s[60:61], s56\n"
    "s_lshl_b32 s60, s60, 16\n"
    "v_mov_b32 v4, 0x3fb8aa3b\n"
    "v_mul_f32 v4, s60, v4\n"
    "v_exp_f32 v4, v4\n"
    "s_lshl_b32 s40, s62, 16\n"
    "s_and_b32 s41, s62, 0xffff0000\n"
    "s_lshl_b32 s42, s63, 16\n"
    "s_and_b32 s43, s63, 0xffff0000\n"
    "s_lshl_b32 s66, s80, 8\n"
    "s_add_u32 s67, s66, 63740\n"
    "v_cvt_pk_bf16_f32 v4, v4, v4\n"
    "v_lshlrev_b32 v4, 16, v4\n"
    "v_mov_b32 v93, 0x3c00\n"
    "s_mov_b32 m0, s66\n"
    "s_nop 0\n"
    "ds_write_addtid_b32 v93 offset:33280\n"
    "v_mov_b32 v8, 163776\n"
    "v_mov_b64 v[10:11], 0\n"
    "v_mov_b64 v[12:13], 0\n"
    "ds_write_b128 v8, v[10:13]\n"
    "s_waitcnt vmcnt(3)\n"
    "s_cmp_ge_u32 s80, 4\n"
    "s_cbranch_scc1 Lpw21_%=\n"
    "s_waitcnt vmcnt(1)\n"
    "Lpw21_%=:\n"
    "v_lshlrev_b32 v0, 16, v240\n"
    "v_mul_f32 v0, 0x3fb8aa3b, v0\n"
    "v_exp_f32 v0, v0\n"
    "s_cmp_lt_u32 s80, 4\n"
    "s_cbranch_scc1 Lon22_%=\n"
    "global_load_dwordx2 v[248:249], v255, s[64:65] offset:2304\n"
    "Lon22_%=:\n"
    "v_lshlrev_b32 v1, 16, v241\n"
    "v_lshlrev_b32 v2, 16, v242\n"
    "v_lshlrev_b32 v3, 16, v243\n"
    "v_cvt_pk_bf16_f32 v0, v0, v0\n"
    "v_pk_mul_f32 v[2:3], v[2:3], v[4:5] op_sel_hi:[1,0]\n"
    "v_lshlrev_b32 v0, 16, v0\n"
    "v_max_f32 v7, |v2|, |v3|\n"
    "v_pk_mul_f32 v[0:1], v[0:1], v[4:5] op_sel_hi:[1,0]\n"
    "v_mul_f32 v5, 0xbfb8aa3b, v0\n"
    "v_max_f32_dpp v7, v7, v7 quad_perm:[1,0,3,2] row_mask:0xf bank_mask:0xf\n"
    "v_mul_f32 v6, 0.15915494, v1\n"
    "v_exp_f32 v82, v5\n"
    "v_max_f32_dpp v7, v7, v7 quad_perm:[2,3,0,1] row_mask:0xf bank_mask:0xf\n"
    "v_fract_f32 v83, v6\n"
    "v_cos_f32 v34, v83\n"
    "v_sin_f32 v35, v83\n"
    "v_max_f32_dpp v7, v7, v7 row_half_mirror row_mask:0xf bank_mask:0xf\n"
    "v_pk_mul_f32 v[34:35], v[34:35], v[82:83] op_sel_hi:[1,0]\n"
    "v_mul_f32 v84, 0x41800000, v5\n"
    "v_mul_f32 v85, 0x41800000, v6\n"
    "v_exp_f32 v84, v84\n"
    "v_fract_f32 v85, v85\n"
    "v_cos_f32 v64, v85\n"
    "v_sin_f32 v65, v85\n"
    "v_max_f32_dpp v7, v7, v7 row_mirror row_mask:0xf bank_mask:0xf\n"
    "v_pk_mul_f32 v[64:65], v[64:65], v[84:85] op_sel_hi:[1,0]\n"
    "v_mul_f32 v86, 0x43800000, v5\n"
    "v_mul_f32 v87, 0x43800000, v6\n"
    "v_exp_f32 v86, v86\n"
    "v_fract_f32 v87, v87\n"
    "v_cos_f32 v66, v87\n"
    "v_sin_f32 v67, v87\n"
    "v_max_f32_dpp v7, v7, v7 row_bcast:15 row_mask:0xa bank_mask:0xf\n"
    "v_pk_mul_f32 v[66:67], v[66:67], v[86:87] op_sel_hi:[1,0]\n"
    "v_mul_f32 v88, 0x44800000, v5\n"
    "v_mul_f32 v89, 0x44800000, v6\n"
    "v_exp_f32 v88, v88\n"
    "v_fract_f32 v89, v89\n"
    "v_cos_f32 v72, v89\n"
    "v_sin_f32 v73, v89\n"
    "v_max_f32_dpp v7, v7, v7 row_bcast:31 row_mask:0xc bank_mask:0xf\n"
    "v_pk_mul_f32 v[72:73], v[72:73], v[88:89] op_sel_hi:[1,0]\n"
    "v_readlane_b32 s56, v7, 63\n"
    "v_pk_mul_f32 v[82:83], v[34:35], v[34:35] op_sel:[0,0] op_sel_hi:[1,0]\n"
    "v_pk_mul_f32 v[84:85], v[64:65], v[64:65] op_sel:[0,0] op_sel_hi:[1,0]\n"
    "v_pk_fma_f32 v[36:37], v[34:35], v[34:35], v[82:83] op_sel:[1,1,0] op_sel_hi:[0,1,1] neg_lo:[1,0,0]\n"
    "v_pk_fma_f32 v[80:81], v[64:65], v[64:65], v[84:85] op_sel:[1,1,0] op_sel_hi:[0,1,1] neg_lo:[1,0,0]\n"
    "s_lshr_b32 s56, s56, 23\n"
    "s_max_u32 s56, s56, 2\n"
    "s_min_u32 s56, s56, 250\n"
    "s_sub_u32 s57, 254, s56\n"
    "s_lshl_b32 s57, s57, 23\n"
    "v_mul_f32 v76, s57, v2\n"
    "v_mul_f32 v77, s57, -v3\n"
    "s_lshl_b32 s57, s80, 2\n"
    "s_add_u32 s57, s57, 163808\n"
    "v_mov_b32 v0, s56\n"
    "v_mov_b32 v1, s57\n"
    "ds_write_b32 v1, v0\n"
    "s_cmp_lt_u32 s80, 4\n"
    "s_cbranch_scc1 Lon23_%=\n"
    "global_load_dwordx2 v[250:251], v255, s[64:65] offset:3456\n"
    "Lon23_%=:\n"
    "v_cvt_pk_f16_f32 v96, v34, v35\n"
    "ds_write_addtid_b32 v96 offset:35360\n"
    "v_cvt_pk_f16_f32 v97, v64, v65\n"
    "v_pk_mul_f32 v[86:87], v[34:35], v[36:37] op_sel:[0,0] op_sel_hi:[1,0]\n"
    "v_pk_mul_f32 v[88:89], v[36:37], v[36:37] op_sel:[0,0] op_sel_hi:[1,0]\n"
    "v_pk_fma_f32 v[38:39], v[34:35], v[36:37], v[86:87] op_sel:[1,1,0] op_sel_hi:[0,1,1] neg_lo:[1,0,0]\n"
    "v_pk_fma_f32 v[40:41], v[36:37], v[36:37], v[88:89] op_sel:[1,1,0] op_sel_hi:[0,1,1] neg_lo:[1,0,0]\n"
    "v_cvt_pk_f16_f32 v98, v36, v37\n"
    "ds_write_addtid_b32 v98 offset:37440\n"
    "v_cvt_pk_f16_f32 v99, v80, v81\n"
    "v_cvt_pk_f16_f32 v100, v76, v77\n"
    "ds_write_addtid_b32 v100 offset:0\n"
    "v_pk_mul_f32 v[82:83], v[80:81], v[64:65] op_sel:[0,0] op_sel_hi:[1,0]\n"
    "v_pk_mul_f32 v[84:85], v[80:81], v[80:81] op_sel:[0,0] op_sel_hi:[1,0]\n"
    "v_pk_fma_f32 v[42:43], v[80:81], v[64:65], v[82:83] op_sel:[1,1,0] op_sel_hi:[0,1,1] neg_lo:[1,0,0]\n"
    "v_pk_fma_f32 v[44:45], v[80:81], v[80:81], v[84:85] op_sel:[1,1,0] op_sel_hi:[0,1,1] neg_lo:[1,0,0]\n"
    "v_cvt_pk_f16_f32 v101, v38, v39\n"
    "ds_write_addtid_b32 v101 offset:39520\n"
    "v_cvt_pk_f16_f32 v102, v40, v41\n"
    "ds_write_addtid_b32 v102 offset:41600\n"
    "v_pk_mul_f32 v[86:87], v[40:41], v[40:41] op_sel:[0,0] op_sel_hi:[1,0]\n"
    "v_pk_mul_f32 v[88:89], v[76:77], v[44:45] op_sel:[0,0] op_sel_hi:[1,0]\n"
    "v_pk_fma_f32 v[48:49], v[40:41], v[40:41], v[86:87] op_sel:[1,1,0] op_sel_hi:[0,1,1] neg_lo:[1,0,0]\n"
    "v_pk_fma_f32 v[76:77], v[76:77], v[44:45], v[88:89] op_sel:[1,1,0] op_sel_hi:[0,1,1] neg_hi:[1,0,0]\n"
    "v_cvt_pk_f16_f32 v103, v42, v43\n"
    "v_pk_mul_f16 v90, v102, v96 op_sel:[0,0] op_sel_hi:[1,0]\n"
    "v_pk_mul_f16 v104, v102, v98 op_sel:[0,0] op_sel_hi:[1,0]\n"
    "v_pk_mul_f16 v91, v102, v101 op_sel:[0,0] op_sel_hi:[1,0]\n"
    "v_pk_fma_f16 v90, v102, v96, v90 op_sel:[1,1,0] op_sel_hi:[0,1,1] neg_lo:[1,0,0]\n"
    "v_pk_fma_f16 v104, v102, v98, v104 op_sel:[1,1,0] op_sel_hi:[0,1,1] neg_lo:[1,0,0]\n"
    "v_pk_fma_f16 v91, v102, v101, v91 op_sel:[1,1,0] op_sel_hi:[0,1,1] neg_lo:[1,0,0]\n"
    "ds_write_addtid_b32 v90 offset:43680\n"
    "ds_write_addtid_b32 v104 offset:45760\n"
    "ds_write_addtid_b32 v91 offset:47840\n"
    "v_cvt_pk_f16_f32 v105, v48, v49\n"
    "ds_write_addtid_b32 v105 offset:49920\n"
    "v_cvt_pk_f16_f32 v106, v76, v77\n"
    "ds_write_addtid_b32 v106 offset:8320\n"
    "v_pk_mul_f32 v[82:83], v[48:49], v[40:41] op_sel:[0,0] op_sel_hi:[1,0]\n"
    "v_pk_mul_f32 v[84:85], v[76:77], v[44:45] op_sel:[0,0] op_sel_hi:[1,0]\n"
    "v_pk_fma_f32 v[56:57], v[48:49], v[40:41], v[82:83] op_sel:[1,1,0] op_sel_hi:[0,1,1] neg_lo:[1,0,0]\n"
    "v_pk_fma_f32 v[76:77], v[76:77], v[44:45], v[84:85] op_sel:[1,1,0] op_sel_hi:[0,1,1] neg_hi:[1,0,0]\n"
    "v_pk_mul_f16 v92, v100, v97 op_sel:[0,0] op_sel_hi:[1,0]\n"
    "v_pk_mul_f16 v93, v100, v99 op_sel:[0,0] op_sel_hi:[1,0]\n"
    "v_pk_mul_f16 v90, v100, v103 op_sel:[0,0] op_sel_hi:[1,0]\n"
    "v_pk_fma_f16 v92, v100, v97, v92 op_sel:[1,1,0] op_sel_hi:[0,1,1] neg_hi:[1,0,0]\n"
    "v_pk_fma_f16 v93, v100, v99, v93 op_sel:[1,1,0] op_sel_hi:[0,1,1] neg_hi:[1,0,0]\n"
    "v_pk_fma_f16 v90, v100, v103, v90 op_sel:[1,1,0] op_sel_hi:[0,1,1] neg_hi:[1,0,0]\n"
    "ds_write_addtid_b32 v92 offset:2080\n"
    "ds_write_addtid_b32 v93 offset:4160\n"
    "ds_write_addtid_b32 v90 offset:6240\n"
    "v_cvt_pk_f16_f32 v107, v56, v57\n"
    "ds_write_addtid_b32 v107 offset:58240\n"
    "v_cvt_pk_f16_f32 v108, v76, v77\n"
    "ds_write_addtid_b32 v108 offset:16640\n"
    "v_pk_mul_f32 v[86:87], v[66:67], v[66:67] op_sel:[0,0] op_sel_hi:[1,0]\n"
    "v_pk_mul_f32 v[88:89], v[76:77], v[44:45] op_sel:[0,0] op_sel_hi:[1,0]\n"
    "v_pk_fma_f32 v[68:69], v[66:67], v[66:67], v[86:87] op_sel:[1,1,0] op_sel_hi:[0,1,1] neg_lo:[1,0,0]\n"
    "v_pk_fma_f32 v[76:77], v[76:77], v[44:45], v[88:89] op_sel:[1,1,0] op_sel_hi:[0,1,1] neg_hi:[1,0,0]\n"
    "v_pk_mul_f16 v91, v105, v96 op_sel:[0,0] op_sel_hi:[1,0]\n"
    "v_pk_mul_f16 v109, v105, v98 op_sel:[0,0] op_sel_hi:[1,0]\n"
    "v_pk_mul_f16 v92, v105, v101 op_sel:[0,0] op_sel_hi:[1,0]\n"
    "v_pk_fma_f16 v91, v105, v96, v91 op_sel:[1,1,0] op_sel_hi:[0,1,1] neg_lo:[1,0,0]\n"
    "v_pk_fma_f16 v109, v105, v98, v109 op_sel:[1,1,0] op_sel_hi:[0,1,1] neg_lo:[1,0,0]\n"
    "v_pk_fma_f16 v92, v105, v101, v92 op_sel:[1,1,0] op_sel_hi:[0,1,1] neg_lo:[1,0,0]\n"
    "ds_write_addtid_b32 v91 offset:52000\n"
    "ds_write_addtid_b32 v109 offset:54080\n"
    "ds_write_addtid_b32 v92 offset:56160\n"
    "v_cvt_pk_f16_f32 v110, v76, v77\n"
    "ds_write_addtid_b32 v110 offset:24960\n"
    "v_pk_mul_f32 v[82:83], v[68:69], v[66:67] op_sel:[0,0] op_sel_hi:[1,0]\n"
    "v_pk_mul_f32 v[84:85], v[72:73], v[66:67] op_sel:[0,0] op_sel_hi:[1,0]\n"
    "v_pk_fma_f32 v[70:71], v[68:69], v[66:67], v[82:83] op_sel:[1,1,0] op_sel_hi:[0,1,1] neg_lo:[1,0,0]\n"
    "v_pk_fma_f32 v[50:51], v[72:73], v[66:67], v[84:85] op_sel:[1,1,0] op_sel_hi:[0,1,1] neg_lo:[1,0,0]\n"
    "v_pk_mul_f16 v93, v106, v97 op_sel:[0,0] op_sel_hi:[1,0]\n"
    "v_pk_mul_f16 v90, v106, v99 op_sel:[0,0] op_sel_hi:[1,0]\n"
    "v_pk_mul_f16 v91, v106, v103 op_sel:[0,0] op_sel_hi:[1,0]\n"
    "v_pk_fma_f16 v93, v106, v97, v93 op_sel:[1,1,0] op_sel_hi:[0,1,1] neg_hi:[1,0,0]\n"
    "v_pk_fma_f16 v90, v106, v99, v90 op_sel:[1,1,0] op_sel_hi:[0,1,1] neg_hi:[1,0,0]\n"
    "v_pk_fma_f16 v91, v106, v103, v91 op_sel:[1,1,0] op_sel_hi:[0,1,1] neg_hi:[1,0,0]\n"
    "ds_write_addtid_b32 v93 offset:10400\n"
    "ds_write_addtid_b32 v90 offset:12480\n"
    "ds_write_addtid_b32 v91 offset:14560\n"
    "v_pk_mul_f16 v92, v108, v97 op_sel:[0,0] op_sel_hi:[1,0]\n"
    "v_pk_mul_f16 v93, v108, v99 op_sel:[0,0] op_sel_hi:[1,0]\n"
    "v_pk_mul_f16 v90, v108, v103 op_sel:[0,0] op_sel_hi:[1,0]\n"
    "v_pk_fma_f16 v92, v108, v97, v92 op_sel:[1,1,0] op_sel_hi:[0,1,1] neg_hi:[1,0,0]\n"
    "v_pk_fma_f16 v93, v108, v99, v93 op_sel:[1,1,0] op_sel_hi:[0,1,1] neg_hi:[1,0,0]\n"
    "v_pk_fma_f16 v90, v108, v103, v90 op_sel:[1,1,0] op_sel_hi:[0,1,1] neg_hi:[1,0,0]\n"
    "ds_write_addtid_b32 v92 offset:18720\n"
    "ds_write_addtid_b32 v93 offset:20800\n"
    "ds_write_addtid_b32 v90 offset:22880\n"
    "v_pk_mul_f16 v91, v110, v97 op_sel:[0,0] op_sel_hi:[1,0]\n"
    "v_pk_mul_f16 v92, v110, v99 op_sel:[0,0] op_sel_hi:[1,0]\n"
    "v_pk_mul_f16 v93, v110, v103 op_sel:[0,0] op_sel_hi:[1,0]\n"
    "v_pk_fma_f16 v91, v110, v97, v91 op_sel:[1,1,0] op_sel_hi:[0,1,1] neg_hi:[1,0,0]\n"
    "v_pk_fma_f16 v92, v110, v99, v92 op_sel:[1,1,0] op_sel_hi:[0,1,1] neg_hi:[1,0,0]\n"
    "v_pk_fma_f16 v93, v110, v103, v93 op_sel:[1,1,0] op_sel_hi:[0,1,1] neg_hi:[1,0,0]\n"
    "ds_write_addtid_b32 v91 offset:27040\n"
    "ds_write_addtid_b32 v92 offset:29120\n"
    "ds_write_addtid_b32 v93 offset:31200\n"
    "v_pk_mul_f32 v[86:87], v[72:73], v[68:69] op_sel:[0,0] op_sel_hi:[1,0]\n"
    "v_pk_mul_f32 v[88:89], v[72:73], v[70:71] op_sel:[0,0] op_sel_hi:[1,0]\n"
    "v_pk_fma_f32 v[52:53], v[72:73], v[68:69], v[86:87] op_sel:[1,1,0] op_sel_hi:[0,1,1] neg_lo:[1,0,0]\n"
    "v_pk_fma_f32 v[54:55], v[72:73], v[70:71], v[88:89] op_sel:[1,1,0] op_sel_hi:[0,1,1] neg_lo:[1,0,0]\n"
    "v_pk_mul_f16 v90, v107, v96 op_sel:[0,0] op_sel_hi:[1,0]\n"
    "v_pk_mul_f16 v111, v107, v98 op_sel:[0,0] op_sel_hi:[1,0]\n"
    "v_pk_mul_f16 v91, v107, v101 op_sel:[0,0] op_sel_hi:[1,0]\n"
    "v_pk_fma_f16 v90, v107, v96, v90 op_sel:[1,1,0] op_sel_hi:[0,1,1] neg_lo:[1,0,0]\n"
    "v_pk_fma_f16 v111, v107, v98, v111 op_sel:[1,1,0] op_sel_hi:[0,1,1] neg_lo:[1,0,0]\n"
    "v_pk_fma_f16 v91, v107, v101, v91 op_sel:[1,1,0] op_sel_hi:[0,1,1] neg_lo:[1,0,0]\n"
    "ds_write_addtid_b32 v90 offset:60320\n"
    "ds_write_addtid_b32 v111 offset:62400\n"
    "s_mov_b32 m0, s67\n"
    "s_nop 0\n"
    "ds_write_addtid_b32 v91 offset:740\n"
    "v_cvt_pk_f16_f32 v112, v66, v67\n"
    "ds_write_addtid_b32 v112 offset:2820\n"
    "v_cvt_pk_f16_f32 v113, v68, v69\n"
    "ds_write_addtid_b32 v113 offset:21540\n"
    "v_cvt_pk_f16_f32 v114, v70, v71\n"
    "ds_write_addtid_b32 v114 offset:31940\n"
    "v_cvt_pk_f16_f32 v115, v72, v73\n"
    "ds_write_addtid_b32 v115 offset:42340\n"
    "v_cvt_pk_f16_f32 v116, v50, v51\n"
    "ds_write_addtid_b32 v116 offset:48580\n"
    "v_cvt_pk_f16_f32 v117, v52, v53\n"
    "ds_write_addtid_b32 v117 offset:54820\n"
    "v_cvt_pk_f16_f32 v118, v54, v55\n"
    "ds_write_addtid_b32 v118 offset:61060\n"
    "s_cmp_lt_u32 s80, 4\n"
    "s_cbranch_scc1 Lon24_%=\n"
    "buffer_load_dwordx4 v[192:195], v220, s[36:39], 0 offen sc1\n"
    "buffer_load_dwordx4 v[196:199], v220, s[36:39], 0 offen offset:16 sc1\n"
    "Lon24_%=:\n"
    "v_pk_mul_f16 v92, v112, v98 op_sel:[0,0] op_sel_hi:[1,0]\n"
    "v_pk_mul_f16 v93, v112, v102 op_sel:[0,0] op_sel_hi:[1,0]\n"
    "v_pk_fma_f16 v92, v112, v98, v92 op_sel:[1,1,0] op_sel_hi:[0,1,1] neg_lo:[1,0,0]\n"
    "v_pk_fma_f16 v93, v112, v102, v93 op_sel:[1,1,0] op_sel_hi:[0,1,1] neg_lo:[1,0,0]\n"
    "ds_write_addtid_b32 v92 offset:4900\n"
    "ds_write_addtid_b32 v93 offset:6980\n"
    "v_pk_mul_f16 v90, v112, v104 op_sel:[0,0] op_sel_hi:[1,0]\n"
    "v_pk_mul_f16 v91, v112, v105 op_sel:[0,0] op_sel_hi:[1,0]\n"
    "v_pk_fma_f16 v90, v112, v104, v90 op_sel:[1,1,0] op_sel_hi:[0,1,1] neg_lo:[1,0,0]\n"
    "v_pk_fma_f16 v91, v112, v105, v91 op_sel:[1,1,0] op_sel_hi:[0,1,1] neg_lo:[1,0,0]\n"
    "ds_write_addtid_b32 v90 offset:9060\n"
    "ds_write_addtid_b32 v91 offset:11140\n"
    "v_pk_mul_f16 v92, v112, v109 op_sel:[0,0] op_sel_hi:[1,0]\n"
    "v_pk_mul_f16 v93, v112, v107 op_sel:[0,0] op_sel_hi:[1,0]\n"
    "v_pk_fma_f16 v92, v112, v109, v92 op_sel:[1,1,0] op_sel_hi:[0,1,1] neg_lo:[1,0,0]\n"
    "v_pk_fma_f16 v93, v112, v107, v93 op_sel:[1,1,0] op_sel_hi:[0,1,1] neg_lo:[1,0,0]\n"
    "ds_write_addtid_b32 v92 offset:13220\n"
    "ds_write_addtid_b32 v93 offset:15300\n"
    "v_pk_mul_f16 v90, v112, v111 op_sel:[0,0] op_sel_hi:[1,0]\n"
    "v_pk_mul_f16 v91, v112, v97 op_sel:[0,0] op_sel_hi:[1,0]\n"
    "v_pk_fma_f16 v90, v112, v111, v90 op_sel:[1,1,0] op_sel_hi:[0,1,1] neg_lo:[1,0,0]\n"
    "v_pk_fma_f16 v91, v112, v97, v91 op_sel:[1,1,0] op_sel_hi:[0,1,1] neg_lo:[1,0,0]\n"
    "ds_write_addtid_b32 v90 offset:17380\n"
    "ds_write_addtid_b32 v91 offset:19460\n"
    "v_pk_mul_f16 v92, v113, v102 op_sel:[0,0] op_sel_hi:[1,0]\n"
    "v_pk_mul_f16 v93, v113, v105 op_sel:[0,0] op_sel_hi:[1,0]\n"
    "v_pk_fma_f16 v92, v113, v102, v92 op_sel:[1,1,0] op_sel_hi:[0,1,1] neg_lo:[1,0,0]\n"
    "v_pk_fma_f16 v93, v113, v105, v93 op_sel:[1,1,0] op_sel_hi:[0,1,1] neg_lo:[1,0,0]\n"
    "ds_write_addtid_b32 v92 offset:23620\n"
    "ds_write_addtid_b32 v93 offset:25700\n"
    "v_pk_mul_f16 v90, v113, v107 op_sel:[0,0] op_sel_hi:[1,0]\n"
    "v_pk_mul_f16 v91, v113, v97 op_sel:[0,0] op_sel_hi:[1,0]\n"
    "v_pk_fma_f16 v90, v113, v107, v90 op_sel:[1,1,0] op_sel_hi:[0,1,1] neg_lo:[1,0,0]\n"
    "v_pk_fma_f16 v91, v113, v97, v91 op_sel:[1,1,0] op_sel_hi:[0,1,1] neg_lo:[1,0,0]\n"
    "ds_write_addtid_b32 v90 offset:27780\n"
    "ds_write_addtid_b32 v91 offset:29860\n"
    "v_pk_mul_f16 v92, v114, v102 op_sel:[0,0] op_sel_hi:[1,0]\n"
    "v_pk_mul_f16 v93, v114, v105 op_sel:[0,0] op_sel_hi:[1,0]\n"
    "v_pk_fma_f16 v92, v114, v102, v92 op_sel:[1,1,0] op_sel_hi:[0,1,1] neg_lo:[1,0,0]\n"
    "v_pk_fma_f16 v93, v114, v105, v93 op_sel:[1,1,0] op_sel_hi:[0,1,1] neg_lo:[1,0,0]\n"
    "ds_write_addtid_b32 v92 offset:34020\n"
    "ds_write_addtid_b32 v93 offset:36100\n"
    "v_pk_mul_f16 v90, v114, v107 op_sel:[0,0] op_sel_hi:[1,0]\n"
    "v_pk_mul_f16 v91, v114, v97 op_sel:[0,0] op_sel_hi:[1,0]\n"
    "v_pk_fma_f16 v90, v114, v107, v90 op_sel:[1,1,0] op_sel_hi:[0,1,1] neg_lo:[1,0,0]\n"
    "v_pk_fma_f16 v91, v114, v97, v91 op_sel:[1,1,0] op_sel_hi:[0,1,1] neg_lo:[1,0,0]\n"
    "ds_write_addtid_b32 v90 offset:38180\n"
    "ds_write_addtid_b32 v91 offset:40260\n"
    "v_pk_mul_f16 v92, v115, v105 op_sel:[0,0] op_sel_hi:[1,0]\n"
    "v_pk_mul_f16 v93, v115, v97 op_sel:[0,0] op_sel_hi:[1,0]\n"
    "v_pk_fma_f16 v92, v115, v105, v92 op_sel:[1,1,0] op_sel_hi:[0,1,1] neg_lo:[1,0,0]\n"
    "v_pk_fma_f16 v93, v115, v97, v93 op_sel:[1,1,0] op_sel_hi:[0,1,1] neg_lo:[1,0,0]\n"
    "ds_write_addtid_b32 v92 offset:44420\n"
    "ds_write_addtid_b32 v93 offset:46500\n"
    "v_pk_mul_f16 v90, v116, v105 op_sel:[0,0] op_sel_hi:[1,0]\n"
    "v_pk_mul_f16 v91, v116, v97 op_sel:[0,0] op_sel_hi:[1,0]\n"
    "v_pk_fma_f16 v90, v116, v105, v90 op_sel:[1,1,0] op_sel_hi:[0,1,1] neg_lo:[1,0,0]\n"
    "v_pk_fma_f16 v91, v116, v97, v91 op_sel:[1,1,0] op_sel_hi:[0,1,1] neg_lo:[1,0,0]\n"
    "ds_write_addtid_b32 v90 offset:50660\n"
    "ds_write_addtid_b32 v91 offset:52740\n"
    "v_pk_mul_f16 v92, v117, v105 op_sel:[0,0] op_sel_hi:[1,0]\n"
    "v_pk_mul_f16 v93, v117, v97 op_sel:[0,0] op_sel_hi:[1,0]\n"
    "v_pk_fma_f16 v92, v117, v105, v92 op_sel:[1,1,0] op_sel_hi:[0,1,1] neg_lo:[1,0,0]\n"
    "v_pk_fma_f16 v93, v117, v97, v93 op_sel:[1,1,0] op_sel_hi:[0,1,1] neg_lo:[1,0,0]\n"
    "ds_write_addtid_b32 v92 offset:56900\n"
    "ds_write_addtid_b32 v93 offset:58980\n"
    "v_pk_mul_f16 v90, v118, v105 op_sel:[0,0] op_sel_hi:[1,0]\n"
    "v_pk_mul_f16 v91, v118, v97 op_sel:[0,0] op_sel_hi:[1,0]\n"
    "v_pk_fma_f16 v90, v118, v105, v90 op_sel:[1,1,0] op_sel_hi:[0,1,1] neg_lo:[1,0,0]\n"
    "v_pk_fma_f16 v91, v118, v97, v91 op_sel:[1,1,0] op_sel_hi:[0,1,1] neg_lo:[1,0,0]\n"
    "ds_write_addtid_b32 v90 offset:63140\n"
    "ds_write_addtid_b32 v91 offset:65220\n"
    "ds_read_b128 v[32:35], v221 offset:0\n"
    "ds_read_b128 v[56:59], v226 offset:0\n"
    "ds_read_b128 v[60:63], v227 offset:0\n"
    "ds_read_b128 v[48:51], v224 offset:0\n"
    "ds_read_b128 v[52:55], v225 offset:0\n"
    "ds_read_b128 v[36:39], v221 offset:64\n"
    "ds_read_b128 v[72:75], v226 offset:64\n"
    "ds_read_b128 v[76:79], v227 offset:64\n"
    "ds_read_b128 v[64:67], v224 offset:64\n"
    "ds_read_b128 v[68:71], v225 offset:64\n"
    "ds_read_b128 v[40:43], v221 offset:128\n"
    "ds_read_b128 v[88:91], v226 offset:128\n"
    "ds_read_b128 v[92:95], v227 offset:128\n"
    "ds_read_b128 v[80:83], v224 offset:128\n"
    "ds_read_b128 v[84:87], v225 offset:128\n"
    "ds_read_b128 v[44:47], v221 offset:192\n"
    "ds_read_b128 v[104:107], v226 offset:192\n"
    "ds_read_b128 v[108:111], v227 offset:192\n"
    "ds_read_b128 v[96:99], v224 offset:192\n"
    "ds_read_b128 v[100:103], v225 offset:192\n"
    "s_waitcnt lgkmcnt(15)\n"
    "v_mfma_f32_16x16x32_f16 v[120:123], v[56:59], v[32:35], 0\n"
    "v_mfma_f32_16x16x32_f16 v[124:127], v[60:63], v[32:35], 0\n"
    "v_mfma_f32_16x16x32_f16 v[112:115], v[48:51], v[32:35], 0\n"
    "v_mfma_f32_16x16x32_f16 v[116:119], v[52:55], v[32:35], 0\n"
    "s_waitcnt lgkmcnt(13)\n"
    "v_mfma_f32_16x16x32_f16 v[120:123], v[72:75], v[36:39], v[120:123]\n"
    "s_waitcnt lgkmcnt(12)\n"
    "v_mfma_f32_16x16x32_f16 v[124:127], v[76:79], v[36:39], v[124:127]\n"
    "s_waitcnt lgkmcnt(11)\n"
    "v_mfma_f32_16x16x32_f16 v[112:115], v[64:67], v[36:39], v[112:115]\n"
    "s_waitcnt lgkmcnt(10)\n"
    "v_mfma_f32_16x16x32_f16 v[116:119], v[68:71], v[36:39], v[116:119]\n"
    "s_waitcnt lgkmcnt(8)\n"
    "v_mfma_f32_16x16x32_f16 v[120:123], v[88:91], v[40:43], v[120:123]\n"
    "s_waitcnt lgkmcnt(7)\n"
    "v_mfma_f32_16x16x32_f16 v[124:127], v[92:95], v[40:43], v[124:127]\n"
    "s_waitcnt lgkmcnt(6)\n"
    "v_mfma_f32_16x16x32_f16 v[112:115], v[80:83], v[40:43], v[112:115]\n"
    "s_waitcnt lgkmcnt(5)\n"
    "v_mfma_f32_16x16x32_f16 v[116:119], v[84:87], v[40:43], v[116:119]\n"
    "s_waitcnt lgkmcnt(3)\n"
    "v_mfma_f32_16x16x32_f16 v[120:123], v[104:107], v[44:47], v[120:123]\n"
    "s_waitcnt lgkmcnt(2)\n"
    "v_mfma_f32_16x16x32_f16 v[124:127], v[108:111], v[44:47], v[124:127]\n"
    "s_waitcnt lgkmcnt(1)\n"
    "v_mfma_f32_16x16x32_f16 v[112:115], v[96:99], v[44:47], v[112:115]\n"
    "s_waitcnt lgkmcnt(0)\n"
    "v_mfma_f32_16x16x32_f16 v[116:119], v[100:103], v[44:47], v[116:119]\n"
    "s_waitcnt vmcnt(0)\n"
    "v_readfirstlane_b32 s83, v252\n"
    "s_nop 0\n"
    "s_add_u32 s83, s83, 1\n"
    "v_and_b32 v1, 7, v239\n"
    "v_lshlrev_b32 v1, 2, v1\n"
    "v_add_u32 v1, 163808, v1\n"
    "ds_write_b128 v222, v[120:123]\n"
    "ds_write_b128 v222, v[124:127] offset:8320\n"
    "s_cmp_lt_u32 s80, 4\n"
    "s_cbranch_scc1 Lon25_%=\n"
    "s_movk_i32 s56, 0x7fff\n"
    "Lgp26_%=:\n"
    "v_xor_b32 v200, s83, v193\n"
    "v_xor_b32 v201, s83, v195\n"
    "v_xor_b32 v202, s83, v197\n"
    "v_xor_b32 v203, s83, v199\n"
    "v_or3_b32 v200, v200, v201, v202\n"
    "v_or_b32 v200, v200, v203\n"
    "v_cmp_ne_u32 vcc, 0, v200\n"
    "s_nop 1\n"
    "s_cbranch_vccz Lgq27_%=\n"
    "s_sub_u32 s56, s56, 1\n"
    "s_cbranch_scc1 Lgq27_%=\n"
    "s_sleep 1\n"
    "buffer_load_dwordx4 v[192:195], v220, s[36:39], 0 offen sc1\n"
    "buffer_load_dwordx4 v[196:199], v220, s[36:39], 0 offen offset:16 sc1\n"
    "s_waitcnt vmcnt(0)\n"
    "s_branch Lgp26_%=\n"
    "Lgq27_%=:\n"
    "v_and_b32 v160, 0xffff0000, v192\n"
    "v_lshlrev_b32 v164, 16, v192\n"
    "v_rcp_f32 v160, v160\n"
    "v_and_b32 v161, 0xffff0000, v194\n"
    "v_lshlrev_b32 v165, 16, v194\n"
    "v_rcp_f32 v161, v161\n"
    "v_and_b32 v162, 0xffff0000, v196\n"
    "v_lshlrev_b32 v166, 16, v196\n"
    "v_rcp_f32 v162, v162\n"
    "v_and_b32 v163, 0xffff0000, v198\n"
    "v_lshlrev_b32 v167, 16, v198\n"
    "v_rcp_f32 v163, v163\n"
    "v_lshlrev_b32 v128, 16, v244\n"
    "v_and_b32 v132, 0xffff0000, v244\n"
    "v_lshlrev_b32 v136, 16, v245\n"
    "v_and_b32 v140, 0xffff0000, v245\n"
    "v_lshlrev_b32 v129, 16, v246\n"
    "v_and_b32 v133, 0xffff0000, v246\n"
    "v_lshlrev_b32 v137, 16, v247\n"
    "v_and_b32 v141, 0xffff0000, v247\n"
    "v_lshlrev_b32 v130, 16, v248\n"
    "v_and_b32 v134, 0xffff0000, v248\n"
    "v_lshlrev_b32 v138, 16, v249\n"
    "v_and_b32 v142, 0xffff0000, v249\n"
    "v_lshlrev_b32 v131, 16, v250\n"
    "v_and_b32 v135, 0xffff0000, v250\n"
    "v_lshlrev_b32 v139, 16, v251\n"
    "v_and_b32 v143, 0xffff0000, v251\n"
    "v_pk_mul_f32 v[164:165], v[164:165], v[160:161] neg_lo:[1,0] neg_hi:[1,0]\n"
    "v_pk_mul_f32 v[166:167], v[166:167], v[162:163] neg_lo:[1,0] neg_hi:[1,0]\n"
    "v_add_u32 v200, 18788, v230\n"
    "v_pk_fma_f32 v[144:145], v[128:129], v[160:161], v[164:165]\n"
    "v_pk_fma_f32 v[146:147], v[130:131], v[162:163], v[166:167]\n"
    "v_cvt_pk_f16_f32 v168, v144, v145\n"
    "v_cvt_pk_f16_f32 v169, v146, v147\n"
    "ds_write2_b32 v200, v168, v169 offset1:1\n"
    "v_pk_fma_f32 v[148:149], v[132:133], v[160:161], v[164:165]\n"
    "v_pk_fma_f32 v[150:151], v[134:135], v[162:163], v[166:167]\n"
    "v_cvt_pk_f16_f32 v170, v148, v149\n"
    "v_cvt_pk_f16_f32 v171, v150, v151\n"
    "v_add_u32 v200, 4576, v200\n"
    "ds_write2_b32 v200, v170, v171 offset1:1\n"
    "v_pk_fma_f32 v[152:153], v[136:137], v[160:161], v[164:165]\n"
    "v_pk_fma_f32 v[154:155], v[138:139], v[162:163], v[166:167]\n"
    "v_cvt_pk_f16_f32 v172, v152, v153\n"
    "v_cvt_pk_f16_f32 v173, v154, v155\n"
    "v_add_u32 v200, 4576, v200\n"
    "ds_write2_b32 v200, v172, v173 offset1:1\n"
    "v_pk_fma_f32 v[156:157], v[140:141], v[160:161], v[164:165]\n"
    "v_pk_fma_f32 v[158:159], v[142:143], v[162:163], v[166:167]\n"
    "v_cvt_pk_f16_f32 v174, v156, v157\n"
    "v_cvt_pk_f16_f32 v175, v158, v159\n"
    "v_add_u32 v200, 4576, v200\n"
    "ds_write2_b32 v200, v174, v175 offset1:1\n"
    "v_pk_fma_f32 v[128:129], v[144:145], s[40:41], v[128:129] op_sel:[0,0,0] op_sel_hi:[1,0,1]\n"
    "v_pk_fma_f32 v[130:131], v[146:147], s[40:41], v[130:131] op_sel:[0,0,0] op_sel_hi:[1,0,1]\n"
    "v_pk_fma_f32 v[132:133], v[148:149], s[40:41], v[132:133] op_sel:[0,1,0] op_sel_hi:[1,1,1]\n"
    "v_pk_fma_f32 v[134:135], v[150:151], s[40:41], v[134:135] op_sel:[0,1,0] op_sel_hi:[1,1,1]\n"
    "v_pk_fma_f32 v[136:137], v[152:153], s[42:43], v[136:137] op_sel:[0,0,0] op_sel_hi:[1,0,1]\n"
    "v_pk_fma_f32 v[138:139], v[154:155], s[42:43], v[138:139] op_sel:[0,0,0] op_sel_hi:[1,0,1]\n"
    "v_pk_fma_f32 v[140:141], v[156:157], s[42:43], v[140:141] op_sel:[0,1,0] op_sel_hi:[1,1,1]\n"
    "v_pk_fma_f32 v[142:143], v[158:159], s[42:43], v[142:143] op_sel:[0,1,0] op_sel_hi:[1,1,1]\n"
    "Lon25_%=:\n"
    "v_and_b32 v2, 3, v239\n"
    "v_mul_u32_u24 v2, 4576, v2\n"
    "v_add_u32 v2, 134496, v2\n"
    "v_mov_b64 v[4:5], 0\n"
    "ds_write_b32 v2, v4\n"
    "s_waitcnt lgkmcnt(0)\n"
    "s_barrier\n"
    "ds_read_b32 v0, v1\n"
    "ds_read_b128 v[120:123], v223\n"
    "ds_read_b128 v[124:127], v223 offset:8320\n"
    "ds_write_b64 v231, v[4:5]\n"
    "s_cmp_ge_u32 s80, 4\n"
    "s_cbranch_scc1 Lon28_%=\n"
    "global_load_dwordx2 v[244:245], v255, s[64:65] offset:0\n"
    "Lon28_%=:\n"
    "s_waitcnt lgkmcnt(3)\n"
    "s_xor_b32 s57, s80, 1\n"
    "v_readlane_b32 s56, v0, s80\n"
    "v_readlane_b32 s57, v0, s57\n"
    "v_max_u32_dpp v0, v0, v0 quad_perm:[1,0,3,2] row_mask:0xf bank_mask:0xf\n"
    "s_max_u32 s72, s56, s57\n"
    "s_sub_u32 s72, 127, s72\n"
    "s_add_i32 s56, s56, s72\n"
    "s_max_i32 s56, s56, 0\n"
    "s_lshl_b32 s56, s56, 23\n"
    "s_add_i32 s57, s57, s72\n"
    "s_max_i32 s57, s57, 0\n"
    "s_lshl_b32 s57, s57, 23\n"
    "v_pk_mul_f32 v[112:113], v[112:113], s[56:57] op_sel_hi:[1,0]\n"
    "v_pk_mul_f32 v[114:115], v[114:115], s[56:57] op_sel_hi:[1,0]\n"
    "v_pk_mul_f32 v[116:117], v[116:117], s[56:57] op_sel_hi:[1,0]\n"
    "v_pk_mul_f32 v[118:119], v[118:119], s[56:57] op_sel_hi:[1,0]\n"
    "s_waitcnt lgkmcnt(1)\n"
    "v_pk_fma_f32 v[112:113], v[120:121], s[56:57], v[112:113] op_sel:[0,1,0] op_sel_hi:[1,1,1]\n"
    "v_pk_fma_f32 v[114:115], v[122:123], s[56:57], v[114:115] op_sel:[0,1,0] op_sel_hi:[1,1,1]\n"
    "v_pk_fma_f32 v[116:117], v[124:125], s[56:57], v[116:117] op_sel:[0,1,0] op_sel_hi:[1,1,1]\n"
    "v_pk_fma_f32 v[118:119], v[126:127], s[56:57], v[118:119] op_sel:[0,1,0] op_sel_hi:[1,1,1]\n"
    "s_bitcmp1_b32 90, s80\n"
    "s_cbranch_scc1 Lodd29_%=\n"
    "v_cvt_pk_f16_f32 v32, v114, v113\n"
    "v_cvt_pk_f16_f32 v33, v112, v112\n"
    "v_cvt_pk_f16_f32 v34, v113, v112\n"
    "v_cvt_pk_f16_f32 v35, v112, v112\n"
    "ds_write_b64 v228, v[32:33] offset:4224\n"
    "ds_write_b64 v228, v[34:35]\n"
    "v_cvt_pk_f16_f32 v36, v119, v118\n"
    "v_cvt_pk_f16_f32 v37, v117, v116\n"
    "v_cvt_pk_f16_f32 v38, v118, v117\n"
    "ds_write_b64 v229, v[36:37] offset:4226\n"
    "ds_write_b32 v229, v38 offset:2\n"
    "ds_write_b16_d16_hi v229, v37 offset:6\n"
    "ds_write_b16 v229, v36\n"
    "s_mov_b64 exec, 1\n"
    "ds_write_b16 v229, v4 offset:8\n"
    "s_mov_b64 exec, -1\n"
    "s_branch Lexd30_%=\n"
    "Lodd29_%=:\n"
    "v_cvt_pk_f16_f32 v32, v114, v114\n"
    "v_cvt_pk_f16_f32 v33, v113, v113\n"
    "v_cvt_pk_f16_f32 v34, v113, v112\n"
    "v_cvt_pk_f16_f32 v35, v112, v112\n"
    "ds_write_b128 v228, v[32:35] offset:4224\n"
    "v_cvt_pk_f16_f32 v36, v114, v113\n"
    "v_cvt_pk_f16_f32 v37, v113, v113\n"
    "v_cvt_pk_f16_f32 v38, v112, v112\n"
    "v_cvt_pk_f16_f32 v39, v112, v112\n"
    "ds_write_b128 v228, v[36:39] offset:0\n"
    "v_cvt_pk_f16_f32 v40, v118, v118\n"
    "v_cvt_pk_f16_f32 v41, v118, v118\n"
    "v_cvt_pk_f16_f32 v42, v117, v117\n"
    "v_cvt_pk_f16_f32 v43, v117, v117\n"
    "ds_write_b128 v229, v[40:43] offset:4224\n"
    "v_cvt_pk_f16_f32 v44, v117, v117\n"
    "v_cvt_pk_f16_f32 v45, v117, v116\n"
    "v_cvt_pk_f16_f32 v46, v116, v116\n"
    "v_cvt_pk_f16_f32 v47, v116, v116\n"
    "ds_write_b128 v229, v[44:47] offset:4240\n"
    "v_cvt_pk_f16_f32 v48, v118, v118\n"
    "v_cvt_pk_f16_f32 v49, v118, v117\n"
    "v_cvt_pk_f16_f32 v50, v117, v117\n"
    "v_cvt_pk_f16_f32 v51, v117, v117\n"
    "ds_write_b128 v229, v[48:51] offset:0\n"
    "v_cvt_pk_f16_f32 v52, v117, v117\n"
    "v_cvt_pk_f16_f32 v53, v116, v116\n"
    "v_cvt_pk_f16_f32 v54, v116, v116\n"
    "v_cvt_pk_f16_f32 v55, v116, v116\n"
    "ds_write_b128 v229, v[52:55] offset:16\n"
    "Lexd30_%=:\n"
    "s_cmp_lt_u32 s80, 4\n"
    "s_cbranch_scc1 Lon31_%=\n"
    "ds_write_b64 v230, v[168:169] offset:448\n"
    "ds_write_b64 v230, v[170:171] offset:5024\n"
    "ds_write_b64 v230, v[172:173] offset:9600\n"
    "ds_write_b64 v230, v[174:175] offset:14176\n"
    "Lon31_%=:\n"
    "s_cmp_ge_u32 s80, 4\n"
    "s_cbranch_scc1 Lon32_%=\n"
    "global_load_dwordx2 v[246:247], v255, s[64:65] offset:1152\n"
    "Lon32_%=:\n"
    "v_add_u32 v0, 1, v0\n"
    "v_lshlrev_b32 v0, 23, v0\n"
    "s_nop 0\n"
    "v_readlane_b32 s68, v0, 0\n"
    "v_readlane_b32 s69, v0, 2\n"
    "v_readlane_b32 s70, v0, 4\n"
    "v_readlane_b32 s71, v0, 6\n"
    "s_waitcnt lgkmcnt(0)\n"
    "s_barrier\n"
    "Lpre5_%=:\n"
    "s_cmp_ge_u32 s80, 4\n"
    "s_cbranch_scc1 Llo13_%=\n"
    "s_cmp_eq_u32 s82, 0\n"
    "s_cbranch_scc1 Lhl16_%=\n"
    "ds_read_b128 v[12:15], v233 offset:1984\n"
    "ds_read_b64 v[16:17], v232 offset:4032\n"
    "ds_read_b64 v[18:19], v232 offset:4040\n"
    "ds_read_b128 v[8:11], v233 offset:1472\n"
    "ds_read_b128 v[4:7], v233 offset:960\n"
    "ds_read_b128 v[0:3], v233 offset:448\n"
    "ds_read_b64 v[20:21], v232 offset:3520\n"
    "ds_read_b64 v[22:23], v232 offset:3528\n"
    "ds_read_b64 v[24:25], v232 offset:3008\n"
    "ds_read_b64 v[26:27], v232 offset:3016\n"
    "ds_read_b64 v[28:29], v232 offset:2496\n"
    "ds_read_b64 v[30:31], v232 offset:2504\n"
    "s_lshl_b32 s56, s80, 11\n"
    "s_add_u32 s56, s56, 36864\n"
    "v_add_u32 v180, s56, v234\n"
    "v_add_u32 v181, 1024, v180\n"
    "s_mul_i32 s56, s80, 4576\n"
    "s_add_u32 s56, s56, 115712\n"
    "v_add_u32 v188, s56, v234\n"
    "v_add_u32 v189, 1024, v188\n"
    "v_mov_b32 v190, 163776\n"
    "v_mov_b32 v191, 1\n"
    "s_waitcnt lgkmcnt(9)\n"
    "v_mfma_f32_16x16x32_f16 v[156:159], v[16:19], v[12:15], 0\n"
    "ds_read_b128 v[44:47], v233 offset:1920\n"
    "ds_read_b64 v[48:49], v232 offset:3968\n"
    "s_waitcnt lgkmcnt(10)\n"
    "v_mfma_f32_16x16x32_f16 v[152:155], v[16:19], v[8:11], 0\n"
    "ds_read_b64 v[50:51], v232 offset:3976\n"
    "s_waitcnt lgkmcnt(10)\n"
    "v_mfma_f32_16x16x32_f16 v[148:151], v[16:19], v[4:7], 0\n"
    "ds_read_b128 v[40:43], v233 offset:1408\n"
    "ds_read_b128 v[36:39], v233 offset:896\n"
    "s_waitcnt lgkmcnt(11)\n"
    "v_mfma_f32_16x16x32_f16 v[144:147], v[16:19], v[0:3], 0\n"
    "ds_read_b128 v[32:35], v233 offset:384\n"
    "global_load_dwordx2 v[248:249], v255, s[64:65] offset:2304\n"
    "s_waitcnt lgkmcnt(10)\n"
    "v_mfma_f32_16x16x32_f16 v[156:159], v[20:23], v[8:11], v[156:159]\n"
    "ds_read_b64 v[52:53], v232 offset:3456\n"
    "ds_read_b64 v[54:55], v232 offset:3464\n"
    "v_mfma_f32_16x16x32_f16 v[152:155], v[20:23], v[4:7], v[152:155]\n"
    "ds_read_b64 v[56:57], v232 offset:2944\n"
    "v_mfma_f32_16x16x32_f16 v[148:151], v[20:23], v[0:3], v[148:151]\n"
    "ds_read_b64 v[58:59], v232 offset:2952\n"
    "ds_read_b64 v[60:61], v232 offset:2432\n"
    "s_waitcnt lgkmcnt(13)\n"
    "v_mfma_f32_16x16x32_f16 v[156:159], v[24:27], v[4:7], v[156:159]\n"
    "ds_read_b64 v[62:63], v232 offset:2440\n"
    "v_mfma_f32_16x16x32_f16 v[152:155], v[24:27], v[0:3], v[152:155]\n"
    "ds_read_b128 v[76:79], v233 offset:1856\n"
    "ds_read_b64 v[80:81], v232 offset:3904\n"
    "s_waitcnt lgkmcnt(14)\n"
    "v_mfma_f32_16x16x32_f16 v[156:159], v[28:31], v[0:3], v[156:159]\n"
    "ds_read_b64 v[82:83], v232 offset:3912\n"
    "s_waitcnt lgkmcnt(12)\n"
    "v_mfma_f32_16x16x32_f16 v[156:159], v[48:51], v[44:47], v[156:159]\n"
    "ds_read_b128 v[72:75], v233 offset:1344\n"
    "ds_read_b128 v[68:71], v233 offset:832\n"
    "s_waitcnt lgkmcnt(13)\n"
    "v_mfma_f32_16x16x32_f16 v[152:155], v[48:51], v[40:43], v[152:155]\n"
    "ds_read_b128 v[64:67], v233 offset:320\n"
    "s_waitcnt lgkmcnt(13)\n"
    "v_mfma_f32_16x16x32_f16 v[148:151], v[48:51], v[36:39], v[148:151]\n"
    "ds_read_b64 v[84:85], v232 offset:3392\n"
    "ds_read_b64 v[86:87], v232 offset:3400\n"
    "s_waitcnt lgkmcnt(14)\n"
    "v_mfma_f32_16x16x32_f16 v[144:147], v[48:51], v[32:35], v[144:147]\n"
    "ds_read_b64 v[88:89], v232 offset:2880\n"
    "s_waitcnt lgkmcnt(13)\n"
    "v_mfma_f32_16x16x32_f16 v[156:159], v[52:55], v[40:43], v[156:159]\n"
    "ds_read_b64 v[90:91], v232 offset:2888\n"
    "ds_read_b64 v[92:93], v232 offset:2368\n"
    "v_mfma_f32_16x16x32_f16 v[152:155], v[52:55], v[36:39], v[152:155]\n"
    "ds_read_b64 v[94:95], v232 offset:2376\n"
    "v_mfma_f32_16x16x32_f16 v[148:151], v[52:55], v[32:35], v[148:151]\n"
    "ds_read_b128 v[12:15], v233 offset:1792\n"
    "ds_read_b64 v[16:17], v232 offset:3840\n"
    "s_waitcnt lgkmcnt(15)\n"
    "v_mfma_f32_16x16x32_f16 v[156:159], v[56:59], v[36:39], v[156:159]\n"
    "ds_read_b64 v[18:19], v232 offset:3848\n"
    "v_mfma_f32_16x16x32_f16 v[152:155], v[56:59], v[32:35], v[152:155]\n"
    "ds_read_b128 v[8:11], v233 offset:1280\n"
    "ds_read_b128 v[4:7], v233 offset:768\n"
    "s_waitcnt lgkmcnt(15)\n"
    "v_mfma_f32_16x16x32_f16 v[156:159], v[60:63], v[32:35], v[156:159]\n"
    "ds_read_b128 v[0:3], v233 offset:256\n"
    "s_waitcnt lgkmcnt(15)\n"
    "v_mfma_f32_16x16x32_f16 v[156:159], v[80:83], v[76:79], v[156:159]\n"
    "ds_read_b64 v[20:21], v232 offset:3328\n"
    "ds_read_b64 v[22:23], v232 offset:3336\n"
    "s_waitcnt lgkmcnt(15)\n"
    "v_mfma_f32_16x16x32_f16 v[152:155], v[80:83], v[72:75], v[152:155]\n"
    "ds_read_b64 v[24:25], v232 offset:2816\n"
    "v_mfma_f32_16x16x32_f16 v[148:151], v[80:83], v[68:71], v[148:151]\n"
    "ds_read_b64 v[26:27], v232 offset:2824\n"
    "ds_read_b64 v[28:29], v232 offset:2304\n"
    "s_waitcnt lgkmcnt(15)\n"
    "v_mfma_f32_16x16x32_f16 v[144:147], v[80:83], v[64:67], v[144:147]\n"
    "ds_read_b64 v[30:31], v232 offset:2312\n"
    "global_load_dwordx2 v[250:251], v255, s[64:65] offset:3456\n"
    "v_mfma_f32_16x16x32_f16 v[156:159], v[84:87], v[72:75], v[156:159]\n"
    "ds_read_b128 v[44:47], v233 offset:1728\n"
    "ds_read_b64 v[48:49], v232 offset:3776\n"
    "v_mfma_f32_16x16x32_f16 v[152:155], v[84:87], v[68:71], v[152:155]\n"
    "ds_read_b64 v[50:51], v232 offset:3784\n"
    "v_mfma_f32_16x16x32_f16 v[148:151], v[84:87], v[64:67], v[148:151]\n"
    "ds_read_b128 v[40:43], v233 offset:1216\n"
    "ds_read_b128 v[36:39], v233 offset:704\n"
    "s_waitcnt lgkmcnt(15)\n"
    "v_mfma_f32_16x16x32_f16 v[156:159], v[88:91], v[68:71], v[156:159]\n"
    "ds_read_b128 v[32:35], v233 offset:192\n"
    "v_mfma_f32_16x16x32_f16 v[152:155], v[88:91], v[64:67], v[152:155]\n"
    "ds_read_b64 v[52:53], v232 offset:3264\n"
    "ds_read_b64 v[54:55], v232 offset:3272\n"
    "v_mfma_f32_16x16x32_f16 v[156:159], v[92:95], v[64:67], v[156:159]\n"
    "ds_read_b64 v[56:57], v232 offset:2752\n"
    "s_waitcnt lgkmcnt(15)\n"
    "v_mfma_f32_16x16x32_f16 v[156:159], v[16:19], v[12:15], v[156:159]\n"
    "ds_read_b64 v[58:59], v232 offset:2760\n"
    "ds_read_b64 v[60:61], v232 offset:2240\n"
    "v_mfma_f32_16x16x32_f16 v[152:155], v[16:19], v[8:11], v[152:155]\n"
    "ds_read_b64 v[62:63], v232 offset:2248\n"
    "v_mfma_f32_16x16x32_f16 v[148:151], v[16:19], v[4:7], v[148:151]\n"
    "ds_read_b128 v[76:79], v233 offset:1664\n"
    "ds_read_b64 v[80:81], v232 offset:3712\n"
    "v_mfma_f32_16x16x32_f16 v[144:147], v[16:19], v[0:3], v[144:147]\n"
    "ds_read_b64 v[82:83], v232 offset:3720\n"
    "s_waitcnt lgkmcnt(15)\n"
    "v_mfma_f32_16x16x32_f16 v[156:159], v[20:23], v[8:11], v[156:159]\n"
    "ds_read_b128 v[72:75], v233 offset:1152\n"
    "ds_read_b128 v[68:71], v233 offset:640\n"
    "v_mfma_f32_16x16x32_f16 v[152:155], v[20:23], v[4:7], v[152:155]\n"
    "ds_read_b128 v[64:67], v233 offset:128\n"
    "v_mfma_f32_16x16x32_f16 v[148:151], v[20:23], v[0:3], v[148:151]\n"
    "ds_read_b64 v[84:85], v232 offset:3200\n"
    "ds_read_b64 v[86:87], v232 offset:3208\n"
    "v_mfma_f32_16x16x32_f16 v[156:159], v[24:27], v[4:7], v[156:159]\n"
    "ds_read_b64 v[88:89], v232 offset:2688\n"
    "v_mfma_f32_16x16x32_f16 v[152:155], v[24:27], v[0:3], v[152:155]\n"
    "ds_read_b64 v[90:91], v232 offset:2696\n"
    "ds_read_b64 v[92:93], v232 offset:2176\n"
    "v_mfma_f32_16x16x32_f16 v[156:159], v[28:31], v[0:3], v[156:159]\n"
    "ds_read_b64 v[94:95], v232 offset:2184\n"
    "s_waitcnt lgkmcnt(15)\n"
    "v_mfma_f32_16x16x32_f16 v[156:159], v[48:51], v[44:47], v[156:159]\n"
    "ds_read_b128 v[12:15], v233 offset:1600\n"
    "ds_read_b64 v[16:17], v232 offset:3648\n"
    "v_mfma_f32_16x16x32_f16 v[152:155], v[48:51], v[40:43], v[152:155]\n"
    "ds_read_b64 v[18:19], v232 offset:3656\n"
    "v_mfma_f32_16x16x32_f16 v[148:151], v[48:51], v[36:39], v[148:151]\n"
    "ds_read_b128 v[8:11], v233 offset:1088\n"
    "v_mfma_f32_16x16x32_f16 v[144:147], v[48:51], v[32:35], v[144:147]\n"
    "ds_read_b128 v[4:7], v233 offset:576\n"
    "buffer_load_dwordx4 v[192:195], v220, s[36:39], 0 offen sc1\n"
    "buffer_load_dwordx4 v[196:199], v220, s[36:39], 0 offen offset:16 sc1\n"
    "v_mfma_f32_16x16x32_f16 v[156:159], v[52:55], v[40:43], v[156:159]\n"
    "ds_read_b128 v[0:3], v233 offset:64\n"
    "v_mfma_f32_16x16x32_f16 v[152:155], v[52:55], v[36:39], v[152:155]\n"
    "ds_read_b64 v[20:21], v232 offset:3136\n"
    "ds_read_b64 v[22:23], v232 offset:3144\n"
    "v_mfma_f32_16x16x32_f16 v[148:151], v[52:55], v[32:35], v[148:151]\n"
    "ds_read_b64 v[24:25], v232 offset:2624\n"
    "s_waitcnt lgkmcnt(15)\n"
    "v_mfma_f32_16x16x32_f16 v[156:159], v[56:59], v[36:39], v[156:159]\n"
    "ds_read_b64 v[26:27], v232 offset:2632\n"
    "v_mfma_f32_16x16x32_f16 v[152:155], v[56:59], v[32:35], v[152:155]\n"
    "ds_read_b64 v[28:29], v232 offset:2112\n"
    "v_mfma_f32_16x16x32_f16 v[156:159], v[60:63], v[32:35], v[156:159]\n"
    "ds_read_b64 v[30:31], v232 offset:2120\n"
    "v_mfma_f32_16x16x32_f16 v[156:159], v[80:83], v[76:79], v[156:159]\n"
    "ds_read_b128 v[44:47], v233 offset:1536\n"
    "ds_read_b64 v[48:49], v232 offset:3584\n"
    "v_mfma_f32_16x16x32_f16 v[152:155], v[80:83], v[72:75], v[152:155]\n"
    "ds_read_b64 v[50:51], v232 offset:3592\n"
    "v_mfma_f32_16x16x32_f16 v[148:151], v[80:83], v[68:71], v[148:151]\n"
    "ds_read_b128 v[40:43], v233 offset:1024\n"
    "v_mfma_f32_16x16x32_f16 v[144:147], v[80:83], v[64:67], v[144:147]\n"
    "ds_read_b128 v[36:39], v233 offset:512\n"
    "s_waitcnt lgkmcnt(15)\n"
    "v_mfma_f32_16x16x32_f16 v[156:159], v[84:87], v[72:75], v[156:159]\n"
    "ds_read_b128 v[32:35], v233 offset:0\n"
    "v_mfma_f32_16x16x32_f16 v[152:155], v[84:87], v[68:71], v[152:155]\n"
    "ds_read_b64 v[52:53], v232 offset:3072\n"
    "ds_read_b64 v[54:55], v232 offset:3080\n"
    "v_mfma_f32_16x16x32_f16 v[148:151], v[84:87], v[64:67], v[148:151]\n"
    "ds_read_b64 v[56:57], v232 offset:2560\n"
    "v_mfma_f32_16x16x32_f16 v[156:159], v[88:91], v[68:71], v[156:159]\n"
    "ds_read_b64 v[58:59], v232 offset:2568\n"
    "v_mfma_f32_16x16x32_f16 v[152:155], v[88:91], v[64:67], v[152:155]\n"
    "ds_read_b64 v[60:61], v232 offset:2048\n"
    "v_mfma_f32_16x16x32_f16 v[156:159], v[92:95], v[64:67], v[156:159]\n"
    "ds_read_b64 v[62:63], v232 offset:2056\n"
    "s_waitcnt lgkmcnt(15)\n"
    "v_mfma_f32_16x16x32_f16 v[156:159], v[16:19], v[12:15], v[156:159]\n"
    "v_mfma_f32_16x16x32_f16 v[152:155], v[16:19], v[8:11], v[152:155]\n"
    "v_mfma_f32_16x16x32_f16 v[148:151], v[16:19], v[4:7], v[148:151]\n"
    "v_mfma_f32_16x16x32_f16 v[144:147], v[16:19], v[0:3], v[144:147]\n"
    "v_mfma_f32_16x16x32_f16 v[156:159], v[20:23], v[8:11], v[156:159]\n"
    "v_mfma_f32_16x16x32_f16 v[152:155], v[20:23], v[4:7], v[152:155]\n"
    "v_mfma_f32_16x16x32_f16 v[148:151], v[20:23], v[0:3], v[148:151]\n"
    "s_waitcnt lgkmcnt(14)\n"
    "v_mfma_f32_16x16x32_f16 v[156:159], v[24:27], v[4:7], v[156:159]\n"
    "v_mfma_f32_16x16x32_f16 v[152:155], v[24:27], v[0:3], v[152:155]\n"
    "s_waitcnt lgkmcnt(12)\n"
    "v_mfma_f32_16x16x32_f16 v[156:159], v[28:31], v[0:3], v[156:159]\n"
    "s_waitcnt lgkmcnt(9)\n"
    "v_mfma_f32_16x16x32_f16 v[156:159], v[48:51], v[44:47], v[156:159]\n"
    "s_waitcnt lgkmcnt(8)\n"
    "v_mfma_f32_16x16x32_f16 v[152:155], v[48:51], v[40:43], v[152:155]\n"
    "s_waitcnt lgkmcnt(7)\n"
    "v_mfma_f32_16x16x32_f16 v[148:151], v[48:51], v[36:39], v[148:151]\n"
    "s_waitcnt lgkmcnt(6)\n"
    "v_mfma_f32_16x16x32_f16 v[144:147], v[48:51], v[32:35], v[144:147]\n"
    "s_waitcnt lgkmcnt(4)\n"
    "v_mfma_f32_16x16x32_f16 v[156:159], v[52:55], v[40:43], v[156:159]\n"
    "v_mfma_f32_16x16x32_f16 v[152:155], v[52:55], v[36:39], v[152:155]\n"
    "v_mfma_f32_16x16x32_f16 v[148:151], v[52:55], v[32:35], v[148:151]\n"
    "s_waitcnt lgkmcnt(2)\n"
    "v_mfma_f32_16x16x32_f16 v[156:159], v[56:59], v[36:39], v[156:159]\n"
    "v_mfma_f32_16x16x32_f16 v[152:155], v[56:59], v[32:35], v[152:155]\n"
    "s_waitcnt lgkmcnt(0)\n"
    "v_mfma_f32_16x16x32_f16 v[156:159], v[60:63], v[32:35], v[156:159]\n"
    "v_cvt_pk_bf16_f32 v176, v144, v145\n"
    "v_cvt_pk_bf16_f32 v177, v146, v147\n"
    "ds_write2_b32 v180, v176, v177 offset0:0 offset1:2\n"
    "v_cvt_pk_bf16_f32 v178, v148, v149\n"
    "v_cvt_pk_bf16_f32 v179, v150, v151\n"
    "ds_write2_b32 v180, v178, v179 offset0:128 offset1:130\n"
    "v_cvt_pk_bf16_f32 v176, v152, v153\n"
    "v_cvt_pk_bf16_f32 v177, v154, v155\n"
    "ds_write2_b32 v181, v176, v177 offset0:0 offset1:2\n"
    "v_cvt_pk_bf16_f32 v178, v156, v157\n"
    "v_cvt_pk_bf16_f32 v179, v158, v159\n"
    "ds_write2_b32 v181, v178, v179 offset0:128 offset1:130\n"
    "s_waitcnt lgkmcnt(0)\n"
    "s_barrier\n"
    "ds_read_b64 v[96:97], v232 offset:4032\n"
    "ds_read_b64 v[98:99], v232 offset:4040\n"
    "ds_read_b64 v[100:101], v232 offset:3520\n"
    "ds_read_b64 v[102:103], v232 offset:3528\n"
    "ds_read_b128 v[76:79], v233 offset:1984\n"
    "ds_read_b64 v[104:105], v232 offset:3008\n"
    "ds_read_b64 v[106:107], v232 offset:3016\n"
    "ds_read_b128 v[72:75], v233 offset:1472\n"
    "ds_read_b64 v[108:109], v232 offset:2496\n"
    "ds_read_b64 v[110:111], v232 offset:2504\n"
    "ds_read_b128 v[68:71], v233 offset:960\n"
    "ds_read_b64 v[112:113], v232 offset:1984\n"
    "ds_read_b64 v[114:115], v232 offset:1992\n"
    "ds_read_b128 v[64:67], v233 offset:448\n"
    "ds_read_b64 v[116:117], v232 offset:1472\n"
    "ds_read_b64 v[118:119], v232 offset:1480\n"
    "ds_read_b64 v[120:121], v232 offset:960\n"
    "ds_read_b64 v[122:123], v232 offset:968\n"
    "ds_read_b64 v[124:125], v232 offset:448\n"
    "ds_read_b64 v[126:127], v232 offset:456\n"
    "s_waitcnt vmcnt(0)\n"
    "s_movk_i32 s56, 0x7fff\n"
    "Lgp33_%=:\n"
    "v_xor_b32 v200, s83, v193\n"
    "v_xor_b32 v201, s83, v195\n"
    "v_xor_b32 v202, s83, v197\n"
    "v_xor_b32 v203, s83, v199\n"
    "v_or3_b32 v200, v200, v201, v202\n"
    "v_or_b32 v200, v200, v203\n"
    "v_cmp_ne_u32 vcc, 0, v200\n"
    "s_nop 1\n"
    "s_cbranch_vccz Lgq34_%=\n"
    "s_sub_u32 s56, s56, 1\n"
    "s_cbranch_scc1 Lgq34_%=\n"
    "s_sleep 1\n"
    "buffer_load_dwordx4 v[192:195], v220, s[36:39], 0 offen sc1\n"
    "buffer_load_dwordx4 v[196:199], v220, s[36:39], 0 offen offset:16 sc1\n"
    "s_waitcnt vmcnt(0)\n"
    "s_branch Lgp33_%=\n"
    "Lgq34_%=:\n"
    "v_and_b32 v160, 0xffff0000, v192\n"
    "v_lshlrev_b32 v164, 16, v192\n"
    "v_rcp_f32 v160, v160\n"
    "v_and_b32 v161, 0xffff0000, v194\n"
    "v_lshlrev_b32 v165, 16, v194\n"
    "v_rcp_f32 v161, v161\n"
    "v_and_b32 v162, 0xffff0000, v196\n"
    "v_lshlrev_b32 v166, 16, v196\n"
    "v_rcp_f32 v162, v162\n"
    "v_and_b32 v163, 0xffff0000, v198\n"
    "v_lshlrev_b32 v167, 16, v198\n"
    "v_rcp_f32 v163, v163\n"
    "v_lshlrev_b32 v128, 16, v244\n"
    "v_and_b32 v132, 0xffff0000, v244\n"
    "v_lshlrev_b32 v136, 16, v245\n"
    "v_and_b32 v140, 0xffff0000, v245\n"
    "v_lshlrev_b32 v129, 16, v246\n"
    "v_and_b32 v133, 0xffff0000, v246\n"
    "v_lshlrev_b32 v137, 16, v247\n"
    "v_and_b32 v141, 0xffff0000, v247\n"
    "v_lshlrev_b32 v130, 16, v248\n"
    "v_and_b32 v134, 0xffff0000, v248\n"
    "v_lshlrev_b32 v138, 16, v249\n"
    "v_and_b32 v142, 0xffff0000, v249\n"
    "v_lshlrev_b32 v131, 16, v250\n"
    "v_and_b32 v135, 0xffff0000, v250\n"
    "v_lshlrev_b32 v139, 16, v251\n"
    "v_and_b32 v143, 0xffff0000, v251\n"
    "v_pk_mul_f32 v[164:165], v[164:165], v[160:161] neg_lo:[1,0] neg_hi:[1,0]\n"
    "v_pk_mul_f32 v[166:167], v[166:167], v[162:163] neg_lo:[1,0] neg_hi:[1,0]\n"
    "v_add_u32 v200, 18788, v230\n"
    "v_pk_fma_f32 v[144:145], v[128:129], v[160:161], v[164:165]\n"
    "v_pk_fma_f32 v[146:147], v[130:131], v[162:163], v[166:167]\n"
    "v_cvt_pk_f16_f32 v168, v144, v145\n"
    "v_cvt_pk_f16_f32 v169, v146, v147\n"
    "ds_write2_b32 v200, v168, v169 offset1:1\n"
    "ds_write_b64 v230, v[168:169] offset:448\n"
    "v_pk_fma_f32 v[148:149], v[132:133], v[160:161], v[164:165]\n"
    "v_pk_fma_f32 v[150:151], v[134:135], v[162:163], v[166:167]\n"
    "v_cvt_pk_f16_f32 v170, v148, v149\n"
    "v_cvt_pk_f16_f32 v171, v150, v151\n"
    "v_add_u32 v200, 4576, v200\n"
    "ds_write2_b32 v200, v170, v171 offset1:1\n"
    "ds_write_b64 v230, v[170:171] offset:5024\n"
    "v_pk_fma_f32 v[152:153], v[136:137], v[160:161], v[164:165]\n"
    "v_pk_fma_f32 v[154:155], v[138:139], v[162:163], v[166:167]\n"
    "v_cvt_pk_f16_f32 v172, v152, v153\n"
    "v_cvt_pk_f16_f32 v173, v154, v155\n"
    "v_add_u32 v200, 4576, v200\n"
    "ds_write2_b32 v200, v172, v173 offset1:1\n"
    "ds_write_b64 v230, v[172:173] offset:9600\n"
    "v_pk_fma_f32 v[156:157], v[140:141], v[160:161], v[164:165]\n"
    "v_pk_fma_f32 v[158:159], v[142:143], v[162:163], v[166:167]\n"
    "v_cvt_pk_f16_f32 v174, v156, v157\n"
    "v_cvt_pk_f16_f32 v175, v158, v159\n"
    "v_add_u32 v200, 4576, v200\n"
    "ds_write2_b32 v200, v174, v175 offset1:1\n"
    "ds_write_b64 v230, v[174:175] offset:14176\n"
    "s_mov_b64 exec, 1\n"
    "ds_add_u32 v190, v191 offset:12\n"
    "s_mov_b64 exec, -1\n"
    "v_pk_fma_f32 v[128:129], v[144:145], s[40:41], v[128:129] op_sel:[0,0,0] op_sel_hi:[1,0,1]\n"
    "v_pk_fma_f32 v[130:131], v[146:147], s[40:41], v[130:131] op_sel:[0,0,0] op_sel_hi:[1,0,1]\n"
    "v_pk_fma_f32 v[132:133], v[148:149], s[40:41], v[132:133] op_sel:[0,1,0] op_sel_hi:[1,1,1]\n"
    "v_pk_fma_f32 v[134:135], v[150:151], s[40:41], v[134:135] op_sel:[0,1,0] op_sel_hi:[1,1,1]\n"
    "v_pk_fma_f32 v[136:137], v[152:153], s[42:43], v[136:137] op_sel:[0,0,0] op_sel_hi:[1,0,1]\n"
    "v_pk_fma_f32 v[138:139], v[154:155], s[42:43], v[138:139] op_sel:[0,0,0] op_sel_hi:[1,0,1]\n"
    "v_pk_fma_f32 v[140:141], v[156:157], s[42:43], v[140:141] op_sel:[0,1,0] op_sel_hi:[1,1,1]\n"
    "v_pk_fma_f32 v[142:143], v[158:159], s[42:43], v[142:143] op_sel:[0,1,0] op_sel_hi:[1,1,1]\n"
    "v_subrev_u32 v230, 2048, v230\n"
    "s_movk_i32 s56, 0x7fff\n"
    "Llw35_%=:\n"
    "ds_read_b32 v183, v190 offset:12\n"
    "s_waitcnt lgkmcnt(0)\n"
    "v_readfirstlane_b32 s57, v183\n"
    "s_nop 0\n"
    "s_cmp_ge_u32 s57, 4\n"
    "s_cbranch_scc1 Llx36_%=\n"
    "s_sub_u32 s56, s56, 1\n"
    "s_cbranch_scc0 Llw35_%=\n"
    "Llx36_%=:\n"
    "ds_read_b128 v[92:95], v233 offset:4032\n"
    "ds_read_b128 v[88:91], v233 offset:3520\n"
    "ds_read_b128 v[84:87], v233 offset:3008\n"
    "ds_read_b128 v[80:83], v233 offset:2496\n"
    "s_waitcnt lgkmcnt(3)\n"
    "v_mfma_f32_16x16x32_f16 v[172:175], v[96:99], v[92:95], 0\n"
    "ds_read_b128 v[28:31], v233 offset:3968\n"
    "s_waitcnt lgkmcnt(3)\n"
    "v_mfma_f32_16x16x32_f16 v[168:171], v[96:99], v[88:91], 0\n"
    "ds_read_b64 v[32:33], v232 offset:3968\n"
    "s_waitcnt lgkmcnt(3)\n"
    "v_mfma_f32_16x16x32_f16 v[164:167], v[96:99], v[84:87], 0\n"
    "ds_read_b64 v[34:35], v232 offset:3976\n"
    "s_waitcnt lgkmcnt(3)\n"
    "v_mfma_f32_16x16x32_f16 v[160:163], v[96:99], v[80:83], 0\n"
    "ds_read_b128 v[24:27], v233 offset:3456\n"
    "v_mfma_f32_16x16x32_f16 v[172:175], v[100:103], v[88:91], v[172:175]\n"
    "ds_read_b128 v[20:23], v233 offset:2944\n"
    "v_mfma_f32_16x16x32_f16 v[168:171], v[100:103], v[84:87], v[168:171]\n"
    "ds_read_b128 v[16:19], v233 offset:2432\n"
    "v_mfma_f32_16x16x32_f16 v[164:167], v[100:103], v[80:83], v[164:167]\n"
    "ds_read_b64 v[36:37], v232 offset:3456\n"
    "v_mfma_f32_16x16x32_f16 v[160:163], v[100:103], v[76:79], v[160:163]\n"
    "ds_read_b64 v[38:39], v232 offset:3464\n"
    "v_mfma_f32_16x16x32_f16 v[172:175], v[104:107], v[84:87], v[172:175]\n"
    "ds_read_b128 v[12:15], v233 offset:1920\n"
    "v_mfma_f32_16x16x32_f16 v[168:171], v[104:107], v[80:83], v[168:171]\n"
    "ds_read_b64 v[40:41], v232 offset:2944\n"
    "v_mfma_f32_16x16x32_f16 v[164:167], v[104:107], v[76:79], v[164:167]\n"
    "ds_read_b64 v[42:43], v232 offset:2952\n"
    "v_mfma_f32_16x16x32_f16 v[160:163], v[104:107], v[72:75], v[160:163]\n"
    "ds_read_b128 v[8:11], v233 offset:1408\n"
    "v_mfma_f32_16x16x32_f16 v[172:175], v[108:111], v[80:83], v[172:175]\n"
    "v_mfma_f32_16x16x32_f16 v[168:171], v[108:111], v[76:79], v[168:171]\n"
    "ds_read_b64 v[44:45], v232 offset:2432\n"
    "v_mfma_f32_16x16x32_f16 v[164:167], v[108:111], v[72:75], v[164:167]\n"
    "ds_read_b64 v[46:47], v232 offset:2440\n"
    "v_mfma_f32_16x16x32_f16 v[160:163], v[108:111], v[68:71], v[160:163]\n"
    "ds_read_b128 v[4:7], v233 offset:896\n"
    "v_mfma_f32_16x16x32_f16 v[172:175], v[112:115], v[76:79], v[172:175]\n"
    "ds_read_b64 v[48:49], v232 offset:1920\n"
    "v_mfma_f32_16x16x32_f16 v[168:171], v[112:115], v[72:75], v[168:171]\n"
    "ds_read_b64 v[50:51], v232 offset:1928\n"
    "v_mfma_f32_16x16x32_f16 v[164:167], v[112:115], v[68:71], v[164:167]\n"
    "ds_read_b128 v[0:3], v233 offset:384\n"
    "v_mfma_f32_16x16x32_f16 v[160:163], v[112:115], v[64:67], v[160:163]\n"
    "ds_read_b64 v[52:53], v232 offset:1408\n"
    "v_mfma_f32_16x16x32_f16 v[172:175], v[116:119], v[72:75], v[172:175]\n"
    "ds_read_b64 v[54:55], v232 offset:1416\n"
    "v_mfma_f32_16x16x32_f16 v[168:171], v[116:119], v[68:71], v[168:171]\n"
    "ds_read_b64 v[56:57], v232 offset:896\n"
    "v_mfma_f32_16x16x32_f16 v[164:167], v[116:119], v[64:67], v[164:167]\n"
    "ds_read_b64 v[58:59], v232 offset:904\n"
    "v_mfma_f32_16x16x32_f16 v[172:175], v[120:123], v[68:71], v[172:175]\n"
    "ds_read_b64 v[60:61], v232 offset:384\n"
    "v_mfma_f32_16x16x32_f16 v[168:171], v[120:123], v[64:67], v[168:171]\n"
    "ds_read_b64 v[62:63], v232 offset:392\n"
    "v_mfma_f32_16x16x32_f16 v[172:175], v[124:127], v[64:67], v[172:175]\n"
    "s_mov_b32 s56, 2\n"
    "Lcl37_%=:\n"
    "s_waitcnt lgkmcnt(15)\n"
    "v_mfma_f32_16x16x32_f16 v[172:175], v[32:35], v[28:31], v[172:175]\n"
    "ds_read_b128 v[92:95], v233 offset:3904\n"
    "v_mfma_f32_16x16x32_f16 v[168:171], v[32:35], v[24:27], v[168:171]\n"
    "ds_read_b64 v[96:97], v232 offset:3904\n"
    "v_mfma_f32_16x16x32_f16 v[164:167], v[32:35], v[20:23], v[164:167]\n"
    "ds_read_b64 v[98:99], v232 offset:3912\n"
    "v_mfma_f32_16x16x32_f16 v[160:163], v[32:35], v[16:19], v[160:163]\n"
    "ds_read_b128 v[88:91], v233 offset:3392\n"
    "v_mfma_f32_16x16x32_f16 v[172:175], v[36:39], v[24:27], v[172:175]\n"
    "ds_read_b128 v[84:87], v233 offset:2880\n"
    "v_mfma_f32_16x16x32_f16 v[168:171], v[36:39], v[20:23], v[168:171]\n"
    "ds_read_b128 v[80:83], v233 offset:2368\n"
    "v_mfma_f32_16x16x32_f16 v[164:167], v[36:39], v[16:19], v[164:167]\n"
    "ds_read_b64 v[100:101], v232 offset:3392\n"
    "v_mfma_f32_16x16x32_f16 v[160:163], v[36:39], v[12:15], v[160:163]\n"
    "ds_read_b64 v[102:103], v232 offset:3400\n"
    "s_waitcnt lgkmcnt(15)\n"
    "v_mfma_f32_16x16x32_f16 v[172:175], v[40:43], v[20:23], v[172:175]\n"
    "ds_read_b128 v[76:79], v233 offset:1856\n"
    "v_mfma_f32_16x16x32_f16 v[168:171], v[40:43], v[16:19], v[168:171]\n"
    "ds_read_b64 v[104:105], v232 offset:2880\n"
    "v_mfma_f32_16x16x32_f16 v[164:167], v[40:43], v[12:15], v[164:167]\n"
    "ds_read_b64 v[106:107], v232 offset:2888\n"
    "v_mfma_f32_16x16x32_f16 v[160:163], v[40:43], v[8:11], v[160:163]\n"
    "ds_read_b128 v[72:75], v233 offset:1344\n"
    "v_mfma_f32_16x16x32_f16 v[172:175], v[44:47], v[16:19], v[172:175]\n"
    "v_mfma_f32_16x16x32_f16 v[168:171], v[44:47], v[12:15], v[168:171]\n"
    "ds_read_b64 v[108:109], v232 offset:2368\n"
    "v_mfma_f32_16x16x32_f16 v[164:167], v[44:47], v[8:11], v[164:167]\n"
    "ds_read_b64 v[110:111], v232 offset:2376\n"
    "v_mfma_f32_16x16x32_f16 v[160:163], v[44:47], v[4:7], v[160:163]\n"
    "ds_read_b128 v[68:71], v233 offset:832\n"
    "v_mfma_f32_16x16x32_f16 v[172:175], v[48:51], v[12:15], v[172:175]\n"
    "ds_read_b64 v[112:113], v232 offset:1856\n"
    "v_mfma_f32_16x16x32_f16 v[168:171], v[48:51], v[8:11], v[168:171]\n"
    "ds_read_b64 v[114:115], v232 offset:1864\n"
    "v_mfma_f32_16x16x32_f16 v[164:167], v[48:51], v[4:7], v[164:167]\n"
    "ds_read_b128 v[64:67], v233 offset:320\n"
    "s_waitcnt lgkmcnt(15)\n"
    "v_mfma_f32_16x16x32_f16 v[160:163], v[48:51], v[0:3], v[160:163]\n"
    "ds_read_b64 v[116:117], v232 offset:1344\n"
    "v_mfma_f32_16x16x32_f16 v[172:175], v[52:55], v[8:11], v[172:175]\n"
    "ds_read_b64 v[118:119], v232 offset:1352\n"
    "v_mfma_f32_16x16x32_f16 v[168:171], v[52:55], v[4:7], v[168:171]\n"
    "ds_read_b64 v[120:121], v232 offset:832\n"
    "v_mfma_f32_16x16x32_f16 v[164:167], v[52:55], v[0:3], v[164:167]\n"
    "ds_read_b64 v[122:123], v232 offset:840\n"
    "v_mfma_f32_16x16x32_f16 v[172:175], v[56:59], v[4:7], v[172:175]\n"
    "ds_read_b64 v[124:125], v232 offset:320\n"
    "v_mfma_f32_16x16x32_f16 v[168:171], v[56:59], v[0:3], v[168:171]\n"
    "ds_read_b64 v[126:127], v232 offset:328\n"
    "v_mfma_f32_16x16x32_f16 v[172:175], v[60:63], v[0:3], v[172:175]\n"
    "v_mfma_f32_16x16x32_f16 v[172:175], v[96:99], v[92:95], v[172:175]\n"
    "ds_read_b128 v[28:31], v233 offset:3840\n"
    "s_waitcnt lgkmcnt(15)\n"
    "v_mfma_f32_16x16x32_f16 v[168:171], v[96:99], v[88:91], v[168:171]\n"
    "ds_read_b64 v[32:33], v232 offset:3840\n"
    "v_mfma_f32_16x16x32_f16 v[164:167], v[96:99], v[84:87], v[164:167]\n"
    "ds_read_b64 v[34:35], v232 offset:3848\n"
    "v_mfma_f32_16x16x32_f16 v[160:163], v[96:99], v[80:83], v[160:163]\n"
    "ds_read_b128 v[24:27], v233 offset:3328\n"
    "v_mfma_f32_16x16x32_f16 v[172:175], v[100:103], v[88:91], v[172:175]\n"
    "ds_read_b128 v[20:23], v233 offset:2816\n"
    "v_mfma_f32_16x16x32_f16 v[168:171], v[100:103], v[84:87], v[168:171]\n"
    "ds_read_b128 v[16:19], v233 offset:2304\n"
    "v_mfma_f32_16x16x32_f16 v[164:167], v[100:103], v[80:83], v[164:167]\n"
    "ds_read_b64 v[36:37], v232 offset:3328\n"
    "v_mfma_f32_16x16x32_f16 v[160:163], v[100:103], v[76:79], v[160:163]\n"
    "ds_read_b64 v[38:39], v232 offset:3336\n"
    "s_waitcnt lgkmcnt(15)\n"
    "v_mfma_f32_16x16x32_f16 v[172:175], v[104:107], v[84:87], v[172:175]\n"
    "ds_read_b128 v[12:15], v233 offset:1792\n"
    "v_mfma_f32_16x16x32_f16 v[168:171], v[104:107], v[80:83], v[168:171]\n"
    "ds_read_b64 v[40:41], v232 offset:2816\n"
    "v_mfma_f32_16x16x32_f16 v[164:167], v[104:107], v[76:79], v[164:167]\n"
    "ds_read_b64 v[42:43], v232 offset:2824\n"
    "s_sub_u32 s56, s56, 1\n"
    "v_mfma_f32_16x16x32_f16 v[160:163], v[104:107], v[72:75], v[160:163]\n"
    "ds_read_b128 v[8:11], v233 offset:1280\n"
    "v_mfma_f32_16x16x32_f16 v[172:175], v[108:111], v[80:83], v[172:175]\n"
    "v_mfma_f32_16x16x32_f16 v[168:171], v[108:111], v[76:79], v[168:171]\n"
    "ds_read_b64 v[44:45], v232 offset:2304\n"
    "v_mfma_f32_16x16x32_f16 v[164:167], v[108:111], v[72:75], v[164:167]\n"
    "ds_read_b64 v[46:47], v232 offset:2312\n"
    "v_mfma_f32_16x16x32_f16 v[160:163], v[108:111], v[68:71], v[160:163]\n"
    "ds_read_b128 v[4:7], v233 offset:768\n"
    "v_mfma_f32_16x16x32_f16 v[172:175], v[112:115], v[76:79], v[172:175]\n"
    "ds_read_b64 v[48:49], v232 offset:1792\n"
    "v_mfma_f32_16x16x32_f16 v[168:171], v[112:115], v[72:75], v[168:171]\n"
    "ds_read_b64 v[50:51], v232 offset:1800\n"
    "v_mfma_f32_16x16x32_f16 v[164:167], v[112:115], v[68:71], v[164:167]\n"
    "ds_read_b128 v[0:3], v233 offset:256\n"
    "s_waitcnt lgkmcnt(15)\n"
    "v_mfma_f32_16x16x32_f16 v[160:163], v[112:115], v[64:67], v[160:163]\n"
    "ds_read_b64 v[52:53], v232 offset:1280\n"
    "v_mfma_f32_16x16x32_f16 v[172:175], v[116:119], v[72:75], v[172:175]\n"
    "ds_read_b64 v[54:55], v232 offset:1288\n"
    "v_mfma_f32_16x16x32_f16 v[168:171], v[116:119], v[68:71], v[168:171]\n"
    "ds_read_b64 v[56:57], v232 offset:768\n"
    "v_mfma_f32_16x16x32_f16 v[164:167], v[116:119], v[64:67], v[164:167]\n"
    "ds_read_b64 v[58:59], v232 offset:776\n"
    "v_mfma_f32_16x16x32_f16 v[172:175], v[120:123], v[68:71], v[172:175]\n"
    "ds_read_b64 v[60:61], v232 offset:256\n"
    "v_mfma_f32_16x16x32_f16 v[168:171], v[120:123], v[64:67], v[168:171]\n"
    "ds_read_b64 v[62:63], v232 offset:264\n"
    "v_mfma_f32_16x16x32_f16 v[172:175], v[124:127], v[64:67], v[172:175]\n"
    "v_subrev_u32 v232, 128, v232\n"
    "v_subrev_u32 v233, 128, v233\n"
    "s_cbranch_scc0 Lcl37_%=\n"
    "v_mfma_f32_16x16x32_f16 v[172:175], v[32:35], v[28:31], v[172:175]\n"
    "s_waitcnt lgkmcnt(15)\n"
    "v_mfma_f32_16x16x32_f16 v[168:171], v[32:35], v[24:27], v[168:171]\n"
    "v_mfma_f32_16x16x32_f16 v[164:167], v[32:35], v[20:23], v[164:167]\n"
    "v_mfma_f32_16x16x32_f16 v[160:163], v[32:35], v[16:19], v[160:163]\n"
    "v_mfma_f32_16x16x32_f16 v[172:175], v[36:39], v[24:27], v[172:175]\n"
    "v_mfma_f32_16x16x32_f16 v[168:171], v[36:39], v[20:23], v[168:171]\n"
    "v_mfma_f32_16x16x32_f16 v[164:167], v[36:39], v[16:19], v[164:167]\n"
    "v_mfma_f32_16x16x32_f16 v[160:163], v[36:39], v[12:15], v[160:163]\n"
    "s_waitcnt lgkmcnt(13)\n"
    "v_mfma_f32_16x16x32_f16 v[172:175], v[40:43], v[20:23], v[172:175]\n"
    "v_mfma_f32_16x16x32_f16 v[168:171], v[40:43], v[16:19], v[168:171]\n"
    "v_mfma_f32_16x16x32_f16 v[164:167], v[40:43], v[12:15], v[164:167]\n"
    "s_waitcnt lgkmcnt(12)\n"
    "v_mfma_f32_16x16x32_f16 v[160:163], v[40:43], v[8:11], v[160:163]\n"
    "s_waitcnt lgkmcnt(10)\n"
    "v_mfma_f32_16x16x32_f16 v[172:175], v[44:47], v[16:19], v[172:175]\n"
    "v_mfma_f32_16x16x32_f16 v[168:171], v[44:47], v[12:15], v[168:171]\n"
    "v_mfma_f32_16x16x32_f16 v[164:167], v[44:47], v[8:11], v[164:167]\n"
    "s_waitcnt lgkmcnt(9)\n"
    "v_mfma_f32_16x16x32_f16 v[160:163], v[44:47], v[4:7], v[160:163]\n"
    "s_waitcnt lgkmcnt(7)\n"
    "v_mfma_f32_16x16x32_f16 v[172:175], v[48:51], v[12:15], v[172:175]\n"
    "v_mfma_f32_16x16x32_f16 v[168:171], v[48:51], v[8:11], v[168:171]\n"
    "v_mfma_f32_16x16x32_f16 v[164:167], v[48:51], v[4:7], v[164:167]\n"
    "s_waitcnt lgkmcnt(6)\n"
    "v_mfma_f32_16x16x32_f16 v[160:163], v[48:51], v[0:3], v[160:163]\n"
    "s_waitcnt lgkmcnt(4)\n"
    "v_mfma_f32_16x16x32_f16 v[172:175], v[52:55], v[8:11], v[172:175]\n"
    "v_mfma_f32_16x16x32_f16 v[168:171], v[52:55], v[4:7], v[168:171]\n"
    "v_mfma_f32_16x16x32_f16 v[164:167], v[52:55], v[0:3], v[164:167]\n"
    "s_waitcnt lgkmcnt(2)\n"
    "v_mfma_f32_16x16x32_f16 v[172:175], v[56:59], v[4:7], v[172:175]\n"
    "v_mfma_f32_16x16x32_f16 v[168:171], v[56:59], v[0:3], v[168:171]\n"
    "s_waitcnt lgkmcnt(0)\n"
    "v_mfma_f32_16x16x32_f16 v[172:175], v[60:63], v[0:3], v[172:175]\n"
    "v_cvt_pk_bf16_f32 v184, v160, v161\n"
    "v_cvt_pk_bf16_f32 v185, v162, v163\n"
    "ds_write2_b32 v188, v184, v185 offset0:0 offset1:2\n"
    "v_cvt_pk_bf16_f32 v186, v164, v165\n"
    "v_cvt_pk_bf16_f32 v187, v166, v167\n"
    "ds_write2_b32 v188, v186, v187 offset0:128 offset1:130\n"
    "v_cvt_pk_bf16_f32 v184, v168, v169\n"
    "v_cvt_pk_bf16_f32 v185, v170, v171\n"
    "ds_write2_b32 v189, v184, v185 offset0:0 offset1:2\n"
    "v_cvt_pk_bf16_f32 v186, v172, v173\n"
    "v_cvt_pk_bf16_f32 v187, v174, v175\n"
    "ds_write2_b32 v189, v186, v187 offset0:128 offset1:130\n"
    "s_mov_b64 exec, 1\n"
    "ds_add_u32 v190, v191 offset:4\n"
    "s_mov_b64 exec, -1\n"
    "s_movk_i32 s56, 0x7fff\n"
    "Llw38_%=:\n"
    "ds_read_b32 v183, v190 offset:4\n"
    "s_waitcnt lgkmcnt(0)\n"
    "v_readfirstlane_b32 s57, v183\n"
    "s_nop 0\n"
    "s_cmp_ge_u32 s57, 4\n"
    "s_cbranch_scc1 Llx39_%=\n"
    "s_sub_u32 s56, s56, 1\n"
    "s_cbranch_scc0 Llw38_%=\n"
    "Llx39_%=:\n"
    "ds_read_b64 v[0:1], v230 offset:0\n"
    "ds_read_b64 v[2:3], v230 offset:4576\n"
    "ds_read_b64 v[4:5], v230 offset:9152\n"
    "ds_read_b64 v[6:7], v230 offset:13728\n"
    "s_waitcnt lgkmcnt(3)\n"
    "v_lshlrev_b32 v8, 16, v0\n"
    "v_and_b32 v9, 0xffff0000, v0\n"
    "v_lshlrev_b32 v10, 16, v1\n"
    "v_and_b32 v11, 0xffff0000, v1\n"
    "v_pk_fma_f32 v[8:9], v[8:9], s[68:69], v[128:129] op_sel:[0,0,0] op_sel_hi:[1,0,1]\n"
    "v_pk_fma_f32 v[10:11], v[10:11], s[68:69], v[130:131] op_sel:[0,0,0] op_sel_hi:[1,0,1]\n"
    "v_cvt_pk_bf16_f32 v16, v8, v9\n"
    "v_cvt_pk_bf16_f32 v17, v10, v11\n"
    "buffer_store_dwordx2 v[16:17], v235, s[24:27], s59 offen offset:0 sc1\n"
    "s_waitcnt lgkmcnt(2)\n"
    "v_lshlrev_b32 v8, 16, v2\n"
    "v_and_b32 v9, 0xffff0000, v2\n"
    "v_lshlrev_b32 v10, 16, v3\n"
    "v_and_b32 v11, 0xffff0000, v3\n"
    "v_pk_fma_f32 v[8:9], v[8:9], s[68:69], v[132:133] op_sel:[0,1,0] op_sel_hi:[1,1,1]\n"
    "v_pk_fma_f32 v[10:11], v[10:11], s[68:69], v[134:135] op_sel:[0,1,0] op_sel_hi:[1,1,1]\n"
    "v_cvt_pk_bf16_f32 v18, v8, v9\n"
    "v_cvt_pk_bf16_f32 v19, v10, v11\n"
    "buffer_store_dwordx2 v[18:19], v235, s[24:27], s59 offen offset:128 sc1\n"
    "s_waitcnt lgkmcnt(1)\n"
    "v_lshlrev_b32 v8, 16, v4\n"
    "v_and_b32 v9, 0xffff0000, v4\n"
    "v_lshlrev_b32 v10, 16, v5\n"
    "v_and_b32 v11, 0xffff0000, v5\n"
    "v_pk_fma_f32 v[8:9], v[8:9], s[70:71], v[136:137] op_sel:[0,0,0] op_sel_hi:[1,0,1]\n"
    "v_pk_fma_f32 v[10:11], v[10:11], s[70:71], v[138:139] op_sel:[0,0,0] op_sel_hi:[1,0,1]\n"
    "v_cvt_pk_bf16_f32 v20, v8, v9\n"
    "v_cvt_pk_bf16_f32 v21, v10, v11\n"
    "buffer_store_dwordx2 v[20:21], v235, s[24:27], s59 offen offset:256 sc1\n"
    "s_waitcnt lgkmcnt(0)\n"
    "v_lshlrev_b32 v8, 16, v6\n"
    "v_and_b32 v9, 0xffff0000, v6\n"
    "v_lshlrev_b32 v10, 16, v7\n"
    "v_and_b32 v11, 0xffff0000, v7\n"
    "v_pk_fma_f32 v[8:9], v[8:9], s[70:71], v[140:141] op_sel:[0,1,0] op_sel_hi:[1,1,1]\n"
    "v_pk_fma_f32 v[10:11], v[10:11], s[70:71], v[142:143] op_sel:[0,1,0] op_sel_hi:[1,1,1]\n"
    "v_cvt_pk_bf16_f32 v22, v8, v9\n"
    "v_cvt_pk_bf16_f32 v23, v10, v11\n"
    "buffer_store_dwordx2 v[22:23], v235, s[24:27], s59 offen offset:384 sc1\n"
    "Lhl16_%=:\n"
    "s_cmp_lt_u32 s80, 4\n"
    "s_cselect_b32 s20, s78, s28\n"
    "s_cselect_b32 s21, s79, s29\n"
    "s_and_b32 s21, s21, 0xffff\n"
    "s_mov_b32 s22, 5308416\n"
    "s_mov_b32 s23, 0x20000\n"
    "s_mov_b32 s28, s30\n"
    "s_and_b32 s29, s31, 0xffff\n"
    "s_mov_b32 s30, 5308416\n"
    "s_mov_b32 s31, 0x20000\n"
    "s_mov_b32 s32, s34\n"
    "s_and_b32 s33, s35, 0xffff\n"
    "s_mov_b32 s34, 69468160\n"
    "s_mov_b32 s35, 0x20000\n"
    "s_mov_b32 s40, 884736\n"
    "s_mov_b32 s41, 957440\n"
    "s_mov_b32 s42, 1030144\n"
    "s_mov_b32 s43, 1102848\n"
    "s_mov_b32 s44, 0\n"
    "s_mov_b32 s45, 0\n"
    "s_mov_b32 s46, 8192\n"
    "s_mov_b32 s47, 16384\n"
    "s_mov_b32 s48, 24576\n"
    "s_and_b32 s85, s85, 0xffff\n"
    "s_mov_b32 s86, 2359296\n"
    "s_mov_b32 s87, 0x20000\n"
    "v_mov_b32 v200, 0x3f803f80\n"
    "v_mov_b32 v201, 0x3f803f80\n"
    "v_mov_b32 v202, 0x3f803f80\n"
    "v_mov_b32 v203, 0x3f803f80\n"
    "s_lshl_b32 s60, s81, 1\n"
    "s_and_b32 s60, s60, 14\n"
    "s_bfe_u32 s61, s81, 0x10003\n"
    "s_or_b32 s60, s60, s61\n"
    "s_lshl_b32 s63, s61, 8\n"
    "v_lshrrev_b32_e32 v152, 3, v239\n"
    "v_and_b32_e32 v153, 7, v239\n"
    "s_and_b32 s54, s80, 3\n"
    "s_mul_i32 s62, s60, 0x120\n"
    "s_xor_b32 s63, s63, 0x100\n"
    "v_bitop3_b32 v153, v152, v153, 6 bitop3:0x6c\n"
    "v_mul_u32_u24_e32 v152, 0x2400, v152\n"
    "s_add_i32 s62, s62, s63\n"
    "s_add_i32 s63, s80, 2\n"
    "v_lshl_or_b32 v152, s54, 7, v152\n"
    "s_lshr_b32 s64, s63, 3\n"
    "s_mulk_i32 s60, 0x240\n"
    "v_lshlrev_b32_e32 v153, 4, v153\n"
    "v_lshl_or_b32 v152, s61, 6, v152\n"
    "v_and_b32_e32 v150, 3, v239\n"
    "v_lshrrev_b32_e32 v151, 2, v239\n"
    "s_and_b32 s65, s80, 1\n"
    "v_add3_u32 v213, v152, s60, v153\n"
    "v_bfe_u32 v152, v239, 4, 1\n"
    "s_lshl_b32 s61, s64, 1\n"
    "v_lshl_or_b32 v151, s65, 4, v151\n"
    "v_bitop3_b32 v150, s61, v150, v152 bitop3:0x36\n"
    "v_mul_u32_u24_e32 v151, 0x2400, v151\n"
    "s_lshl_b32 s60, s62, 1\n"
    "v_lshlrev_b32_e32 v150, 4, v150\n"
    "v_add3_u32 v214, s60, v151, v150\n"
    "s_lshl_b32 s60, s65, 11\n"
    "s_lshl_b32 s61, s64, 10\n"
    "s_not_b32 s63, s63\n"
    "s_or_b32 s50, s61, s60\n"
    "s_lshl_b32 s51, s80, 10\n"
    "s_mul_i32 s76, s81, 0x2400\n"
    "s_lshr_b32 s60, s80, 1\n"
    "s_bfe_u32 s61, s81, 0x20002\n"
    "s_lshr_b32 s55, s80, 2\n"
    "s_bfe_u32 s53, s63, 0x10002\n"
    "s_lshl_b32 s49, s80, 12\n"
    "s_bitset1_b32 s50, 15\n"
    "s_add_i32 s51, s51, 0x9000\n"
    "s_bitset1_b32 s76, 26\n"
    "s_xor_b32 s77, s61, s60\n"
    "s_lshl1_add_u32 s56, s83, 1\n"
    "v_mov_b32 v144, s56\n"
    "v_lshlrev_b32 v145, 10, v239\n"
    "s_and_b32 s56, s81, 7\n"
    "s_lshl_b32 s56, s56, 7\n"
    "s_add_u32 s56, s56, 155648\n"
    "s_cmp_eq_u32 s82, 0\n"
    "s_cbranch_scc1 Lnf42_%=\n"
    "s_waitcnt vmcnt(0)\n"
    "s_mov_b64 exec, 1\n"
    "ds_add_rtn_u32 v191, v190, v191 offset:8\n"
    "s_mov_b64 exec, -1\n"
    "s_waitcnt lgkmcnt(0)\n"
    "v_readfirstlane_b32 s57, v191\n"
    "s_nop 0\n"
    "s_cmp_lg_u32 s57, 3\n"
    "s_cbranch_scc1 Lnf42_%=\n"
    "s_mov_b64 exec, 0xffff\n"
    "buffer_atomic_add v144, v145, s[36:39], s56 offen sc1\n"
    "s_mov_b64 exec, -1\n"
    "Lnf42_%=:\n"
    "s_lshl_b32 s60, s80, 1\n"
    "s_and_b32 s61, s60, 12\n"
    "v_lshrrev_b32_e32 v151, 2, v239\n"
    "s_and_b32 s62, s81, 15\n"
    "v_lshrrev_b32_e32 v150, 4, v239\n"
    "v_and_b32_e32 v152, 3, v239\n"
    "v_and_or_b32 v153, v151, 3, s61\n"
    "s_bitcmp1_b32 s80, 0\n"
    "s_cselect_b32 s63, 0x1200, 0\n"
    "v_lshlrev_b32_e32 v150, 6, v150\n"
    "v_lshlrev_b32_e32 v152, 4, v152\n"
    "v_cmp_eq_u32_e32 vcc, s62, v153\n"
    "v_bfe_u32 v153, v239, 1, 1\n"
    "v_or3_b32 v150, s63, v150, v152\n"
    "s_and_b32 s63, s80, 3\n"
    "v_and_or_b32 v154, v151, 6, v153\n"
    "s_and_b32 s65, s81, 7\n"
    "v_lshlrev_b32_e32 v154, 4, v154\n"
    "s_lshl_b32 s64, s63, 5\n"
    "s_lshl_b32 s66, s65, 1\n"
    "s_bfe_u32 s67, s81, 0x10003\n"
    "v_xor_b32_e32 v155, s64, v154\n"
    "s_add_i32 s60, s60, 4\n"
    "v_and_b32_e32 v156, 8, v151\n"
    "s_lshl_b32 s64, s80, 2\n"
    "s_or_b32 s66, s66, s67\n"
    "s_lshl_b32 s73, s67, 8\n"
    "s_and_b32 s60, s60, 16\n"
    "v_bfe_u32 v158, v239, 3, 2\n"
    "s_mul_i32 s72, s66, 0x120\n"
    "s_xor_b32 s73, s73, 0x100\n"
    "v_and_or_b32 v156, s64, 4, v156\n"
    "v_lshrrev_b32_e32 v157, 3, v239\n"
    "s_add_i32 s73, s72, s73\n"
    "v_or3_b32 v156, v156, v158, s60\n"
    "v_and_b32_e32 v158, 7, v239\n"
    "s_add_i32 s60, s67, s80\n"
    "v_or_b32_e32 v156, s73, v156\n"
    "v_bitop3_b32 v158, v157, v158, 6 bitop3:0x6c\n"
    "s_lshl_b32 s60, s60, 5\n"
    "v_mul_u32_u24_e32 v156, 0x480, v156\n"
    "v_lshlrev_b32_e32 v158, 4, v158\n"
    "s_add_i32 s60, s60, s72\n"
    "v_or_b32_e32 v217, v156, v158\n"
    "v_or_b32_e32 v156, s60, v157\n"
    "s_lshr_b32 s60, s81, 4\n"
    "s_lshl_b32 s64, s60, 1\n"
    "s_lshr_b32 s72, s80, 2\n"
    "s_or_b32 s64, s64, s72\n"
    "v_bfrev_b32_e32 v152, 1\n"
    "s_xor_b32 s64, s64, 30\n"
    "v_cndmask_b32_e32 v219, v152, v150, vcc\n"
    "v_lshlrev_b32_e32 v150, 3, v239\n"
    "s_mulk_i32 s64, 0x240\n"
    "s_lshl_b32 s74, s63, 3\n"
    "v_and_b32_e32 v150, 8, v150\n"
    "v_mul_u32_u24_e32 v156, 0x480, v156\n"
    "s_or_b32 s64, s64, s74\n"
    "v_lshl_or_b32 v152, v151, 7, v150\n"
    "v_or_b32_e32 v216, v156, v158\n"
    "v_or_b32_e32 v156, s64, v157\n"
    "v_lshlrev_b32_e32 v151, 6, v151\n"
    "v_lshl_or_b32 v215, v156, 7, v158\n"
    "v_lshl_or_b32 v151, v153, 10, v151\n"
    "v_bfe_u32 v153, v239, 4, 1\n"
    "v_and_b32_e32 v156, 2, v239\n"
    "v_bitop3_b32 v156, v153, s63, v156 bitop3:0x36\n"
    "v_lshl_or_b32 v151, v156, 4, v151\n"
    "s_mov_b32 s64, 0x8000\n"
    "v_or3_b32 v212, v151, v150, s64\n"
    "s_lshl_b32 s64, s72, 12\n"
    "s_add_i32 s64, s64, 0x9000\n"
    "v_or_b32_e32 v150, s64, v152\n"
    "s_movk_i32 s64, 0x60\n"
    "v_or_b32_e32 v208, v155, v150\n"
    "v_bitop3_b32 v211, v155, s64, v150 bitop3:0x36\n"
    "v_bitop3_b32 v210, v155, 64, v150 bitop3:0x36\n"
    "v_bitop3_b32 v209, v155, 32, v150 bitop3:0x36\n"
    "v_lshl_or_b32 v150, s63, 12, v152\n"
    "s_lshl_b32 s63, s63, 8\n"
    "v_or_b32_e32 v204, v154, v150\n"
    "v_bitop3_b32 v207, v154, s64, v150 bitop3:0x36\n"
    "v_bitop3_b32 v206, v154, 64, v150 bitop3:0x36\n"
    "v_bitop3_b32 v205, v154, 32, v150 bitop3:0x36\n"
    "v_and_or_b32 v150, v239, 48, s63\n"
    "s_mul_i32 s64, s66, 0x480\n"
    "v_lshl_or_b32 v150, s67, 7, v150\n"
    "v_add_u32_e32 v150, s64, v150\n"
    "s_movk_i32 s61, 0x1200\n"
    "v_add_u32_e32 v252, 0x8000, v150\n"
    "v_lshrrev_b32_e32 v150, 5, v239\n"
    "v_mov_b32_e32 v151, s73\n"
    "v_mad_u32_u24 v150, v150, s61, v151\n"
    "v_lshlrev_b32_e32 v151, 2, v153\n"
    "v_or3_b32 v150, v150, v151, s74\n"
    "v_mov_b32_e32 v151, 0x8000\n"
    "s_lshl_b32 s61, s65, 7\n"
    "v_lshl_add_u32 v253, v150, 2, v151\n"
    "v_lshl_or_b32 v150, v239, 10, s61\n"
    "s_lshl_b32 s61, s60, 8\n"
    "v_lshl_add_u32 v151, v239, 7, s61\n"
    "v_or_b32_e32 v218, v155, v152\n"
    "v_add_u32_e32 v151, 0x5700, v151\n"
    "v_mov_b32_e32 v152, 0x6000\n"
    "v_cmp_gt_u32_e32 vcc, 20, v239\n"
    "v_add_u32_e32 v150, 0x2a000, v150\n"
    "s_lshl_b32 s60, s60, 3\n"
    "v_cndmask_b32_e32 v151, v152, v151, vcc\n"
    "v_cmp_gt_u32_e32 vcc, 18, v239\n"
    "s_or_b32 s60, s60, s80\n"
    "s_mul_i32 s66, s66, 0x51000\n"
    "v_cndmask_b32_e32 v254, v151, v150, vcc\n"
    "v_min_u32_e32 v150, 20, v239\n"
    "v_mad_u64_u32 v[150:151], s[60:61], s60, 21, v[150:151]\n"
    "s_lshl_b32 s61, s62, 10\n"
    "s_lshl_b32 s62, s81, 7\n"
    "s_and_b32 s62, s62, 0x4000\n"
    "s_or_b32 s61, s61, s62\n"
    "s_mul_i32 s60, s80, 0x1c00\n"
    "s_xor_b32 s61, s61, 0x4000\n"
    "s_add_i32 s60, s60, 0x14c00\n"
    "s_add_i32 s61, s61, 0x22000\n"
    "s_cmp_eq_u32 s80, 0\n"
    "v_lshl_add_u32 v250, v150, 7, s66\n"
    "s_cselect_b32 s60, s61, s60\n"
    "v_min_u32_e32 v150, 55, v239\n"
    "v_lshl_add_u32 v249, v150, 7, s60\n"
    "Lpo20_%=:\n"
    "s_movk_i32 s56, 0x7fff\n"
    "s_cmp_lg_u32 s80, 0\n"
    "s_cbranch_scc1 Lpq44_%=\n"
    "s_mul_i32 s57, s83, 36\n"
    "s_add_u32 s57, s57, 18\n"
    "s_mov_b64 exec, 0xff\n"
    "Lpa43_%=:\n"
    "v_mov_b32 v148, 0\n"
    "buffer_atomic_or v148, v249, s[36:39], 0 offen sc0 sc1\n"
    "s_waitcnt vmcnt(0)\n"
    "v_cmp_ne_u32 vcc, s57, v148\n"
    "s_nop 1\n"
    "s_cbranch_vccz Lpd41_%=\n"
    "s_mov_b64 exec, vcc\n"
    "s_sub_u32 s56, s56, 1\n"
    "s_cbranch_scc0 Lpa43_%=\n"
    "s_branch Lpd41_%=\n"
    "Lpq44_%=:\n"
    "s_lshl_b32 s57, s81, 2\n"
    "s_add_u32 s57, s57, s80\n"
    "s_cmp_ge_u32 s57, 3\n"
    "s_cbranch_scc1 Lnp17_%=\n"
    "Lpl40_%=:\n"
    "buffer_load_dword v148, v249, s[36:39], 0 offen sc1\n"
    "s_waitcnt vmcnt(0)\n"
    "v_cmp_ne_u32 vcc, s83, v148\n"
    "s_nop 1\n"
    "s_cbranch_vccz Lpd41_%=\n"
    "s_sub_u32 s56, s56, 1\n"
    "s_cbranch_scc0 Lpl40_%=\n"
    "Lpd41_%=:\n"
    "s_mov_b64 exec, -1\n"
    "Lnp17_%=:\n"
    "s_barrier\n"
    "s_or_b32 s56, s81, s80\n"
    "s_cbranch_scc1 Lep45_%=\n"
    "v_mov_b32 v144, s83\n"
    "s_mov_b64 exec, 1\n"
    "buffer_store_dword v144, v145, s[36:39], 0 offen sc1\n"
    "s_mov_b64 exec, -1\n"
    "Lep45_%=:\n"
    "s_add_u32 m0, s51, 0\n"
    "s_nop 0\n"
    "buffer_load_dwordx4 v215, s[24:27], s44 offen sc1 lds\n"
    "s_add_u32 s44, s44, 4096\n"
    "s_add_u32 m0, s51, 45056\n"
    "s_nop 0\n"
    "buffer_load_dwordx4 v215, s[24:27], s44 offen sc1 lds\n"
    "s_add_u32 s44, s44, 4096\n"
    "s_add_u32 m0, s51, 90112\n"
    "s_nop 0\n"
    "buffer_load_dwordx4 v215, s[24:27], s44 offen sc1 lds\n"
    "s_add_u32 s44, s44, 4096\n"
    "ds_read_b64_tr_b16 v[144:145], v204 offset:0\n"
    "ds_read_b64_tr_b16 v[146:147], v204 offset:2048\n"
    "ds_read_b64_tr_b16 v[148:149], v204 offset:16384\n"
    "ds_read_b64_tr_b16 v[150:151], v204 offset:18432\n"
    "ds_read_b64_tr_b16 v[152:153], v212 offset:0\n"
    "ds_read_b64_tr_b16 v[154:155], v212 offset:2048\n"
    "ds_read_b64_tr_b16 v[156:157], v205 offset:0\n"
    "ds_read_b64_tr_b16 v[158:159], v205 offset:2048\n"
    "ds_read_b64_tr_b16 v[160:161], v205 offset:16384\n"
    "ds_read_b64_tr_b16 v[162:163], v205 offset:18432\n"
    "ds_read_b64_tr_b16 v[164:165], v206 offset:0\n"
    "ds_read_b64_tr_b16 v[166:167], v206 offset:2048\n"
    "ds_read_b64_tr_b16 v[168:169], v206 offset:16384\n"
    "ds_read_b64_tr_b16 v[170:171], v206 offset:18432\n"
    "ds_read_b64_tr_b16 v[172:173], v207 offset:0\n"
    "ds_read_b64_tr_b16 v[174:175], v207 offset:2048\n"
    "ds_read_b64_tr_b16 v[176:177], v207 offset:16384\n"
    "ds_read_b64_tr_b16 v[178:179], v207 offset:18432\n"
    "s_waitcnt vmcnt(2)\n"
    "s_barrier\n"
    "ds_read_b64_tr_b16 v[180:181], v208 offset:0\n"
    "ds_read_b64_tr_b16 v[182:183], v208 offset:2048\n"
    "ds_read_b64_tr_b16 v[184:185], v209 offset:0\n"
    "ds_read_b64_tr_b16 v[186:187], v209 offset:2048\n"
    "ds_read_b64_tr_b16 v[188:189], v210 offset:0\n"
    "ds_read_b64_tr_b16 v[190:191], v210 offset:2048\n"
    "ds_read_b64_tr_b16 v[192:193], v211 offset:0\n"
    "ds_read_b64_tr_b16 v[194:195], v211 offset:2048\n"
    "s_mov_b32 vcc_lo, s77\n"
    "s_mov_b32 vcc_hi, 0\n"
    "s_mov_b64 vcc, vcc\n"
    "s_sub_u32 s76, s76, 256\n"
    "s_waitcnt lgkmcnt(6)\n"
    "v_mfma_f32_16x16x32_bf16 v[0:3], v[144:147], v[180:183], 0\n"
    "v_mfma_f32_16x16x32_bf16 v[16:19], v[148:151], v[180:183], 0\n"
    "v_mfma_f32_16x16x32_bf16 v[32:35], v[152:155], v[180:183], 0\n"
    "v_mfma_f32_16x16x32_bf16 v[196:199], v[200:203], v[180:183], 0\n"
    "s_waitcnt lgkmcnt(4)\n"
    "v_mfma_f32_16x16x32_bf16 v[4:7], v[144:147], v[184:187], 0\n"
    "v_mfma_f32_16x16x32_bf16 v[20:23], v[148:151], v[184:187], 0\n"
    "v_mfma_f32_16x16x32_bf16 v[36:39], v[152:155], v[184:187], 0\n"
    "s_waitcnt lgkmcnt(2)\n"
    "v_mfma_f32_16x16x32_bf16 v[8:11], v[144:147], v[188:191], 0\n"
    "v_mfma_f32_16x16x32_bf16 v[24:27], v[148:151], v[188:191], 0\n"
    "v_mfma_f32_16x16x32_bf16 v[40:43], v[152:155], v[188:191], 0\n"
    "s_waitcnt lgkmcnt(0)\n"
    "v_mfma_f32_16x16x32_bf16 v[12:15], v[144:147], v[192:195], 0\n"
    "v_mfma_f32_16x16x32_bf16 v[28:31], v[148:151], v[192:195], 0\n"
    "v_mfma_f32_16x16x32_bf16 v[44:47], v[152:155], v[192:195], 0\n"
    "s_waitcnt vmcnt(1)\n"
    "s_barrier\n"
    "v_mfma_f32_16x16x32_bf16 v[48:51], v[156:159], v[180:183], 0\n"
    "ds_read_b64_tr_b16 v[144:145], v204 offset:45056\n"
    "v_mfma_f32_16x16x32_bf16 v[52:55], v[156:159], v[184:187], 0\n"
    "ds_read_b64_tr_b16 v[146:147], v204 offset:47104\n"
    "s_add_u32 m0, s51, 0\n"
    "v_mfma_f32_16x16x32_bf16 v[56:59], v[156:159], v[188:191], 0\n"
    "buffer_load_dwordx4 v215, s[24:27], s44 offen sc1 lds\n"
    "ds_read_b64_tr_b16 v[148:149], v204 offset:61440\n"
    "v_mfma_f32_16x16x32_bf16 v[60:63], v[156:159], v[192:195], 0\n"
    "s_add_u32 s44, s44, 4096\n"
    "ds_read_b64_tr_b16 v[150:151], v204 offset:63488\n"
    "v_mfma_f32_16x16x32_bf16 v[64:67], v[160:163], v[180:183], 0\n"
    "ds_read_b64_tr_b16 v[152:153], v212 offset:45056\n"
    "v_mfma_f32_16x16x32_bf16 v[68:71], v[160:163], v[184:187], 0\n"
    "ds_read_b64_tr_b16 v[154:155], v212 offset:47104\n"
    "v_mfma_f32_16x16x32_bf16 v[72:75], v[160:163], v[188:191], 0\n"
    "ds_read_b64_tr_b16 v[156:157], v205 offset:45056\n"
    "s_cbranch_vccnz Lh46_%=\n"
    "buffer_store_dwordx4 v[180:183], v219, s[32:35], s76 offen offset:256\n"
    "Lh46_%=:\n"
    "v_mfma_f32_16x16x32_bf16 v[76:79], v[160:163], v[192:195], 0\n"
    "ds_read_b64_tr_b16 v[158:159], v205 offset:47104\n"
    "s_add_u32 m0, s49, 0\n"
    "v_mfma_f32_16x16x32_bf16 v[80:83], v[164:167], v[180:183], 0\n"
    "buffer_load_dwordx4 v213, s[20:23], s40 offen lds\n"
    "ds_read_b64_tr_b16 v[160:161], v205 offset:61440\n"
    "v_mfma_f32_16x16x32_bf16 v[84:87], v[164:167], v[184:187], 0\n"
    "ds_read_b64_tr_b16 v[162:163], v205 offset:63488\n"
    "v_mfma_f32_16x16x32_bf16 v[88:91], v[164:167], v[188:191], 0\n"
    "v_add_u32 v204, 90112, v204\n"
    "v_mfma_f32_16x16x32_bf16 v[92:95], v[164:167], v[192:195], 0\n"
    "s_cmp_lg_u32 s82, 2\n"
    "s_cbranch_scc1 Lcp47_%=\n"
    "s_lshl_b32 s56, s83, 4\n"
    "v_mov_b32 v220, s56\n"
    "v_mov_b32 v221, 0\n"
    "s_mov_b64 exec, 1\n"
    "buffer_store_dword v220, v221, s[36:39], s79 offen sc1\n"
    "s_mov_b64 exec, -1\n"
    "Lcp47_%=:\n"
    "v_add_u32 v212, 90112, v212\n"
    "v_add_u32 v205, 90112, v205\n"
    "v_mfma_f32_16x16x32_bf16 v[96:99], v[168:171], v[180:183], 0\n"
    "ds_read_b64_tr_b16 v[164:165], v206 offset:45056\n"
    "v_mfma_f32_16x16x32_bf16 v[112:115], v[172:175], v[180:183], 0\n"
    "ds_read_b64_tr_b16 v[166:167], v206 offset:47104\n"
    "v_mfma_f32_16x16x32_bf16 v[128:131], v[176:179], v[180:183], 0\n"
    "buffer_load_dwordx4 v213, s[20:23], s41 offen offset:1024 lds\n"
    "v_mfma_f32_16x16x32_bf16 v[100:103], v[168:171], v[184:187], 0\n"
    "ds_read_b64_tr_b16 v[180:181], v208 offset:45056\n"
    "v_mfma_f32_16x16x32_bf16 v[116:119], v[172:175], v[184:187], 0\n"
    "ds_read_b64_tr_b16 v[182:183], v208 offset:47104\n"
    "v_mfma_f32_16x16x32_bf16 v[132:135], v[176:179], v[184:187], 0\n"
    "v_add_u32 v208, 90112, v208\n"
    "v_mfma_f32_16x16x32_bf16 v[104:107], v[168:171], v[188:191], 0\n"
    "ds_read_b64_tr_b16 v[184:185], v209 offset:45056\n"
    "v_mfma_f32_16x16x32_bf16 v[120:123], v[172:175], v[188:191], 0\n"
    "ds_read_b64_tr_b16 v[186:187], v209 offset:47104\n"
    "v_mfma_f32_16x16x32_bf16 v[136:139], v[176:179], v[188:191], 0\n"
    "buffer_load_dwordx4 v213, s[20:23], s42 offen offset:2048 lds\n"
    "v_mfma_f32_16x16x32_bf16 v[108:111], v[168:171], v[192:195], 0\n"
    "ds_read_b64_tr_b16 v[188:189], v210 offset:45056\n"
    "v_mfma_f32_16x16x32_bf16 v[124:127], v[172:175], v[192:195], 0\n"
    "ds_read_b64_tr_b16 v[190:191], v210 offset:47104\n"
    "v_mfma_f32_16x16x32_bf16 v[140:143], v[176:179], v[192:195], 0\n"
    "v_add_u32 v209, 90112, v209\n"
    "v_add_u32 v210, 90112, v210\n"
    "s_waitcnt lgkmcnt(4)\n"
    "v_mfma_f32_16x16x32_bf16 v[0:3], v[144:147], v[180:183], v[0:3]\n"
    "ds_read_b64_tr_b16 v[192:193], v211 offset:45056\n"
    "v_mfma_f32_16x16x32_bf16 v[16:19], v[148:151], v[180:183], v[16:19]\n"
    "ds_read_b64_tr_b16 v[194:195], v211 offset:47104\n"
    "v_mfma_f32_16x16x32_bf16 v[32:35], v[152:155], v[180:183], v[32:35]\n"
    "buffer_load_dwordx4 v213, s[20:23], s43 offen offset:3072 lds\n"
    "ds_read_b64_tr_b16 v[168:169], v206 offset:61440\n"
    "v_mfma_f32_16x16x32_bf16 v[196:199], v[200:203], v[180:183], v[196:199]\n"
    "ds_read_b64_tr_b16 v[170:171], v206 offset:63488\n"
    "s_waitcnt lgkmcnt(6)\n"
    "v_mfma_f32_16x16x32_bf16 v[4:7], v[144:147], v[184:187], v[4:7]\n"
    "ds_read_b64_tr_b16 v[172:173], v207 offset:45056\n"
    "v_mfma_f32_16x16x32_bf16 v[20:23], v[148:151], v[184:187], v[20:23]\n"
    "ds_read_b64_tr_b16 v[174:175], v207 offset:47104\n"
    "v_mfma_f32_16x16x32_bf16 v[36:39], v[152:155], v[184:187], v[36:39]\n"
    "ds_read_b64_tr_b16 v[176:177], v207 offset:61440\n"
    "s_waitcnt lgkmcnt(7)\n"
    "v_mfma_f32_16x16x32_bf16 v[8:11], v[144:147], v[188:191], v[8:11]\n"
    "ds_read_b64_tr_b16 v[178:179], v207 offset:63488\n"
    "s_add_u32 m0, s50, 0\n"
    "s_cmp_eq_u32 s53, 0\n"
    "v_mfma_f32_16x16x32_bf16 v[24:27], v[148:151], v[188:191], v[24:27]\n"
    "s_cbranch_scc1 Lm48_%=\n"
    "buffer_load_dwordx4 v214, s[20:23], s40 offen lds\n"
    "Lm48_%=:\n"
    "v_mfma_f32_16x16x32_bf16 v[40:43], v[152:155], v[188:191], v[40:43]\n"
    "s_add_u32 s20, s20, 294912\n"
    "s_addc_u32 s21, s21, 0\n"
    "v_add_u32 v206, 90112, v206\n"
    "s_waitcnt lgkmcnt(6)\n"
    "v_mfma_f32_16x16x32_bf16 v[12:15], v[144:147], v[192:195], v[12:15]\n"
    "v_add_u32 v207, 90112, v207\n"
    "v_mfma_f32_16x16x32_bf16 v[28:31], v[148:151], v[192:195], v[28:31]\n"
    "v_add_u32 v211, 90112, v211\n"
    "v_mfma_f32_16x16x32_bf16 v[44:47], v[152:155], v[192:195], v[44:47]\n"
    "s_waitcnt vmcnt(5) lgkmcnt(0)\n"
    "s_barrier\n"
    "v_mfma_f32_16x16x32_bf16 v[48:51], v[156:159], v[180:183], v[48:51]\n"
    "ds_read_b64_tr_b16 v[144:145], v204 offset:0\n"
    "v_mfma_f32_16x16x32_bf16 v[52:55], v[156:159], v[184:187], v[52:55]\n"
    "ds_read_b64_tr_b16 v[146:147], v204 offset:2048\n"
    "s_add_u32 m0, s51, 45056\n"
    "v_mfma_f32_16x16x32_bf16 v[56:59], v[156:159], v[188:191], v[56:59]\n"
    "buffer_load_dwordx4 v215, s[24:27], s44 offen sc1 lds\n"
    "ds_read_b64_tr_b16 v[148:149], v204 offset:16384\n"
    "v_mfma_f32_16x16x32_bf16 v[60:63], v[156:159], v[192:195], v[60:63]\n"
    "s_add_u32 s44, s44, 4096\n"
    "ds_read_b64_tr_b16 v[150:151], v204 offset:18432\n"
    "v_mfma_f32_16x16x32_bf16 v[64:67], v[160:163], v[180:183], v[64:67]\n"
    "ds_read_b64_tr_b16 v[152:153], v212 offset:0\n"
    "v_mfma_f32_16x16x32_bf16 v[68:71], v[160:163], v[184:187], v[68:71]\n"
    "ds_read_b64_tr_b16 v[154:155], v212 offset:2048\n"
    "v_mfma_f32_16x16x32_bf16 v[72:75], v[160:163], v[188:191], v[72:75]\n"
    "ds_read_b64_tr_b16 v[156:157], v205 offset:0\n"
    "s_cbranch_vccnz Lh49_%=\n"
    "buffer_store_dwordx4 v[180:183], v219, s[32:35], s76 offen offset:512\n"
    "Lh49_%=:\n"
    "v_mfma_f32_16x16x32_bf16 v[76:79], v[160:163], v[192:195], v[76:79]\n"
    "ds_read_b64_tr_b16 v[158:159], v205 offset:2048\n"
    "s_add_u32 m0, s49, 45056\n"
    "v_mfma_f32_16x16x32_bf16 v[80:83], v[164:167], v[180:183], v[80:83]\n"
    "buffer_load_dwordx4 v213, s[20:23], s40 offen lds\n"
    "ds_read_b64_tr_b16 v[160:161], v205 offset:16384\n"
    "v_mfma_f32_16x16x32_bf16 v[84:87], v[164:167], v[184:187], v[84:87]\n"
    "ds_read_b64_tr_b16 v[162:163], v205 offset:18432\n"
    "v_mfma_f32_16x16x32_bf16 v[88:91], v[164:167], v[188:191], v[88:91]\n"
    "v_subrev_u32 v204, 90112, v204\n"
    "v_mfma_f32_16x16x32_bf16 v[92:95], v[164:167], v[192:195], v[92:95]\n"
    "v_subrev_u32 v212, 90112, v212\n"
    "v_subrev_u32 v205, 90112, v205\n"
    "v_mfma_f32_16x16x32_bf16 v[96:99], v[168:171], v[180:183], v[96:99]\n"
    "ds_read_b64_tr_b16 v[164:165], v206 offset:0\n"
    "v_mfma_f32_16x16x32_bf16 v[112:115], v[172:175], v[180:183], v[112:115]\n"
    "ds_read_b64_tr_b16 v[166:167], v206 offset:2048\n"
    "v_mfma_f32_16x16x32_bf16 v[128:131], v[176:179], v[180:183], v[128:131]\n"
    "buffer_load_dwordx4 v213, s[20:23], s41 offen offset:1024 lds\n"
    "v_mfma_f32_16x16x32_bf16 v[100:103], v[168:171], v[184:187], v[100:103]\n"
    "ds_read_b64_tr_b16 v[180:181], v208 offset:0\n"
    "v_mfma_f32_16x16x32_bf16 v[116:119], v[172:175], v[184:187], v[116:119]\n"
    "ds_read_b64_tr_b16 v[182:183], v208 offset:2048\n"
    "v_mfma_f32_16x16x32_bf16 v[132:135], v[176:179], v[184:187], v[132:135]\n"
    "v_subrev_u32 v208, 90112, v208\n"
    "v_mfma_f32_16x16x32_bf16 v[104:107], v[168:171], v[188:191], v[104:107]\n"
    "ds_read_b64_tr_b16 v[184:185], v209 offset:0\n"
    "v_mfma_f32_16x16x32_bf16 v[120:123], v[172:175], v[188:191], v[120:123]\n"
    "ds_read_b64_tr_b16 v[186:187], v209 offset:2048\n"
    "v_mfma_f32_16x16x32_bf16 v[136:139], v[176:179], v[188:191], v[136:139]\n"
    "buffer_load_dwordx4 v213, s[20:23], s42 offen offset:2048 lds\n"
    "v_mfma_f32_16x16x32_bf16 v[108:111], v[168:171], v[192:195], v[108:111]\n"
    "ds_read_b64_tr_b16 v[188:189], v210 offset:0\n"
    "v_mfma_f32_16x16x32_bf16 v[124:127], v[172:175], v[192:195], v[124:127]\n"
    "ds_read_b64_tr_b16 v[190:191], v210 offset:2048\n"
    "v_mfma_f32_16x16x32_bf16 v[140:143], v[176:179], v[192:195], v[140:143]\n"
    "v_subrev_u32 v209, 90112, v209\n"
    "v_subrev_u32 v210, 90112, v210\n"
    "s_waitcnt lgkmcnt(4)\n"
    "v_mfma_f32_16x16x32_bf16 v[0:3], v[144:147], v[180:183], v[0:3]\n"
    "ds_read_b64_tr_b16 v[192:193], v211 offset:0\n"
    "v_mfma_f32_16x16x32_bf16 v[16:19], v[148:151], v[180:183], v[16:19]\n"
    "ds_read_b64_tr_b16 v[194:195], v211 offset:2048\n"
    "v_mfma_f32_16x16x32_bf16 v[32:35], v[152:155], v[180:183], v[32:35]\n"
    "buffer_load_dwordx4 v213, s[20:23], s43 offen offset:3072 lds\n"
    "ds_read_b64_tr_b16 v[168:169], v206 offset:16384\n"
    "v_mfma_f32_16x16x32_bf16 v[196:199], v[200:203], v[180:183], v[196:199]\n"
    "ds_read_b64_tr_b16 v[170:171], v206 offset:18432\n"
    "s_waitcnt lgkmcnt(6)\n"
    "v_mfma_f32_16x16x32_bf16 v[4:7], v[144:147], v[184:187], v[4:7]\n"
    "ds_read_b64_tr_b16 v[172:173], v207 offset:0\n"
    "v_mfma_f32_16x16x32_bf16 v[20:23], v[148:151], v[184:187], v[20:23]\n"
    "ds_read_b64_tr_b16 v[174:175], v207 offset:2048\n"
    "v_mfma_f32_16x16x32_bf16 v[36:39], v[152:155], v[184:187], v[36:39]\n"
    "ds_read_b64_tr_b16 v[176:177], v207 offset:16384\n"
    "s_waitcnt lgkmcnt(7)\n"
    "v_mfma_f32_16x16x32_bf16 v[8:11], v[144:147], v[188:191], v[8:11]\n"
    "ds_read_b64_tr_b16 v[178:179], v207 offset:18432\n"
    "s_add_u32 m0, s50, 45056\n"
    "s_cmp_eq_u32 s53, 0\n"
    "v_mfma_f32_16x16x32_bf16 v[24:27], v[148:151], v[188:191], v[24:27]\n"
    "s_cbranch_scc1 Lm50_%=\n"
    "buffer_load_dwordx4 v214, s[20:23], s40 offen lds\n"
    "Lm50_%=:\n"
    "v_mfma_f32_16x16x32_bf16 v[40:43], v[152:155], v[188:191], v[40:43]\n"
    "s_add_u32 s20, s20, 294912\n"
    "s_addc_u32 s21, s21, 0\n"
    "v_subrev_u32 v206, 90112, v206\n"
    "s_waitcnt lgkmcnt(6)\n"
    "v_mfma_f32_16x16x32_bf16 v[12:15], v[144:147], v[192:195], v[12:15]\n"
    "v_subrev_u32 v207, 90112, v207\n"
    "v_mfma_f32_16x16x32_bf16 v[28:31], v[148:151], v[192:195], v[28:31]\n"
    "v_subrev_u32 v211, 90112, v211\n"
    "v_mfma_f32_16x16x32_bf16 v[44:47], v[152:155], v[192:195], v[44:47]\n"
    "s_waitcnt vmcnt(5) lgkmcnt(0)\n"
    "s_barrier\n"
    "v_mfma_f32_16x16x32_bf16 v[48:51], v[156:159], v[180:183], v[48:51]\n"
    "ds_read_b64_tr_b16 v[144:145], v204 offset:0\n"
    "v_mfma_f32_16x16x32_bf16 v[52:55], v[156:159], v[184:187], v[52:55]\n"
    "ds_read_b64_tr_b16 v[146:147], v204 offset:2048\n"
    "s_add_u32 m0, s51, 90112\n"
    "v_mfma_f32_16x16x32_bf16 v[56:59], v[156:159], v[188:191], v[56:59]\n"
    "buffer_load_dwordx4 v215, s[24:27], s44 offen sc1 lds\n"
    "ds_read_b64_tr_b16 v[148:149], v204 offset:16384\n"
    "v_mfma_f32_16x16x32_bf16 v[60:63], v[156:159], v[192:195], v[60:63]\n"
    "s_add_u32 s44, s44, 4096\n"
    "ds_read_b64_tr_b16 v[150:151], v204 offset:18432\n"
    "v_mfma_f32_16x16x32_bf16 v[64:67], v[160:163], v[180:183], v[64:67]\n"
    "ds_read_b64_tr_b16 v[152:153], v212 offset:0\n"
    "v_mfma_f32_16x16x32_bf16 v[68:71], v[160:163], v[184:187], v[68:71]\n"
    "ds_read_b64_tr_b16 v[154:155], v212 offset:2048\n"
    "v_mfma_f32_16x16x32_bf16 v[72:75], v[160:163], v[188:191], v[72:75]\n"
    "ds_read_b64_tr_b16 v[156:157], v205 offset:0\n"
    "s_cbranch_vccnz Lh51_%=\n"
    "buffer_store_dwordx4 v[180:183], v219, s[32:35], s76 offen offset:768\n"
    "Lh51_%=:\n"
    "v_mfma_f32_16x16x32_bf16 v[76:79], v[160:163], v[192:195], v[76:79]\n"
    "ds_read_b64_tr_b16 v[158:159], v205 offset:2048\n"
    "s_add_u32 m0, s49, 90112\n"
    "v_mfma_f32_16x16x32_bf16 v[80:83], v[164:167], v[180:183], v[80:83]\n"
    "buffer_load_dwordx4 v213, s[20:23], s40 offen lds\n"
    "ds_read_b64_tr_b16 v[160:161], v205 offset:16384\n"
    "v_mfma_f32_16x16x32_bf16 v[84:87], v[164:167], v[184:187], v[84:87]\n"
    "ds_read_b64_tr_b16 v[162:163], v205 offset:18432\n"
    "v_mfma_f32_16x16x32_bf16 v[88:91], v[164:167], v[188:191], v[88:91]\n"
    "v_mfma_f32_16x16x32_bf16 v[92:95], v[164:167], v[192:195], v[92:95]\n"
    "v_mfma_f32_16x16x32_bf16 v[96:99], v[168:171], v[180:183], v[96:99]\n"
    "ds_read_b64_tr_b16 v[164:165], v206 offset:0\n"
    "v_mfma_f32_16x16x32_bf16 v[112:115], v[172:175], v[180:183], v[112:115]\n"
    "ds_read_b64_tr_b16 v[166:167], v206 offset:2048\n"
    "v_mfma_f32_16x16x32_bf16 v[128:131], v[176:179], v[180:183], v[128:131]\n"
    "buffer_load_dwordx4 v213, s[20:23], s41 offen offset:1024 lds\n"
    "v_mfma_f32_16x16x32_bf16 v[100:103], v[168:171], v[184:187], v[100:103]\n"
    "ds_read_b64_tr_b16 v[180:181], v208 offset:0\n"
    "v_mfma_f32_16x16x32_bf16 v[116:119], v[172:175], v[184:187], v[116:119]\n"
    "ds_read_b64_tr_b16 v[182:183], v208 offset:2048\n"
    "v_mfma_f32_16x16x32_bf16 v[132:135], v[176:179], v[184:187], v[132:135]\n"
    "s_cmp_lg_u32 s80, 0\n"
    "s_cbranch_scc1 Lww52_%=\n"
    "buffer_load_dword v250, v250, s[28:31], 0 offen\n"
    "Lww52_%=:\n"
    "v_mfma_f32_16x16x32_bf16 v[104:107], v[168:171], v[188:191], v[104:107]\n"
    "ds_read_b64_tr_b16 v[184:185], v209 offset:0\n"
    "v_mfma_f32_16x16x32_bf16 v[120:123], v[172:175], v[188:191], v[120:123]\n"
    "ds_read_b64_tr_b16 v[186:187], v209 offset:2048\n"
    "v_mfma_f32_16x16x32_bf16 v[136:139], v[176:179], v[188:191], v[136:139]\n"
    "buffer_load_dwordx4 v213, s[20:23], s42 offen offset:2048 lds\n"
    "v_mfma_f32_16x16x32_bf16 v[108:111], v[168:171], v[192:195], v[108:111]\n"
    "ds_read_b64_tr_b16 v[188:189], v210 offset:0\n"
    "v_mfma_f32_16x16x32_bf16 v[124:127], v[172:175], v[192:195], v[124:127]\n"
    "ds_read_b64_tr_b16 v[190:191], v210 offset:2048\n"
    "v_mfma_f32_16x16x32_bf16 v[140:143], v[176:179], v[192:195], v[140:143]\n"
    "s_waitcnt lgkmcnt(4)\n"
    "v_mfma_f32_16x16x32_bf16 v[0:3], v[144:147], v[180:183], v[0:3]\n"
    "ds_read_b64_tr_b16 v[192:193], v211 offset:0\n"
    "v_mfma_f32_16x16x32_bf16 v[16:19], v[148:151], v[180:183], v[16:19]\n"
    "ds_read_b64_tr_b16 v[194:195], v211 offset:2048\n"
    "v_mfma_f32_16x16x32_bf16 v[32:35], v[152:155], v[180:183], v[32:35]\n"
    "buffer_load_dwordx4 v213, s[20:23], s43 offen offset:3072 lds\n"
    "ds_read_b64_tr_b16 v[168:169], v206 offset:16384\n"
    "v_mfma_f32_16x16x32_bf16 v[196:199], v[200:203], v[180:183], v[196:199]\n"
    "ds_read_b64_tr_b16 v[170:171], v206 offset:18432\n"
    "s_waitcnt lgkmcnt(6)\n"
    "v_mfma_f32_16x16x32_bf16 v[4:7], v[144:147], v[184:187], v[4:7]\n"
    "ds_read_b64_tr_b16 v[172:173], v207 offset:0\n"
    "v_mfma_f32_16x16x32_bf16 v[20:23], v[148:151], v[184:187], v[20:23]\n"
    "ds_read_b64_tr_b16 v[174:175], v207 offset:2048\n"
    "v_mfma_f32_16x16x32_bf16 v[36:39], v[152:155], v[184:187], v[36:39]\n"
    "ds_read_b64_tr_b16 v[176:177], v207 offset:16384\n"
    "s_waitcnt lgkmcnt(7)\n"
    "v_mfma_f32_16x16x32_bf16 v[8:11], v[144:147], v[188:191], v[8:11]\n"
    "ds_read_b64_tr_b16 v[178:179], v207 offset:18432\n"
    "s_add_u32 m0, s50, 90112\n"
    "s_cmp_eq_u32 s53, 0\n"
    "v_mfma_f32_16x16x32_bf16 v[24:27], v[148:151], v[188:191], v[24:27]\n"
    "s_cbranch_scc1 Lm53_%=\n"
    "buffer_load_dwordx4 v214, s[20:23], s40 offen lds\n"
    "Lm53_%=:\n"
    "v_mfma_f32_16x16x32_bf16 v[40:43], v[152:155], v[188:191], v[40:43]\n"
    "s_add_u32 s20, s20, 294912\n"
    "s_addc_u32 s21, s21, 0\n"
    "s_waitcnt lgkmcnt(6)\n"
    "v_mfma_f32_16x16x32_bf16 v[12:15], v[144:147], v[192:195], v[12:15]\n"
    "v_mfma_f32_16x16x32_bf16 v[28:31], v[148:151], v[192:195], v[28:31]\n"
    "v_mfma_f32_16x16x32_bf16 v[44:47], v[152:155], v[192:195], v[44:47]\n"
    "s_waitcnt vmcnt(5) lgkmcnt(0)\n"
    "s_barrier\n"
    "v_mfma_f32_16x16x32_bf16 v[48:51], v[156:159], v[180:183], v[48:51]\n"
    "ds_read_b64_tr_b16 v[144:145], v204 offset:45056\n"
    "v_mfma_f32_16x16x32_bf16 v[52:55], v[156:159], v[184:187], v[52:55]\n"
    "ds_read_b64_tr_b16 v[146:147], v204 offset:47104\n"
    "s_add_u32 m0, s51, 0\n"
    "v_mfma_f32_16x16x32_bf16 v[56:59], v[156:159], v[188:191], v[56:59]\n"
    "buffer_load_dwordx4 v215, s[24:27], s44 offen sc1 lds\n"
    "ds_read_b64_tr_b16 v[148:149], v204 offset:61440\n"
    "v_mfma_f32_16x16x32_bf16 v[60:63], v[156:159], v[192:195], v[60:63]\n"
    "s_add_u32 s44, s44, 4096\n"
    "ds_read_b64_tr_b16 v[150:151], v204 offset:63488\n"
    "v_mfma_f32_16x16x32_bf16 v[64:67], v[160:163], v[180:183], v[64:67]\n"
    "ds_read_b64_tr_b16 v[152:153], v212 offset:45056\n"
    "v_mfma_f32_16x16x32_bf16 v[68:71], v[160:163], v[184:187], v[68:71]\n"
    "ds_read_b64_tr_b16 v[154:155], v212 offset:47104\n"
    "v_mfma_f32_16x16x32_bf16 v[72:75], v[160:163], v[188:191], v[72:75]\n"
    "ds_read_b64_tr_b16 v[156:157], v205 offset:45056\n"
    "s_cbranch_vccnz Lh54_%=\n"
    "buffer_store_dwordx4 v[180:183], v219, s[32:35], s76 offen offset:1024\n"
    "Lh54_%=:\n"
    "v_mfma_f32_16x16x32_bf16 v[76:79], v[160:163], v[192:195], v[76:79]\n"
    "ds_read_b64_tr_b16 v[158:159], v205 offset:47104\n"
    "s_add_u32 m0, s49, 0\n"
    "v_mfma_f32_16x16x32_bf16 v[80:83], v[164:167], v[180:183], v[80:83]\n"
    "buffer_load_dwordx4 v213, s[20:23], s40 offen lds\n"
    "ds_read_b64_tr_b16 v[160:161], v205 offset:61440\n"
    "v_mfma_f32_16x16x32_bf16 v[84:87], v[164:167], v[184:187], v[84:87]\n"
    "ds_read_b64_tr_b16 v[162:163], v205 offset:63488\n"
    "v_mfma_f32_16x16x32_bf16 v[88:91], v[164:167], v[188:191], v[88:91]\n"
    "v_add_u32 v204, 90112, v204\n"
    "v_mfma_f32_16x16x32_bf16 v[92:95], v[164:167], v[192:195], v[92:95]\n"
    "v_add_u32 v212, 90112, v212\n"
    "v_add_u32 v205, 90112, v205\n"
    "v_mfma_f32_16x16x32_bf16 v[96:99], v[168:171], v[180:183], v[96:99]\n"
    "ds_read_b64_tr_b16 v[164:165], v206 offset:45056\n"
    "v_mfma_f32_16x16x32_bf16 v[112:115], v[172:175], v[180:183], v[112:115]\n"
    "ds_read_b64_tr_b16 v[166:167], v206 offset:47104\n"
    "v_mfma_f32_16x16x32_bf16 v[128:131], v[176:179], v[180:183], v[128:131]\n"
    "buffer_load_dwordx4 v213, s[20:23], s41 offen offset:1024 lds\n"
    "v_mfma_f32_16x16x32_bf16 v[100:103], v[168:171], v[184:187], v[100:103]\n"
    "ds_read_b64_tr_b16 v[180:181], v208 offset:45056\n"
    "v_mfma_f32_16x16x32_bf16 v[116:119], v[172:175], v[184:187], v[116:119]\n"
    "ds_read_b64_tr_b16 v[182:183], v208 offset:47104\n"
    "v_mfma_f32_16x16x32_bf16 v[132:135], v[176:179], v[184:187], v[132:135]\n"
    "s_cmp_lg_u32 s80, 1\n"
    "s_cbranch_scc1 Lww55_%=\n"
    "buffer_load_dword v250, v250, s[28:31], 0 offen\n"
    "Lww55_%=:\n"
    "v_add_u32 v208, 90112, v208\n"
    "v_mfma_f32_16x16x32_bf16 v[104:107], v[168:171], v[188:191], v[104:107]\n"
    "ds_read_b64_tr_b16 v[184:185], v209 offset:45056\n"
    "v_mfma_f32_16x16x32_bf16 v[120:123], v[172:175], v[188:191], v[120:123]\n"
    "ds_read_b64_tr_b16 v[186:187], v209 offset:47104\n"
    "v_mfma_f32_16x16x32_bf16 v[136:139], v[176:179], v[188:191], v[136:139]\n"
    "buffer_load_dwordx4 v213, s[20:23], s42 offen offset:2048 lds\n"
    "v_mfma_f32_16x16x32_bf16 v[108:111], v[168:171], v[192:195], v[108:111]\n"
    "ds_read_b64_tr_b16 v[188:189], v210 offset:45056\n"
    "v_mfma_f32_16x16x32_bf16 v[124:127], v[172:175], v[192:195], v[124:127]\n"
    "ds_read_b64_tr_b16 v[190:191], v210 offset:47104\n"
    "v_mfma_f32_16x16x32_bf16 v[140:143], v[176:179], v[192:195], v[140:143]\n"
    "v_add_u32 v209, 90112, v209\n"
    "v_add_u32 v210, 90112, v210\n"
    "s_waitcnt lgkmcnt(4)\n"
    "v_mfma_f32_16x16x32_bf16 v[0:3], v[144:147], v[180:183], v[0:3]\n"
    "ds_read_b64_tr_b16 v[192:193], v211 offset:45056\n"
    "v_mfma_f32_16x16x32_bf16 v[16:19], v[148:151], v[180:183], v[16:19]\n"
    "ds_read_b64_tr_b16 v[194:195], v211 offset:47104\n"
    "v_mfma_f32_16x16x32_bf16 v[32:35], v[152:155], v[180:183], v[32:35]\n"
    "buffer_load_dwordx4 v213, s[20:23], s43 offen offset:3072 lds\n"
    "ds_read_b64_tr_b16 v[168:169], v206 offset:61440\n"
    "v_mfma_f32_16x16x32_bf16 v[196:199], v[200:203], v[180:183], v[196:199]\n"
    "ds_read_b64_tr_b16 v[170:171], v206 offset:63488\n"
    "s_waitcnt lgkmcnt(6)\n"
    "v_mfma_f32_16x16x32_bf16 v[4:7], v[144:147], v[184:187], v[4:7]\n"
    "ds_read_b64_tr_b16 v[172:173], v207 offset:45056\n"
    "v_mfma_f32_16x16x32_bf16 v[20:23], v[148:151], v[184:187], v[20:23]\n"
    "ds_read_b64_tr_b16 v[174:175], v207 offset:47104\n"
    "v_mfma_f32_16x16x32_bf16 v[36:39], v[152:155], v[184:187], v[36:39]\n"
    "ds_read_b64_tr_b16 v[176:177], v207 offset:61440\n"
    "s_waitcnt lgkmcnt(7)\n"
    "v_mfma_f32_16x16x32_bf16 v[8:11], v[144:147], v[188:191], v[8:11]\n"
    "ds_read_b64_tr_b16 v[178:179], v207 offset:63488\n"
    "s_add_u32 m0, s50, 0\n"
    "s_cmp_eq_u32 s53, 0\n"
    "v_mfma_f32_16x16x32_bf16 v[24:27], v[148:151], v[188:191], v[24:27]\n"
    "s_cbranch_scc1 Lm56_%=\n"
    "buffer_load_dwordx4 v214, s[20:23], s40 offen lds\n"
    "Lm56_%=:\n"
    "v_mfma_f32_16x16x32_bf16 v[40:43], v[152:155], v[188:191], v[40:43]\n"
    "s_add_u32 s20, s20, 294912\n"
    "s_addc_u32 s21, s21, 0\n"
    "v_add_u32 v206, 90112, v206\n"
    "s_waitcnt lgkmcnt(6)\n"
    "v_mfma_f32_16x16x32_bf16 v[12:15], v[144:147], v[192:195], v[12:15]\n"
    "v_add_u32 v207, 90112, v207\n"
    "v_mfma_f32_16x16x32_bf16 v[28:31], v[148:151], v[192:195], v[28:31]\n"
    "v_add_u32 v211, 90112, v211\n"
    "v_mfma_f32_16x16x32_bf16 v[44:47], v[152:155], v[192:195], v[44:47]\n"
    "s_waitcnt vmcnt(5) lgkmcnt(0)\n"
    "s_barrier\n"
    "v_mfma_f32_16x16x32_bf16 v[48:51], v[156:159], v[180:183], v[48:51]\n"
    "ds_read_b64_tr_b16 v[144:145], v204 offset:0\n"
    "v_mfma_f32_16x16x32_bf16 v[52:55], v[156:159], v[184:187], v[52:55]\n"
    "ds_read_b64_tr_b16 v[146:147], v204 offset:2048\n"
    "s_add_u32 m0, s51, 45056\n"
    "v_mfma_f32_16x16x32_bf16 v[56:59], v[156:159], v[188:191], v[56:59]\n"
    "buffer_load_dwordx4 v215, s[24:27], s44 offen sc1 lds\n"
    "ds_read_b64_tr_b16 v[148:149], v204 offset:16384\n"
    "v_mfma_f32_16x16x32_bf16 v[60:63], v[156:159], v[192:195], v[60:63]\n"
    "s_add_u32 s44, s44, 4096\n"
    "ds_read_b64_tr_b16 v[150:151], v204 offset:18432\n"
    "v_mfma_f32_16x16x32_bf16 v[64:67], v[160:163], v[180:183], v[64:67]\n"
    "ds_read_b64_tr_b16 v[152:153], v212 offset:0\n"
    "v_mfma_f32_16x16x32_bf16 v[68:71], v[160:163], v[184:187], v[68:71]\n"
    "ds_read_b64_tr_b16 v[154:155], v212 offset:2048\n"
    "v_mfma_f32_16x16x32_bf16 v[72:75], v[160:163], v[188:191], v[72:75]\n"
    "ds_read_b64_tr_b16 v[156:157], v205 offset:0\n"
    "s_cbranch_vccnz Lh57_%=\n"
    "buffer_store_dwordx4 v[180:183], v219, s[32:35], s76 offen offset:1280\n"
    "Lh57_%=:\n"
    "v_mfma_f32_16x16x32_bf16 v[76:79], v[160:163], v[192:195], v[76:79]\n"
    "ds_read_b64_tr_b16 v[158:159], v205 offset:2048\n"
    "s_add_u32 m0, s49, 45056\n"
    "v_mfma_f32_16x16x32_bf16 v[80:83], v[164:167], v[180:183], v[80:83]\n"
    "buffer_load_dwordx4 v213, s[20:23], s40 offen lds\n"
    "ds_read_b64_tr_b16 v[160:161], v205 offset:16384\n"
    "v_mfma_f32_16x16x32_bf16 v[84:87], v[164:167], v[184:187], v[84:87]\n"
    "ds_read_b64_tr_b16 v[162:163], v205 offset:18432\n"
    "v_mfma_f32_16x16x32_bf16 v[88:91], v[164:167], v[188:191], v[88:91]\n"
    "v_subrev_u32 v204, 90112, v204\n"
    "v_mfma_f32_16x16x32_bf16 v[92:95], v[164:167], v[192:195], v[92:95]\n"
    "v_subrev_u32 v212, 90112, v212\n"
    "v_subrev_u32 v205, 90112, v205\n"
    "v_mfma_f32_16x16x32_bf16 v[96:99], v[168:171], v[180:183], v[96:99]\n"
    "ds_read_b64_tr_b16 v[164:165], v206 offset:0\n"
    "v_mfma_f32_16x16x32_bf16 v[112:115], v[172:175], v[180:183], v[112:115]\n"
    "ds_read_b64_tr_b16 v[166:167], v206 offset:2048\n"
    "v_mfma_f32_16x16x32_bf16 v[128:131], v[176:179], v[180:183], v[128:131]\n"
    "buffer_load_dwordx4 v213, s[20:23], s41 offen offset:1024 lds\n"
    "v_mfma_f32_16x16x32_bf16 v[100:103], v[168:171], v[184:187], v[100:103]\n"
    "ds_read_b64_tr_b16 v[180:181], v208 offset:0\n"
    "v_mfma_f32_16x16x32_bf16 v[116:119], v[172:175], v[184:187], v[116:119]\n"
    "ds_read_b64_tr_b16 v[182:183], v208 offset:2048\n"
    "v_mfma_f32_16x16x32_bf16 v[132:135], v[176:179], v[184:187], v[132:135]\n"
    "s_cmp_lg_u32 s80, 2\n"
    "s_cbranch_scc1 Lww58_%=\n"
    "buffer_load_dword v250, v250, s[28:31], 0 offen\n"
    "Lww58_%=:\n"
    "v_subrev_u32 v208, 90112, v208\n"
    "v_mfma_f32_16x16x32_bf16 v[104:107], v[168:171], v[188:191], v[104:107]\n"
    "ds_read_b64_tr_b16 v[184:185], v209 offset:0\n"
    "v_mfma_f32_16x16x32_bf16 v[120:123], v[172:175], v[188:191], v[120:123]\n"
    "ds_read_b64_tr_b16 v[186:187], v209 offset:2048\n"
    "v_mfma_f32_16x16x32_bf16 v[136:139], v[176:179], v[188:191], v[136:139]\n"
    "buffer_load_dwordx4 v213, s[20:23], s42 offen offset:2048 lds\n"
    "v_mfma_f32_16x16x32_bf16 v[108:111], v[168:171], v[192:195], v[108:111]\n"
    "ds_read_b64_tr_b16 v[188:189], v210 offset:0\n"
    "v_mfma_f32_16x16x32_bf16 v[124:127], v[172:175], v[192:195], v[124:127]\n"
    "ds_read_b64_tr_b16 v[190:191], v210 offset:2048\n"
    "v_mfma_f32_16x16x32_bf16 v[140:143], v[176:179], v[192:195], v[140:143]\n"
    "v_subrev_u32 v209, 90112, v209\n"
    "v_subrev_u32 v210, 90112, v210\n"
    "s_waitcnt lgkmcnt(4)\n"
    "v_mfma_f32_16x16x32_bf16 v[0:3], v[144:147], v[180:183], v[0:3]\n"
    "ds_read_b64_tr_b16 v[192:193], v211 offset:0\n"
    "v_mfma_f32_16x16x32_bf16 v[16:19], v[148:151], v[180:183], v[16:19]\n"
    "ds_read_b64_tr_b16 v[194:195], v211 offset:2048\n"
    "v_mfma_f32_16x16x32_bf16 v[32:35], v[152:155], v[180:183], v[32:35]\n"
    "buffer_load_dwordx4 v213, s[20:23], s43 offen offset:3072 lds\n"
    "ds_read_b64_tr_b16 v[168:169], v206 offset:16384\n"
    "v_mfma_f32_16x16x32_bf16 v[196:199], v[200:203], v[180:183], v[196:199]\n"
    "ds_read_b64_tr_b16 v[170:171], v206 offset:18432\n"
    "s_waitcnt lgkmcnt(6)\n"
    "v_mfma_f32_16x16x32_bf16 v[4:7], v[144:147], v[184:187], v[4:7]\n"
    "ds_read_b64_tr_b16 v[172:173], v207 offset:0\n"
    "v_mfma_f32_16x16x32_bf16 v[20:23], v[148:151], v[184:187], v[20:23]\n"
    "ds_read_b64_tr_b16 v[174:175], v207 offset:2048\n"
    "v_mfma_f32_16x16x32_bf16 v[36:39], v[152:155], v[184:187], v[36:39]\n"
    "ds_read_b64_tr_b16 v[176:177], v207 offset:16384\n"
    "s_waitcnt lgkmcnt(7)\n"
    "v_mfma_f32_16x16x32_bf16 v[8:11], v[144:147], v[188:191], v[8:11]\n"
    "ds_read_b64_tr_b16 v[178:179], v207 offset:18432\n"
    "s_add_u32 m0, s50, 45056\n"
    "s_cmp_eq_u32 s53, 0\n"
    "v_mfma_f32_16x16x32_bf16 v[24:27], v[148:151], v[188:191], v[24:27]\n"
    "s_cbranch_scc1 Lm59_%=\n"
    "buffer_load_dwordx4 v214, s[20:23], s40 offen lds\n"
    "Lm59_%=:\n"
    "v_mfma_f32_16x16x32_bf16 v[40:43], v[152:155], v[188:191], v[40:43]\n"
    "s_add_u32 s20, s20, 294912\n"
    "s_addc_u32 s21, s21, 0\n"
    "v_subrev_u32 v206, 90112, v206\n"
    "s_waitcnt lgkmcnt(6)\n"
    "v_mfma_f32_16x16x32_bf16 v[12:15], v[144:147], v[192:195], v[12:15]\n"
    "v_subrev_u32 v207, 90112, v207\n"
    "v_mfma_f32_16x16x32_bf16 v[28:31], v[148:151], v[192:195], v[28:31]\n"
    "v_subrev_u32 v211, 90112, v211\n"
    "v_mfma_f32_16x16x32_bf16 v[44:47], v[152:155], v[192:195], v[44:47]\n"
    "s_waitcnt vmcnt(5) lgkmcnt(0)\n"
    "s_barrier\n"
    "v_mfma_f32_16x16x32_bf16 v[48:51], v[156:159], v[180:183], v[48:51]\n"
    "ds_read_b64_tr_b16 v[144:145], v204 offset:0\n"
    "v_mfma_f32_16x16x32_bf16 v[52:55], v[156:159], v[184:187], v[52:55]\n"
    "ds_read_b64_tr_b16 v[146:147], v204 offset:2048\n"
    "s_add_u32 m0, s51, 90112\n"
    "v_mfma_f32_16x16x32_bf16 v[56:59], v[156:159], v[188:191], v[56:59]\n"
    "buffer_load_dwordx4 v215, s[24:27], s44 offen sc1 lds\n"
    "ds_read_b64_tr_b16 v[148:149], v204 offset:16384\n"
    "v_mfma_f32_16x16x32_bf16 v[60:63], v[156:159], v[192:195], v[60:63]\n"
    "s_add_u32 s44, s44, 4096\n"
    "ds_read_b64_tr_b16 v[150:151], v204 offset:18432\n"
    "v_mfma_f32_16x16x32_bf16 v[64:67], v[160:163], v[180:183], v[64:67]\n"
    "ds_read_b64_tr_b16 v[152:153], v212 offset:0\n"
    "v_mfma_f32_16x16x32_bf16 v[68:71], v[160:163], v[184:187], v[68:71]\n"
    "ds_read_b64_tr_b16 v[154:155], v212 offset:2048\n"
    "v_mfma_f32_16x16x32_bf16 v[72:75], v[160:163], v[188:191], v[72:75]\n"
    "ds_read_b64_tr_b16 v[156:157], v205 offset:0\n"
    "s_cbranch_vccnz Lh60_%=\n"
    "buffer_store_dwordx4 v[180:183], v219, s[32:35], s76 offen offset:1536\n"
    "Lh60_%=:\n"
    "v_mfma_f32_16x16x32_bf16 v[76:79], v[160:163], v[192:195], v[76:79]\n"
    "ds_read_b64_tr_b16 v[158:159], v205 offset:2048\n"
    "s_add_u32 m0, s49, 90112\n"
    "v_mfma_f32_16x16x32_bf16 v[80:83], v[164:167], v[180:183], v[80:83]\n"
    "buffer_load_dwordx4 v213, s[20:23], s40 offen lds\n"
    "ds_read_b64_tr_b16 v[160:161], v205 offset:16384\n"
    "v_mfma_f32_16x16x32_bf16 v[84:87], v[164:167], v[184:187], v[84:87]\n"
    "ds_read_b64_tr_b16 v[162:163], v205 offset:18432\n"
    "v_mfma_f32_16x16x32_bf16 v[88:91], v[164:167], v[188:191], v[88:91]\n"
    "v_mfma_f32_16x16x32_bf16 v[92:95], v[164:167], v[192:195], v[92:95]\n"
    "v_mfma_f32_16x16x32_bf16 v[96:99], v[168:171], v[180:183], v[96:99]\n"
    "ds_read_b64_tr_b16 v[164:165], v206 offset:0\n"
    "v_mfma_f32_16x16x32_bf16 v[112:115], v[172:175], v[180:183], v[112:115]\n"
    "ds_read_b64_tr_b16 v[166:167], v206 offset:2048\n"
    "v_mfma_f32_16x16x32_bf16 v[128:131], v[176:179], v[180:183], v[128:131]\n"
    "buffer_load_dwordx4 v213, s[20:23], s41 offen offset:1024 lds\n"
    "v_mfma_f32_16x16x32_bf16 v[100:103], v[168:171], v[184:187], v[100:103]\n"
    "ds_read_b64_tr_b16 v[180:181], v208 offset:0\n"
    "v_mfma_f32_16x16x32_bf16 v[116:119], v[172:175], v[184:187], v[116:119]\n"
    "ds_read_b64_tr_b16 v[182:183], v208 offset:2048\n"
    "v_mfma_f32_16x16x32_bf16 v[132:135], v[176:179], v[184:187], v[132:135]\n"
    "s_cmp_lg_u32 s80, 3\n"
    "s_cbranch_scc1 Lww61_%=\n"
    "buffer_load_dword v250, v250, s[28:31], 0 offen\n"
    "Lww61_%=:\n"
    "v_mfma_f32_16x16x32_bf16 v[104:107], v[168:171], v[188:191], v[104:107]\n"
    "ds_read_b64_tr_b16 v[184:185], v209 offset:0\n"
    "v_mfma_f32_16x16x32_bf16 v[120:123], v[172:175], v[188:191], v[120:123]\n"
    "ds_read_b64_tr_b16 v[186:187], v209 offset:2048\n"
    "v_mfma_f32_16x16x32_bf16 v[136:139], v[176:179], v[188:191], v[136:139]\n"
    "buffer_load_dwordx4 v213, s[20:23], s42 offen offset:2048 lds\n"
    "v_mfma_f32_16x16x32_bf16 v[108:111], v[168:171], v[192:195], v[108:111]\n"
    "ds_read_b64_tr_b16 v[188:189], v210 offset:0\n"
    "v_mfma_f32_16x16x32_bf16 v[124:127], v[172:175], v[192:195], v[124:127]\n"
    "ds_read_b64_tr_b16 v[190:191], v210 offset:2048\n"
    "v_mfma_f32_16x16x32_bf16 v[140:143], v[176:179], v[192:195], v[140:143]\n"
    "s_waitcnt lgkmcnt(4)\n"
    "v_mfma_f32_16x16x32_bf16 v[0:3], v[144:147], v[180:183], v[0:3]\n"
    "ds_read_b64_tr_b16 v[192:193], v211 offset:0\n"
    "v_mfma_f32_16x16x32_bf16 v[16:19], v[148:151], v[180:183], v[16:19]\n"
    "ds_read_b64_tr_b16 v[194:195], v211 offset:2048\n"
    "v_mfma_f32_16x16x32_bf16 v[32:35], v[152:155], v[180:183], v[32:35]\n"
    "buffer_load_dwordx4 v213, s[20:23], s43 offen offset:3072 lds\n"
    "ds_read_b64_tr_b16 v[168:169], v206 offset:16384\n"
    "v_mfma_f32_16x16x32_bf16 v[196:199], v[200:203], v[180:183], v[196:199]\n"
    "ds_read_b64_tr_b16 v[170:171], v206 offset:18432\n"
    "s_waitcnt lgkmcnt(6)\n"
    "v_mfma_f32_16x16x32_bf16 v[4:7], v[144:147], v[184:187], v[4:7]\n"
    "ds_read_b64_tr_b16 v[172:173], v207 offset:0\n"
    "v_mfma_f32_16x16x32_bf16 v[20:23], v[148:151], v[184:187], v[20:23]\n"
    "ds_read_b64_tr_b16 v[174:175], v207 offset:2048\n"
    "v_mfma_f32_16x16x32_bf16 v[36:39], v[152:155], v[184:187], v[36:39]\n"
    "ds_read_b64_tr_b16 v[176:177], v207 offset:16384\n"
    "s_waitcnt lgkmcnt(7)\n"
    "v_mfma_f32_16x16x32_bf16 v[8:11], v[144:147], v[188:191], v[8:11]\n"
    "ds_read_b64_tr_b16 v[178:179], v207 offset:18432\n"
    "s_add_u32 m0, s50, 90112\n"
    "s_cmp_eq_u32 s53, 0\n"
    "v_mfma_f32_16x16x32_bf16 v[24:27], v[148:151], v[188:191], v[24:27]\n"
    "s_cbranch_scc1 Lm62_%=\n"
    "buffer_load_dwordx4 v214, s[20:23], s40 offen lds\n"
    "Lm62_%=:\n"
    "v_mfma_f32_16x16x32_bf16 v[40:43], v[152:155], v[188:191], v[40:43]\n"
    "s_add_u32 s20, s20, 294912\n"
    "s_addc_u32 s21, s21, 0\n"
    "s_waitcnt lgkmcnt(6)\n"
    "v_mfma_f32_16x16x32_bf16 v[12:15], v[144:147], v[192:195], v[12:15]\n"
    "v_mfma_f32_16x16x32_bf16 v[28:31], v[148:151], v[192:195], v[28:31]\n"
    "v_mfma_f32_16x16x32_bf16 v[44:47], v[152:155], v[192:195], v[44:47]\n"
    "s_waitcnt vmcnt(5) lgkmcnt(0)\n"
    "s_barrier\n"
    "v_mfma_f32_16x16x32_bf16 v[48:51], v[156:159], v[180:183], v[48:51]\n"
    "ds_read_b64_tr_b16 v[144:145], v204 offset:45056\n"
    "v_mfma_f32_16x16x32_bf16 v[52:55], v[156:159], v[184:187], v[52:55]\n"
    "ds_read_b64_tr_b16 v[146:147], v204 offset:47104\n"
    "s_add_u32 m0, s51, 0\n"
    "v_mfma_f32_16x16x32_bf16 v[56:59], v[156:159], v[188:191], v[56:59]\n"
    "buffer_load_dwordx4 v215, s[24:27], s44 offen sc1 lds\n"
    "ds_read_b64_tr_b16 v[148:149], v204 offset:61440\n"
    "v_mfma_f32_16x16x32_bf16 v[60:63], v[156:159], v[192:195], v[60:63]\n"
    "s_add_u32 s44, s44, 4096\n"
    "ds_read_b64_tr_b16 v[150:151], v204 offset:63488\n"
    "v_mfma_f32_16x16x32_bf16 v[64:67], v[160:163], v[180:183], v[64:67]\n"
    "ds_read_b64_tr_b16 v[152:153], v212 offset:45056\n"
    "v_mfma_f32_16x16x32_bf16 v[68:71], v[160:163], v[184:187], v[68:71]\n"
    "ds_read_b64_tr_b16 v[154:155], v212 offset:47104\n"
    "v_mfma_f32_16x16x32_bf16 v[72:75], v[160:163], v[188:191], v[72:75]\n"
    "ds_read_b64_tr_b16 v[156:157], v205 offset:45056\n"
    "s_cbranch_vccnz Lh63_%=\n"
    "buffer_store_dwordx4 v[180:183], v219, s[32:35], s76 offen offset:1792\n"
    "Lh63_%=:\n"
    "v_mfma_f32_16x16x32_bf16 v[76:79], v[160:163], v[192:195], v[76:79]\n"
    "ds_read_b64_tr_b16 v[158:159], v205 offset:47104\n"
    "s_add_u32 m0, s49, 0\n"
    "v_mfma_f32_16x16x32_bf16 v[80:83], v[164:167], v[180:183], v[80:83]\n"
    "buffer_load_dwordx4 v213, s[20:23], s40 offen lds\n"
    "ds_read_b64_tr_b16 v[160:161], v205 offset:61440\n"
    "v_mfma_f32_16x16x32_bf16 v[84:87], v[164:167], v[184:187], v[84:87]\n"
    "ds_read_b64_tr_b16 v[162:163], v205 offset:63488\n"
    "v_mfma_f32_16x16x32_bf16 v[88:91], v[164:167], v[188:191], v[88:91]\n"
    "v_add_u32 v204, 90112, v204\n"
    "v_mfma_f32_16x16x32_bf16 v[92:95], v[164:167], v[192:195], v[92:95]\n"
    "v_add_u32 v212, 90112, v212\n"
    "v_add_u32 v205, 90112, v205\n"
    "v_mfma_f32_16x16x32_bf16 v[96:99], v[168:171], v[180:183], v[96:99]\n"
    "ds_read_b64_tr_b16 v[164:165], v206 offset:45056\n"
    "v_mfma_f32_16x16x32_bf16 v[112:115], v[172:175], v[180:183], v[112:115]\n"
    "ds_read_b64_tr_b16 v[166:167], v206 offset:47104\n"
    "v_mfma_f32_16x16x32_bf16 v[128:131], v[176:179], v[180:183], v[128:131]\n"
    "buffer_load_dwordx4 v213, s[20:23], s41 offen offset:1024 lds\n"
    "v_mfma_f32_16x16x32_bf16 v[100:103], v[168:171], v[184:187], v[100:103]\n"
    "ds_read_b64_tr_b16 v[180:181], v208 offset:45056\n"
    "v_mfma_f32_16x16x32_bf16 v[116:119], v[172:175], v[184:187], v[116:119]\n"
    "ds_read_b64_tr_b16 v[182:183], v208 offset:47104\n"
    "v_mfma_f32_16x16x32_bf16 v[132:135], v[176:179], v[184:187], v[132:135]\n"
    "s_cmp_lg_u32 s80, 4\n"
    "s_cbranch_scc1 Lww64_%=\n"
    "buffer_load_dword v250, v250, s[28:31], 0 offen\n"
    "Lww64_%=:\n"
    "v_add_u32 v208, 90112, v208\n"
    "v_mfma_f32_16x16x32_bf16 v[104:107], v[168:171], v[188:191], v[104:107]\n"
    "ds_read_b64_tr_b16 v[184:185], v209 offset:45056\n"
    "v_mfma_f32_16x16x32_bf16 v[120:123], v[172:175], v[188:191], v[120:123]\n"
    "ds_read_b64_tr_b16 v[186:187], v209 offset:47104\n"
    "v_mfma_f32_16x16x32_bf16 v[136:139], v[176:179], v[188:191], v[136:139]\n"
    "buffer_load_dwordx4 v213, s[20:23], s42 offen offset:2048 lds\n"
    "v_mfma_f32_16x16x32_bf16 v[108:111], v[168:171], v[192:195], v[108:111]\n"
    "ds_read_b64_tr_b16 v[188:189], v210 offset:45056\n"
    "v_mfma_f32_16x16x32_bf16 v[124:127], v[172:175], v[192:195], v[124:127]\n"
    "ds_read_b64_tr_b16 v[190:191], v210 offset:47104\n"
    "v_mfma_f32_16x16x32_bf16 v[140:143], v[176:179], v[192:195], v[140:143]\n"
    "v_add_u32 v209, 90112, v209\n"
    "v_add_u32 v210, 90112, v210\n"
    "s_waitcnt lgkmcnt(4)\n"
    "v_mfma_f32_16x16x32_bf16 v[0:3], v[144:147], v[180:183], v[0:3]\n"
    "ds_read_b64_tr_b16 v[192:193], v211 offset:45056\n"
    "v_mfma_f32_16x16x32_bf16 v[16:19], v[148:151], v[180:183], v[16:19]\n"
    "ds_read_b64_tr_b16 v[194:195], v211 offset:47104\n"
    "v_mfma_f32_16x16x32_bf16 v[32:35], v[152:155], v[180:183], v[32:35]\n"
    "buffer_load_dwordx4 v213, s[20:23], s43 offen offset:3072 lds\n"
    "ds_read_b64_tr_b16 v[168:169], v206 offset:61440\n"
    "v_mfma_f32_16x16x32_bf16 v[196:199], v[200:203], v[180:183], v[196:199]\n"
    "ds_read_b64_tr_b16 v[170:171], v206 offset:63488\n"
    "s_waitcnt lgkmcnt(6)\n"
    "v_mfma_f32_16x16x32_bf16 v[4:7], v[144:147], v[184:187], v[4:7]\n"
    "ds_read_b64_tr_b16 v[172:173], v207 offset:45056\n"
    "v_mfma_f32_16x16x32_bf16 v[20:23], v[148:151], v[184:187], v[20:23]\n"
    "ds_read_b64_tr_b16 v[174:175], v207 offset:47104\n"
    "v_mfma_f32_16x16x32_bf16 v[36:39], v[152:155], v[184:187], v[36:39]\n"
    "ds_read_b64_tr_b16 v[176:177], v207 offset:61440\n"
    "s_waitcnt lgkmcnt(7)\n"
    "v_mfma_f32_16x16x32_bf16 v[8:11], v[144:147], v[188:191], v[8:11]\n"
    "ds_read_b64_tr_b16 v[178:179], v207 offset:63488\n"
    "s_add_u32 m0, s50, 0\n"
    "s_cmp_eq_u32 s53, 0\n"
    "v_mfma_f32_16x16x32_bf16 v[24:27], v[148:151], v[188:191], v[24:27]\n"
    "s_cbranch_scc1 Lm65_%=\n"
    "buffer_load_dwordx4 v214, s[20:23], s40 offen lds\n"
    "Lm65_%=:\n"
    "v_mfma_f32_16x16x32_bf16 v[40:43], v[152:155], v[188:191], v[40:43]\n"
    "s_add_u32 s20, s20, 294912\n"
    "s_addc_u32 s21, s21, 0\n"
    "v_add_u32 v206, 90112, v206\n"
    "s_waitcnt lgkmcnt(6)\n"
    "v_mfma_f32_16x16x32_bf16 v[12:15], v[144:147], v[192:195], v[12:15]\n"
    "v_add_u32 v207, 90112, v207\n"
    "v_mfma_f32_16x16x32_bf16 v[28:31], v[148:151], v[192:195], v[28:31]\n"
    "v_add_u32 v211, 90112, v211\n"
    "v_mfma_f32_16x16x32_bf16 v[44:47], v[152:155], v[192:195], v[44:47]\n"
    "s_waitcnt vmcnt(5) lgkmcnt(0)\n"
    "s_barrier\n"
    "v_mfma_f32_16x16x32_bf16 v[48:51], v[156:159], v[180:183], v[48:51]\n"
    "ds_read_b64_tr_b16 v[144:145], v204 offset:0\n"
    "v_mfma_f32_16x16x32_bf16 v[52:55], v[156:159], v[184:187], v[52:55]\n"
    "ds_read_b64_tr_b16 v[146:147], v204 offset:2048\n"
    "s_add_u32 m0, s51, 45056\n"
    "v_mfma_f32_16x16x32_bf16 v[56:59], v[156:159], v[188:191], v[56:59]\n"
    "buffer_load_dwordx4 v215, s[24:27], s44 offen sc1 lds\n"
    "ds_read_b64_tr_b16 v[148:149], v204 offset:16384\n"
    "v_mfma_f32_16x16x32_bf16 v[60:63], v[156:159], v[192:195], v[60:63]\n"
    "s_add_u32 s44, s44, 4096\n"
    "ds_read_b64_tr_b16 v[150:151], v204 offset:18432\n"
    "v_mfma_f32_16x16x32_bf16 v[64:67], v[160:163], v[180:183], v[64:67]\n"
    "ds_read_b64_tr_b16 v[152:153], v212 offset:0\n"
    "v_mfma_f32_16x16x32_bf16 v[68:71], v[160:163], v[184:187], v[68:71]\n"
    "ds_read_b64_tr_b16 v[154:155], v212 offset:2048\n"
    "v_mfma_f32_16x16x32_bf16 v[72:75], v[160:163], v[188:191], v[72:75]\n"
    "ds_read_b64_tr_b16 v[156:157], v205 offset:0\n"
    "s_cbranch_vccnz Lh66_%=\n"
    "buffer_store_dwordx4 v[180:183], v219, s[32:35], s76 offen offset:2048\n"
    "Lh66_%=:\n"
    "v_mfma_f32_16x16x32_bf16 v[76:79], v[160:163], v[192:195], v[76:79]\n"
    "ds_read_b64_tr_b16 v[158:159], v205 offset:2048\n"
    "s_add_u32 m0, s49, 45056\n"
    "v_mfma_f32_16x16x32_bf16 v[80:83], v[164:167], v[180:183], v[80:83]\n"
    "buffer_load_dwordx4 v213, s[20:23], s40 offen lds\n"
    "ds_read_b64_tr_b16 v[160:161], v205 offset:16384\n"
    "v_mfma_f32_16x16x32_bf16 v[84:87], v[164:167], v[184:187], v[84:87]\n"
    "ds_read_b64_tr_b16 v[162:163], v205 offset:18432\n"
    "v_mfma_f32_16x16x32_bf16 v[88:91], v[164:167], v[188:191], v[88:91]\n"
    "v_subrev_u32 v204, 90112, v204\n"
    "v_mfma_f32_16x16x32_bf16 v[92:95], v[164:167], v[192:195], v[92:95]\n"
    "v_subrev_u32 v212, 90112, v212\n"
    "v_subrev_u32 v205, 90112, v205\n"
    "v_mfma_f32_16x16x32_bf16 v[96:99], v[168:171], v[180:183], v[96:99]\n"
    "ds_read_b64_tr_b16 v[164:165], v206 offset:0\n"
    "v_mfma_f32_16x16x32_bf16 v[112:115], v[172:175], v[180:183], v[112:115]\n"
    "ds_read_b64_tr_b16 v[166:167], v206 offset:2048\n"
    "v_mfma_f32_16x16x32_bf16 v[128:131], v[176:179], v[180:183], v[128:131]\n"
    "buffer_load_dwordx4 v213, s[20:23], s41 offen offset:1024 lds\n"
    "v_mfma_f32_16x16x32_bf16 v[100:103], v[168:171], v[184:187], v[100:103]\n"
    "ds_read_b64_tr_b16 v[180:181], v208 offset:0\n"
    "v_mfma_f32_16x16x32_bf16 v[116:119], v[172:175], v[184:187], v[116:119]\n"
    "ds_read_b64_tr_b16 v[182:183], v208 offset:2048\n"
    "v_mfma_f32_16x16x32_bf16 v[132:135], v[176:179], v[184:187], v[132:135]\n"
    "s_cmp_lg_u32 s80, 5\n"
    "s_cbranch_scc1 Lww67_%=\n"
    "buffer_load_dword v250, v250, s[28:31], 0 offen\n"
    "Lww67_%=:\n"
    "v_subrev_u32 v208, 90112, v208\n"
    "v_mfma_f32_16x16x32_bf16 v[104:107], v[168:171], v[188:191], v[104:107]\n"
    "ds_read_b64_tr_b16 v[184:185], v209 offset:0\n"
    "v_mfma_f32_16x16x32_bf16 v[120:123], v[172:175], v[188:191], v[120:123]\n"
    "ds_read_b64_tr_b16 v[186:187], v209 offset:2048\n"
    "v_mfma_f32_16x16x32_bf16 v[136:139], v[176:179], v[188:191], v[136:139]\n"
    "buffer_load_dwordx4 v213, s[20:23], s42 offen offset:2048 lds\n"
    "v_mfma_f32_16x16x32_bf16 v[108:111], v[168:171], v[192:195], v[108:111]\n"
    "ds_read_b64_tr_b16 v[188:189], v210 offset:0\n"
    "v_mfma_f32_16x16x32_bf16 v[124:127], v[172:175], v[192:195], v[124:127]\n"
    "ds_read_b64_tr_b16 v[190:191], v210 offset:2048\n"
    "v_mfma_f32_16x16x32_bf16 v[140:143], v[176:179], v[192:195], v[140:143]\n"
    "v_subrev_u32 v209, 90112, v209\n"
    "v_subrev_u32 v210, 90112, v210\n"
    "s_waitcnt lgkmcnt(4)\n"
    "v_mfma_f32_16x16x32_bf16 v[0:3], v[144:147], v[180:183], v[0:3]\n"
    "ds_read_b64_tr_b16 v[192:193], v211 offset:0\n"
    "v_mfma_f32_16x16x32_bf16 v[16:19], v[148:151], v[180:183], v[16:19]\n"
    "ds_read_b64_tr_b16 v[194:195], v211 offset:2048\n"
    "v_mfma_f32_16x16x32_bf16 v[32:35], v[152:155], v[180:183], v[32:35]\n"
    "buffer_load_dwordx4 v213, s[20:23], s43 offen offset:3072 lds\n"
    "ds_read_b64_tr_b16 v[168:169], v206 offset:16384\n"
    "v_mfma_f32_16x16x32_bf16 v[196:199], v[200:203], v[180:183], v[196:199]\n"
    "ds_read_b64_tr_b16 v[170:171], v206 offset:18432\n"
    "s_waitcnt lgkmcnt(6)\n"
    "v_mfma_f32_16x16x32_bf16 v[4:7], v[144:147], v[184:187], v[4:7]\n"
    "ds_read_b64_tr_b16 v[172:173], v207 offset:0\n"
    "v_mfma_f32_16x16x32_bf16 v[20:23], v[148:151], v[184:187], v[20:23]\n"
    "ds_read_b64_tr_b16 v[174:175], v207 offset:2048\n"
    "v_mfma_f32_16x16x32_bf16 v[36:39], v[152:155], v[184:187], v[36:39]\n"
    "ds_read_b64_tr_b16 v[176:177], v207 offset:16384\n"
    "s_waitcnt lgkmcnt(7)\n"
    "v_mfma_f32_16x16x32_bf16 v[8:11], v[144:147], v[188:191], v[8:11]\n"
    "ds_read_b64_tr_b16 v[178:179], v207 offset:18432\n"
    "s_add_u32 m0, s50, 45056\n"
    "s_cmp_eq_u32 s53, 0\n"
    "v_mfma_f32_16x16x32_bf16 v[24:27], v[148:151], v[188:191], v[24:27]\n"
    "s_cbranch_scc1 Lm68_%=\n"
    "buffer_load_dwordx4 v214, s[20:23], s40 offen lds\n"
    "Lm68_%=:\n"
    "v_mfma_f32_16x16x32_bf16 v[40:43], v[152:155], v[188:191], v[40:43]\n"
    "s_add_u32 s20, s20, 294912\n"
    "s_addc_u32 s21, s21, 0\n"
    "v_subrev_u32 v206, 90112, v206\n"
    "s_waitcnt lgkmcnt(6)\n"
    "v_mfma_f32_16x16x32_bf16 v[12:15], v[144:147], v[192:195], v[12:15]\n"
    "v_subrev_u32 v207, 90112, v207\n"
    "v_mfma_f32_16x16x32_bf16 v[28:31], v[148:151], v[192:195], v[28:31]\n"
    "v_subrev_u32 v211, 90112, v211\n"
    "v_mfma_f32_16x16x32_bf16 v[44:47], v[152:155], v[192:195], v[44:47]\n"
    "s_waitcnt vmcnt(5) lgkmcnt(0)\n"
    "s_barrier\n"
    "v_mfma_f32_16x16x32_bf16 v[48:51], v[156:159], v[180:183], v[48:51]\n"
    "ds_read_b64_tr_b16 v[144:145], v204 offset:0\n"
    "v_mfma_f32_16x16x32_bf16 v[52:55], v[156:159], v[184:187], v[52:55]\n"
    "ds_read_b64_tr_b16 v[146:147], v204 offset:2048\n"
    "s_add_u32 m0, s51, 90112\n"
    "v_mfma_f32_16x16x32_bf16 v[56:59], v[156:159], v[188:191], v[56:59]\n"
    "buffer_load_dwordx4 v215, s[24:27], s44 offen sc1 lds\n"
    "ds_read_b64_tr_b16 v[148:149], v204 offset:16384\n"
    "v_mfma_f32_16x16x32_bf16 v[60:63], v[156:159], v[192:195], v[60:63]\n"
    "s_add_u32 s44, s44, 4096\n"
    "ds_read_b64_tr_b16 v[150:151], v204 offset:18432\n"
    "v_mfma_f32_16x16x32_bf16 v[64:67], v[160:163], v[180:183], v[64:67]\n"
    "ds_read_b64_tr_b16 v[152:153], v212 offset:0\n"
    "v_mfma_f32_16x16x32_bf16 v[68:71], v[160:163], v[184:187], v[68:71]\n"
    "ds_read_b64_tr_b16 v[154:155], v212 offset:2048\n"
    "v_mfma_f32_16x16x32_bf16 v[72:75], v[160:163], v[188:191], v[72:75]\n"
    "ds_read_b64_tr_b16 v[156:157], v205 offset:0\n"
    "s_cbranch_vccnz Lh69_%=\n"
    "buffer_store_dwordx4 v[180:183], v219, s[32:35], s76 offen offset:2304\n"
    "Lh69_%=:\n"
    "v_mfma_f32_16x16x32_bf16 v[76:79], v[160:163], v[192:195], v[76:79]\n"
    "ds_read_b64_tr_b16 v[158:159], v205 offset:2048\n"
    "s_add_u32 m0, s49, 90112\n"
    "v_mfma_f32_16x16x32_bf16 v[80:83], v[164:167], v[180:183], v[80:83]\n"
    "buffer_load_dwordx4 v213, s[20:23], s40 offen lds\n"
    "ds_read_b64_tr_b16 v[160:161], v205 offset:16384\n"
    "v_mfma_f32_16x16x32_bf16 v[84:87], v[164:167], v[184:187], v[84:87]\n"
    "ds_read_b64_tr_b16 v[162:163], v205 offset:18432\n"
    "v_mfma_f32_16x16x32_bf16 v[88:91], v[164:167], v[188:191], v[88:91]\n"
    "v_mfma_f32_16x16x32_bf16 v[92:95], v[164:167], v[192:195], v[92:95]\n"
    "v_mfma_f32_16x16x32_bf16 v[96:99], v[168:171], v[180:183], v[96:99]\n"
    "ds_read_b64_tr_b16 v[164:165], v206 offset:0\n"
    "v_mfma_f32_16x16x32_bf16 v[112:115], v[172:175], v[180:183], v[112:115]\n"
    "ds_read_b64_tr_b16 v[166:167], v206 offset:2048\n"
    "v_mfma_f32_16x16x32_bf16 v[128:131], v[176:179], v[180:183], v[128:131]\n"
    "buffer_load_dwordx4 v213, s[20:23], s41 offen offset:1024 lds\n"
    "v_mfma_f32_16x16x32_bf16 v[100:103], v[168:171], v[184:187], v[100:103]\n"
    "ds_read_b64_tr_b16 v[180:181], v208 offset:0\n"
    "v_mfma_f32_16x16x32_bf16 v[116:119], v[172:175], v[184:187], v[116:119]\n"
    "ds_read_b64_tr_b16 v[182:183], v208 offset:2048\n"
    "v_mfma_f32_16x16x32_bf16 v[132:135], v[176:179], v[184:187], v[132:135]\n"
    "s_cmp_lg_u32 s80, 6\n"
    "s_cbranch_scc1 Lww70_%=\n"
    "buffer_load_dword v250, v250, s[28:31], 0 offen\n"
    "Lww70_%=:\n"
    "v_mfma_f32_16x16x32_bf16 v[104:107], v[168:171], v[188:191], v[104:107]\n"
    "ds_read_b64_tr_b16 v[184:185], v209 offset:0\n"
    "v_mfma_f32_16x16x32_bf16 v[120:123], v[172:175], v[188:191], v[120:123]\n"
    "ds_read_b64_tr_b16 v[186:187], v209 offset:2048\n"
    "v_mfma_f32_16x16x32_bf16 v[136:139], v[176:179], v[188:191], v[136:139]\n"
    "buffer_load_dwordx4 v213, s[20:23], s42 offen offset:2048 lds\n"
    "v_mfma_f32_16x16x32_bf16 v[108:111], v[168:171], v[192:195], v[108:111]\n"
    "ds_read_b64_tr_b16 v[188:189], v210 offset:0\n"
    "v_mfma_f32_16x16x32_bf16 v[124:127], v[172:175], v[192:195], v[124:127]\n"
    "ds_read_b64_tr_b16 v[190:191], v210 offset:2048\n"
    "v_mfma_f32_16x16x32_bf16 v[140:143], v[176:179], v[192:195], v[140:143]\n"
    "s_waitcnt lgkmcnt(4)\n"
    "v_mfma_f32_16x16x32_bf16 v[0:3], v[144:147], v[180:183], v[0:3]\n"
    "ds_read_b64_tr_b16 v[192:193], v211 offset:0\n"
    "v_mfma_f32_16x16x32_bf16 v[16:19], v[148:151], v[180:183], v[16:19]\n"
    "ds_read_b64_tr_b16 v[194:195], v211 offset:2048\n"
    "v_mfma_f32_16x16x32_bf16 v[32:35], v[152:155], v[180:183], v[32:35]\n"
    "buffer_load_dwordx4 v213, s[20:23], s43 offen offset:3072 lds\n"
    "ds_read_b64_tr_b16 v[168:169], v206 offset:16384\n"
    "v_mfma_f32_16x16x32_bf16 v[196:199], v[200:203], v[180:183], v[196:199]\n"
    "ds_read_b64_tr_b16 v[170:171], v206 offset:18432\n"
    "s_waitcnt lgkmcnt(6)\n"
    "v_mfma_f32_16x16x32_bf16 v[4:7], v[144:147], v[184:187], v[4:7]\n"
    "ds_read_b64_tr_b16 v[172:173], v207 offset:0\n"
    "v_mfma_f32_16x16x32_bf16 v[20:23], v[148:151], v[184:187], v[20:23]\n"
    "ds_read_b64_tr_b16 v[174:175], v207 offset:2048\n"
    "v_mfma_f32_16x16x32_bf16 v[36:39], v[152:155], v[184:187], v[36:39]\n"
    "ds_read_b64_tr_b16 v[176:177], v207 offset:16384\n"
    "s_waitcnt lgkmcnt(7)\n"
    "v_mfma_f32_16x16x32_bf16 v[8:11], v[144:147], v[188:191], v[8:11]\n"
    "ds_read_b64_tr_b16 v[178:179], v207 offset:18432\n"
    "s_add_u32 m0, s50, 90112\n"
    "s_cmp_eq_u32 s53, 0\n"
    "v_mfma_f32_16x16x32_bf16 v[24:27], v[148:151], v[188:191], v[24:27]\n"
    "s_cbranch_scc1 Lm71_%=\n"
    "buffer_load_dwordx4 v214, s[20:23], s40 offen lds\n"
    "Lm71_%=:\n"
    "v_mfma_f32_16x16x32_bf16 v[40:43], v[152:155], v[188:191], v[40:43]\n"
    "s_add_u32 s20, s20, 294912\n"
    "s_addc_u32 s21, s21, 0\n"
    "s_waitcnt lgkmcnt(6)\n"
    "v_mfma_f32_16x16x32_bf16 v[12:15], v[144:147], v[192:195], v[12:15]\n"
    "v_mfma_f32_16x16x32_bf16 v[28:31], v[148:151], v[192:195], v[28:31]\n"
    "v_mfma_f32_16x16x32_bf16 v[44:47], v[152:155], v[192:195], v[44:47]\n"
    "s_waitcnt vmcnt(5) lgkmcnt(0)\n"
    "s_barrier\n"
    "v_mfma_f32_16x16x32_bf16 v[48:51], v[156:159], v[180:183], v[48:51]\n"
    "ds_read_b64_tr_b16 v[144:145], v204 offset:45056\n"
    "v_mfma_f32_16x16x32_bf16 v[52:55], v[156:159], v[184:187], v[52:55]\n"
    "ds_read_b64_tr_b16 v[146:147], v204 offset:47104\n"
    "s_add_u32 m0, s51, 0\n"
    "v_mfma_f32_16x16x32_bf16 v[56:59], v[156:159], v[188:191], v[56:59]\n"
    "buffer_load_dwordx4 v215, s[24:27], s44 offen sc1 lds\n"
    "ds_read_b64_tr_b16 v[148:149], v204 offset:61440\n"
    "v_mfma_f32_16x16x32_bf16 v[60:63], v[156:159], v[192:195], v[60:63]\n"
    "s_add_u32 s44, s44, 4096\n"
    "ds_read_b64_tr_b16 v[150:151], v204 offset:63488\n"
    "v_mfma_f32_16x16x32_bf16 v[64:67], v[160:163], v[180:183], v[64:67]\n"
    "ds_read_b64_tr_b16 v[152:153], v212 offset:45056\n"
    "v_mfma_f32_16x16x32_bf16 v[68:71], v[160:163], v[184:187], v[68:71]\n"
    "ds_read_b64_tr_b16 v[154:155], v212 offset:47104\n"
    "v_mfma_f32_16x16x32_bf16 v[72:75], v[160:163], v[188:191], v[72:75]\n"
    "ds_read_b64_tr_b16 v[156:157], v205 offset:45056\n"
    "s_cbranch_vccnz Lh72_%=\n"
    "buffer_store_dwordx4 v[180:183], v219, s[32:35], s76 offen offset:2560\n"
    "Lh72_%=:\n"
    "v_mfma_f32_16x16x32_bf16 v[76:79], v[160:163], v[192:195], v[76:79]\n"
    "ds_read_b64_tr_b16 v[158:159], v205 offset:47104\n"
    "s_add_u32 m0, s49, 0\n"
    "v_mfma_f32_16x16x32_bf16 v[80:83], v[164:167], v[180:183], v[80:83]\n"
    "buffer_load_dwordx4 v213, s[20:23], s40 offen lds\n"
    "ds_read_b64_tr_b16 v[160:161], v205 offset:61440\n"
    "v_mfma_f32_16x16x32_bf16 v[84:87], v[164:167], v[184:187], v[84:87]\n"
    "ds_read_b64_tr_b16 v[162:163], v205 offset:63488\n"
    "v_mfma_f32_16x16x32_bf16 v[88:91], v[164:167], v[188:191], v[88:91]\n"
    "buffer_load_dword v255, v254, s[36:39], 0 offen sc1\n"
    "v_add_u32 v204, 90112, v204\n"
    "v_mfma_f32_16x16x32_bf16 v[92:95], v[164:167], v[192:195], v[92:95]\n"
    "v_add_u32 v212, 90112, v212\n"
    "v_add_u32 v205, 90112, v205\n"
    "v_mfma_f32_16x16x32_bf16 v[96:99], v[168:171], v[180:183], v[96:99]\n"
    "ds_read_b64_tr_b16 v[164:165], v206 offset:45056\n"
    "v_mfma_f32_16x16x32_bf16 v[112:115], v[172:175], v[180:183], v[112:115]\n"
    "ds_read_b64_tr_b16 v[166:167], v206 offset:47104\n"
    "v_mfma_f32_16x16x32_bf16 v[128:131], v[176:179], v[180:183], v[128:131]\n"
    "buffer_load_dwordx4 v213, s[20:23], s41 offen offset:1024 lds\n"
    "v_mfma_f32_16x16x32_bf16 v[100:103], v[168:171], v[184:187], v[100:103]\n"
    "ds_read_b64_tr_b16 v[180:181], v208 offset:45056\n"
    "v_mfma_f32_16x16x32_bf16 v[116:119], v[172:175], v[184:187], v[116:119]\n"
    "ds_read_b64_tr_b16 v[182:183], v208 offset:47104\n"
    "v_mfma_f32_16x16x32_bf16 v[132:135], v[176:179], v[184:187], v[132:135]\n"
    "s_cmp_lg_u32 s80, 7\n"
    "s_cbranch_scc1 Lww73_%=\n"
    "buffer_load_dword v250, v250, s[28:31], 0 offen\n"
    "Lww73_%=:\n"
    "v_add_u32 v208, 90112, v208\n"
    "v_mfma_f32_16x16x32_bf16 v[104:107], v[168:171], v[188:191], v[104:107]\n"
    "ds_read_b64_tr_b16 v[184:185], v209 offset:45056\n"
    "v_mfma_f32_16x16x32_bf16 v[120:123], v[172:175], v[188:191], v[120:123]\n"
    "ds_read_b64_tr_b16 v[186:187], v209 offset:47104\n"
    "v_mfma_f32_16x16x32_bf16 v[136:139], v[176:179], v[188:191], v[136:139]\n"
    "buffer_load_dwordx4 v213, s[20:23], s42 offen offset:2048 lds\n"
    "v_mfma_f32_16x16x32_bf16 v[108:111], v[168:171], v[192:195], v[108:111]\n"
    "ds_read_b64_tr_b16 v[188:189], v210 offset:45056\n"
    "v_mfma_f32_16x16x32_bf16 v[124:127], v[172:175], v[192:195], v[124:127]\n"
    "ds_read_b64_tr_b16 v[190:191], v210 offset:47104\n"
    "v_mfma_f32_16x16x32_bf16 v[140:143], v[176:179], v[192:195], v[140:143]\n"
    "v_add_u32 v209, 90112, v209\n"
    "v_add_u32 v210, 90112, v210\n"
    "s_waitcnt lgkmcnt(4)\n"
    "v_mfma_f32_16x16x32_bf16 v[0:3], v[144:147], v[180:183], v[0:3]\n"
    "ds_read_b64_tr_b16 v[192:193], v211 offset:45056\n"
    "v_mfma_f32_16x16x32_bf16 v[16:19], v[148:151], v[180:183], v[16:19]\n"
    "ds_read_b64_tr_b16 v[194:195], v211 offset:47104\n"
    "v_mfma_f32_16x16x32_bf16 v[32:35], v[152:155], v[180:183], v[32:35]\n"
    "buffer_load_dwordx4 v213, s[20:23], s43 offen offset:3072 lds\n"
    "ds_read_b64_tr_b16 v[168:169], v206 offset:61440\n"
    "v_mfma_f32_16x16x32_bf16 v[196:199], v[200:203], v[180:183], v[196:199]\n"
    "ds_read_b64_tr_b16 v[170:171], v206 offset:63488\n"
    "s_waitcnt lgkmcnt(6)\n"
    "v_mfma_f32_16x16x32_bf16 v[4:7], v[144:147], v[184:187], v[4:7]\n"
    "ds_read_b64_tr_b16 v[172:173], v207 offset:45056\n"
    "v_mfma_f32_16x16x32_bf16 v[20:23], v[148:151], v[184:187], v[20:23]\n"
    "ds_read_b64_tr_b16 v[174:175], v207 offset:47104\n"
    "v_mfma_f32_16x16x32_bf16 v[36:39], v[152:155], v[184:187], v[36:39]\n"
    "ds_read_b64_tr_b16 v[176:177], v207 offset:61440\n"
    "s_waitcnt lgkmcnt(7)\n"
    "v_mfma_f32_16x16x32_bf16 v[8:11], v[144:147], v[188:191], v[8:11]\n"
    "ds_read_b64_tr_b16 v[178:179], v207 offset:63488\n"
    "s_add_u32 m0, s50, 0\n"
    "s_cmp_eq_u32 s53, 0\n"
    "v_mfma_f32_16x16x32_bf16 v[24:27], v[148:151], v[188:191], v[24:27]\n"
    "s_cbranch_scc1 Lm74_%=\n"
    "buffer_load_dwordx4 v214, s[20:23], s40 offen lds\n"
    "Lm74_%=:\n"
    "v_mfma_f32_16x16x32_bf16 v[40:43], v[152:155], v[188:191], v[40:43]\n"
    "s_add_u32 s20, s20, 294912\n"
    "s_addc_u32 s21, s21, 0\n"
    "v_add_u32 v206, 90112, v206\n"
    "s_waitcnt lgkmcnt(6)\n"
    "v_mfma_f32_16x16x32_bf16 v[12:15], v[144:147], v[192:195], v[12:15]\n"
    "v_add_u32 v207, 90112, v207\n"
    "v_mfma_f32_16x16x32_bf16 v[28:31], v[148:151], v[192:195], v[28:31]\n"
    "v_add_u32 v211, 90112, v211\n"
    "v_mfma_f32_16x16x32_bf16 v[44:47], v[152:155], v[192:195], v[44:47]\n"
    "s_waitcnt vmcnt(6) lgkmcnt(0)\n"
    "s_barrier\n"
    "v_mfma_f32_16x16x32_bf16 v[48:51], v[156:159], v[180:183], v[48:51]\n"
    "ds_read_b64_tr_b16 v[144:145], v204 offset:0\n"
    "v_mfma_f32_16x16x32_bf16 v[52:55], v[156:159], v[184:187], v[52:55]\n"
    "ds_read_b64_tr_b16 v[146:147], v204 offset:2048\n"
    "s_add_u32 m0, s51, 45056\n"
    "v_mfma_f32_16x16x32_bf16 v[56:59], v[156:159], v[188:191], v[56:59]\n"
    "buffer_load_dwordx4 v215, s[24:27], s44 offen sc1 lds\n"
    "ds_read_b64_tr_b16 v[148:149], v204 offset:16384\n"
    "v_mfma_f32_16x16x32_bf16 v[60:63], v[156:159], v[192:195], v[60:63]\n"
    "s_add_u32 s44, s44, 4096\n"
    "ds_read_b64_tr_b16 v[150:151], v204 offset:18432\n"
    "v_mfma_f32_16x16x32_bf16 v[64:67], v[160:163], v[180:183], v[64:67]\n"
    "ds_read_b64_tr_b16 v[152:153], v212 offset:0\n"
    "v_mfma_f32_16x16x32_bf16 v[68:71], v[160:163], v[184:187], v[68:71]\n"
    "ds_read_b64_tr_b16 v[154:155], v212 offset:2048\n"
    "v_mfma_f32_16x16x32_bf16 v[72:75], v[160:163], v[188:191], v[72:75]\n"
    "ds_read_b64_tr_b16 v[156:157], v205 offset:0\n"
    "s_cbranch_vccnz Lh75_%=\n"
    "buffer_store_dwordx4 v[180:183], v219, s[32:35], s76 offen offset:2816\n"
    "Lh75_%=:\n"
    "v_mfma_f32_16x16x32_bf16 v[76:79], v[160:163], v[192:195], v[76:79]\n"
    "ds_read_b64_tr_b16 v[158:159], v205 offset:2048\n"
    "s_add_u32 m0, s49, 45056\n"
    "v_mfma_f32_16x16x32_bf16 v[80:83], v[164:167], v[180:183], v[80:83]\n"
    "buffer_load_dwordx4 v213, s[20:23], s40 offen lds\n"
    "ds_read_b64_tr_b16 v[160:161], v205 offset:16384\n"
    "v_mfma_f32_16x16x32_bf16 v[84:87], v[164:167], v[184:187], v[84:87]\n"
    "ds_read_b64_tr_b16 v[162:163], v205 offset:18432\n"
    "v_mfma_f32_16x16x32_bf16 v[88:91], v[164:167], v[188:191], v[88:91]\n"
    "v_subrev_u32 v204, 90112, v204\n"
    "v_mfma_f32_16x16x32_bf16 v[92:95], v[164:167], v[192:195], v[92:95]\n"
    "v_subrev_u32 v212, 90112, v212\n"
    "v_subrev_u32 v205, 90112, v205\n"
    "v_mfma_f32_16x16x32_bf16 v[96:99], v[168:171], v[180:183], v[96:99]\n"
    "ds_read_b64_tr_b16 v[164:165], v206 offset:0\n"
    "v_mfma_f32_16x16x32_bf16 v[112:115], v[172:175], v[180:183], v[112:115]\n"
    "ds_read_b64_tr_b16 v[166:167], v206 offset:2048\n"
    "v_mfma_f32_16x16x32_bf16 v[128:131], v[176:179], v[180:183], v[128:131]\n"
    "buffer_load_dwordx4 v213, s[20:23], s41 offen offset:1024 lds\n"
    "v_mfma_f32_16x16x32_bf16 v[100:103], v[168:171], v[184:187], v[100:103]\n"
    "ds_read_b64_tr_b16 v[180:181], v208 offset:0\n"
    "v_mfma_f32_16x16x32_bf16 v[116:119], v[172:175], v[184:187], v[116:119]\n"
    "ds_read_b64_tr_b16 v[182:183], v208 offset:2048\n"
    "v_mfma_f32_16x16x32_bf16 v[132:135], v[176:179], v[184:187], v[132:135]\n"
    "v_subrev_u32 v208, 90112, v208\n"
    "v_mfma_f32_16x16x32_bf16 v[104:107], v[168:171], v[188:191], v[104:107]\n"
    "ds_read_b64_tr_b16 v[184:185], v209 offset:0\n"
    "v_mfma_f32_16x16x32_bf16 v[120:123], v[172:175], v[188:191], v[120:123]\n"
    "ds_read_b64_tr_b16 v[186:187], v209 offset:2048\n"
    "v_mfma_f32_16x16x32_bf16 v[136:139], v[176:179], v[188:191], v[136:139]\n"
    "buffer_load_dwordx4 v213, s[20:23], s42 offen offset:2048 lds\n"
    "v_mfma_f32_16x16x32_bf16 v[108:111], v[168:171], v[192:195], v[108:111]\n"
    "ds_read_b64_tr_b16 v[188:189], v210 offset:0\n"
    "v_mfma_f32_16x16x32_bf16 v[124:127], v[172:175], v[192:195], v[124:127]\n"
    "ds_read_b64_tr_b16 v[190:191], v210 offset:2048\n"
    "v_mfma_f32_16x16x32_bf16 v[140:143], v[176:179], v[192:195], v[140:143]\n"
    "v_subrev_u32 v209, 90112, v209\n"
    "v_subrev_u32 v210, 90112, v210\n"
    "s_waitcnt lgkmcnt(4)\n"
    "v_mfma_f32_16x16x32_bf16 v[0:3], v[144:147], v[180:183], v[0:3]\n"
    "ds_read_b64_tr_b16 v[192:193], v211 offset:0\n"
    "v_mfma_f32_16x16x32_bf16 v[16:19], v[148:151], v[180:183], v[16:19]\n"
    "ds_read_b64_tr_b16 v[194:195], v211 offset:2048\n"
    "v_mfma_f32_16x16x32_bf16 v[32:35], v[152:155], v[180:183], v[32:35]\n"
    "buffer_load_dwordx4 v213, s[20:23], s43 offen offset:3072 lds\n"
    "ds_read_b64_tr_b16 v[168:169], v206 offset:16384\n"
    "v_mfma_f32_16x16x32_bf16 v[196:199], v[200:203], v[180:183], v[196:199]\n"
    "ds_read_b64_tr_b16 v[170:171], v206 offset:18432\n"
    "s_waitcnt lgkmcnt(6)\n"
    "v_mfma_f32_16x16x32_bf16 v[4:7], v[144:147], v[184:187], v[4:7]\n"
    "ds_read_b64_tr_b16 v[172:173], v207 offset:0\n"
    "v_mfma_f32_16x16x32_bf16 v[20:23], v[148:151], v[184:187], v[20:23]\n"
    "ds_read_b64_tr_b16 v[174:175], v207 offset:2048\n"
    "v_mfma_f32_16x16x32_bf16 v[36:39], v[152:155], v[184:187], v[36:39]\n"
    "ds_read_b64_tr_b16 v[176:177], v207 offset:16384\n"
    "s_waitcnt lgkmcnt(7)\n"
    "v_mfma_f32_16x16x32_bf16 v[8:11], v[144:147], v[188:191], v[8:11]\n"
    "ds_read_b64_tr_b16 v[178:179], v207 offset:18432\n"
    "s_add_u32 m0, s50, 45056\n"
    "s_cmp_eq_u32 s53, 0\n"
    "v_mfma_f32_16x16x32_bf16 v[24:27], v[148:151], v[188:191], v[24:27]\n"
    "s_cbranch_scc1 Lm76_%=\n"
    "buffer_load_dwordx4 v214, s[20:23], s40 offen lds\n"
    "Lm76_%=:\n"
    "v_mfma_f32_16x16x32_bf16 v[40:43], v[152:155], v[188:191], v[40:43]\n"
    "s_add_u32 s20, s20, 294912\n"
    "s_addc_u32 s21, s21, 0\n"
    "v_subrev_u32 v206, 90112, v206\n"
    "s_waitcnt lgkmcnt(6)\n"
    "v_mfma_f32_16x16x32_bf16 v[12:15], v[144:147], v[192:195], v[12:15]\n"
    "v_subrev_u32 v207, 90112, v207\n"
    "v_mfma_f32_16x16x32_bf16 v[28:31], v[148:151], v[192:195], v[28:31]\n"
    "v_subrev_u32 v211, 90112, v211\n"
    "v_mfma_f32_16x16x32_bf16 v[44:47], v[152:155], v[192:195], v[44:47]\n"
    "s_waitcnt vmcnt(5) lgkmcnt(0)\n"
    "s_barrier\n"
    "s_movk_i32 s56, 0x7fff\n"
    "s_movk_i32 s59, 18432\n"
    "s_lshl_b32 s57, s83, 4\n"
    "Lcl77_%=:\n"
    "v_xor_b32 v251, s57, v255\n"
    "v_cmp_lt_u32 s[70:71], 15, v251\n"
    "s_nop 0\n"
    "s_cmp_eq_u64 s[70:71], 0\n"
    "s_cbranch_scc1 Lcm78_%=\n"
    "s_sub_u32 s56, s56, 1\n"
    "s_cbranch_scc1 Lcm78_%=\n"
    "buffer_load_dword v255, v254, s[36:39], 0 offen sc1\n"
    "s_waitcnt vmcnt(0)\n"
    "s_branch Lcl77_%=\n"
    "Lcm78_%=:\n"
    "v_mfma_f32_16x16x32_bf16 v[48:51], v[156:159], v[180:183], v[48:51]\n"
    "ds_read_b64_tr_b16 v[144:145], v204 offset:0\n"
    "v_mfma_f32_16x16x32_bf16 v[52:55], v[156:159], v[184:187], v[52:55]\n"
    "ds_read_b64_tr_b16 v[146:147], v204 offset:2048\n"
    "s_add_u32 m0, s51, 90112\n"
    "v_mfma_f32_16x16x32_bf16 v[56:59], v[156:159], v[188:191], v[56:59]\n"
    "buffer_load_dwordx4 v215, s[24:27], s44 offen sc1 lds\n"
    "ds_read_b64_tr_b16 v[148:149], v204 offset:16384\n"
    "v_mfma_f32_16x16x32_bf16 v[60:63], v[156:159], v[192:195], v[60:63]\n"
    "s_add_u32 s44, s44, 4096\n"
    "ds_read_b64_tr_b16 v[150:151], v204 offset:18432\n"
    "v_mfma_f32_16x16x32_bf16 v[64:67], v[160:163], v[180:183], v[64:67]\n"
    "ds_read_b64_tr_b16 v[152:153], v212 offset:0\n"
    "v_mfma_f32_16x16x32_bf16 v[68:71], v[160:163], v[184:187], v[68:71]\n"
    "ds_read_b64_tr_b16 v[154:155], v212 offset:2048\n"
    "v_mfma_f32_16x16x32_bf16 v[72:75], v[160:163], v[188:191], v[72:75]\n"
    "ds_read_b64_tr_b16 v[156:157], v205 offset:0\n"
    "s_cbranch_vccnz Lh79_%=\n"
    "buffer_store_dwordx4 v[180:183], v219, s[32:35], s76 offen offset:3072\n"
    "Lh79_%=:\n"
    "v_mfma_f32_16x16x32_bf16 v[76:79], v[160:163], v[192:195], v[76:79]\n"
    "ds_read_b64_tr_b16 v[158:159], v205 offset:2048\n"
    "s_add_u32 m0, s49, 90112\n"
    "v_mfma_f32_16x16x32_bf16 v[80:83], v[164:167], v[180:183], v[80:83]\n"
    "buffer_load_dwordx4 v213, s[20:23], s40 offen lds\n"
    "ds_read_b64_tr_b16 v[160:161], v205 offset:16384\n"
    "v_mfma_f32_16x16x32_bf16 v[84:87], v[164:167], v[184:187], v[84:87]\n"
    "ds_read_b64_tr_b16 v[162:163], v205 offset:18432\n"
    "v_mfma_f32_16x16x32_bf16 v[88:91], v[164:167], v[188:191], v[88:91]\n"
    "v_mfma_f32_16x16x32_bf16 v[92:95], v[164:167], v[192:195], v[92:95]\n"
    "v_readlane_b32 s70, v251, 18\n"
    "v_readlane_b32 s71, v251, 19\n"
    "v_readlane_b32 s72, v251, 20\n"
    "s_and_b32 s70, s70, 1\n"
    "s_and_b32 s71, s71, 1\n"
    "s_and_b32 s72, s72, 14\n"
    "s_or_b32 s71, s71, s72\n"
    "s_lshl1_add_u32 s77, s71, s70\n"
    "v_mfma_f32_16x16x32_bf16 v[96:99], v[168:171], v[180:183], v[96:99]\n"
    "ds_read_b64_tr_b16 v[164:165], v206 offset:0\n"
    "v_mfma_f32_16x16x32_bf16 v[112:115], v[172:175], v[180:183], v[112:115]\n"
    "ds_read_b64_tr_b16 v[166:167], v206 offset:2048\n"
    "v_mfma_f32_16x16x32_bf16 v[128:131], v[176:179], v[180:183], v[128:131]\n"
    "buffer_load_dwordx4 v213, s[20:23], s41 offen offset:1024 lds\n"
    "v_mfma_f32_16x16x32_bf16 v[100:103], v[168:171], v[184:187], v[100:103]\n"
    "ds_read_b64_tr_b16 v[180:181], v208 offset:0\n"
    "v_mfma_f32_16x16x32_bf16 v[116:119], v[172:175], v[184:187], v[116:119]\n"
    "ds_read_b64_tr_b16 v[182:183], v208 offset:2048\n"
    "v_mfma_f32_16x16x32_bf16 v[132:135], v[176:179], v[184:187], v[132:135]\n"
    "v_mfma_f32_16x16x32_bf16 v[104:107], v[168:171], v[188:191], v[104:107]\n"
    "ds_read_b64_tr_b16 v[184:185], v209 offset:0\n"
    "v_mfma_f32_16x16x32_bf16 v[120:123], v[172:175], v[188:191], v[120:123]\n"
    "ds_read_b64_tr_b16 v[186:187], v209 offset:2048\n"
    "v_mfma_f32_16x16x32_bf16 v[136:139], v[176:179], v[188:191], v[136:139]\n"
    "buffer_load_dwordx4 v213, s[20:23], s42 offen offset:2048 lds\n"
    "v_mfma_f32_16x16x32_bf16 v[108:111], v[168:171], v[192:195], v[108:111]\n"
    "ds_read_b64_tr_b16 v[188:189], v210 offset:0\n"
    "v_mfma_f32_16x16x32_bf16 v[124:127], v[172:175], v[192:195], v[124:127]\n"
    "ds_read_b64_tr_b16 v[190:191], v210 offset:2048\n"
    "v_mfma_f32_16x16x32_bf16 v[140:143], v[176:179], v[192:195], v[140:143]\n"
    "buffer_load_dwordx4 v[220:223], v252, s[36:39], 0 offen offset:0 sc1\n"
    "s_waitcnt lgkmcnt(4)\n"
    "v_mfma_f32_16x16x32_bf16 v[0:3], v[144:147], v[180:183], v[0:3]\n"
    "ds_read_b64_tr_b16 v[192:193], v211 offset:0\n"
    "v_mfma_f32_16x16x32_bf16 v[16:19], v[148:151], v[180:183], v[16:19]\n"
    "ds_read_b64_tr_b16 v[194:195], v211 offset:2048\n"
    "v_mfma_f32_16x16x32_bf16 v[32:35], v[152:155], v[180:183], v[32:35]\n"
    "buffer_load_dwordx4 v213, s[20:23], s43 offen offset:3072 lds\n"
    "ds_read_b64_tr_b16 v[168:169], v206 offset:16384\n"
    "v_mfma_f32_16x16x32_bf16 v[196:199], v[200:203], v[180:183], v[196:199]\n"
    "ds_read_b64_tr_b16 v[170:171], v206 offset:18432\n"
    "s_waitcnt lgkmcnt(6)\n"
    "v_mfma_f32_16x16x32_bf16 v[4:7], v[144:147], v[184:187], v[4:7]\n"
    "ds_read_b64_tr_b16 v[172:173], v207 offset:0\n"
    "v_mfma_f32_16x16x32_bf16 v[20:23], v[148:151], v[184:187], v[20:23]\n"
    "ds_read_b64_tr_b16 v[174:175], v207 offset:2048\n"
    "v_mfma_f32_16x16x32_bf16 v[36:39], v[152:155], v[184:187], v[36:39]\n"
    "ds_read_b64_tr_b16 v[176:177], v207 offset:16384\n"
    "s_waitcnt lgkmcnt(7)\n"
    "v_mfma_f32_16x16x32_bf16 v[8:11], v[144:147], v[188:191], v[8:11]\n"
    "ds_read_b64_tr_b16 v[178:179], v207 offset:18432\n"
    "s_add_u32 m0, s50, 90112\n"
    "s_cmp_eq_u32 s53, 0\n"
    "v_mfma_f32_16x16x32_bf16 v[24:27], v[148:151], v[188:191], v[24:27]\n"
    "s_cbranch_scc1 Lm80_%=\n"
    "buffer_load_dwordx4 v214, s[20:23], s40 offen lds\n"
    "Lm80_%=:\n"
    "v_mfma_f32_16x16x32_bf16 v[40:43], v[152:155], v[188:191], v[40:43]\n"
    "s_add_u32 s20, s20, 294912\n"
    "s_addc_u32 s21, s21, 0\n"
    "s_waitcnt lgkmcnt(6)\n"
    "v_mfma_f32_16x16x32_bf16 v[12:15], v[144:147], v[192:195], v[12:15]\n"
    "v_mfma_f32_16x16x32_bf16 v[28:31], v[148:151], v[192:195], v[28:31]\n"
    "v_mfma_f32_16x16x32_bf16 v[44:47], v[152:155], v[192:195], v[44:47]\n"
    "s_waitcnt vmcnt(6) lgkmcnt(0)\n"
    "s_barrier\n"
    "v_mfma_f32_16x16x32_bf16 v[48:51], v[156:159], v[180:183], v[48:51]\n"
    "ds_read_b64_tr_b16 v[144:145], v204 offset:45056\n"
    "v_mfma_f32_16x16x32_bf16 v[52:55], v[156:159], v[184:187], v[52:55]\n"
    "ds_read_b64_tr_b16 v[146:147], v204 offset:47104\n"
    "s_add_u32 m0, s51, 0\n"
    "v_mfma_f32_16x16x32_bf16 v[56:59], v[156:159], v[188:191], v[56:59]\n"
    "buffer_load_dwordx4 v215, s[24:27], s44 offen sc1 lds\n"
    "ds_read_b64_tr_b16 v[148:149], v204 offset:61440\n"
    "v_mfma_f32_16x16x32_bf16 v[60:63], v[156:159], v[192:195], v[60:63]\n"
    "s_add_u32 s44, s44, 4096\n"
    "ds_read_b64_tr_b16 v[150:151], v204 offset:63488\n"
    "v_mfma_f32_16x16x32_bf16 v[64:67], v[160:163], v[180:183], v[64:67]\n"
    "ds_read_b64_tr_b16 v[152:153], v212 offset:45056\n"
    "v_mfma_f32_16x16x32_bf16 v[68:71], v[160:163], v[184:187], v[68:71]\n"
    "ds_read_b64_tr_b16 v[154:155], v212 offset:47104\n"
    "v_mfma_f32_16x16x32_bf16 v[72:75], v[160:163], v[188:191], v[72:75]\n"
    "ds_read_b64_tr_b16 v[156:157], v205 offset:45056\n"
    "s_cbranch_vccnz Lh81_%=\n"
    "buffer_store_dwordx4 v[180:183], v219, s[32:35], s76 offen offset:3328\n"
    "Lh81_%=:\n"
    "v_mfma_f32_16x16x32_bf16 v[76:79], v[160:163], v[192:195], v[76:79]\n"
    "ds_read_b64_tr_b16 v[158:159], v205 offset:47104\n"
    "s_add_u32 m0, s49, 0\n"
    "v_mfma_f32_16x16x32_bf16 v[80:83], v[164:167], v[180:183], v[80:83]\n"
    "buffer_load_dwordx4 v213, s[20:23], s40 offen lds\n"
    "ds_read_b64_tr_b16 v[160:161], v205 offset:61440\n"
    "v_mfma_f32_16x16x32_bf16 v[84:87], v[164:167], v[184:187], v[84:87]\n"
    "ds_read_b64_tr_b16 v[162:163], v205 offset:63488\n"
    "v_mfma_f32_16x16x32_bf16 v[88:91], v[164:167], v[188:191], v[88:91]\n"
    "v_add_u32 v204, 90112, v204\n"
    "v_mfma_f32_16x16x32_bf16 v[92:95], v[164:167], v[192:195], v[92:95]\n"
    "buffer_load_dwordx4 v[224:227], v252, s[36:39], s59 offen offset:0 sc1\n"
    "v_add_u32 v212, 90112, v212\n"
    "v_add_u32 v205, 90112, v205\n"
    "v_mfma_f32_16x16x32_bf16 v[96:99], v[168:171], v[180:183], v[96:99]\n"
    "ds_read_b64_tr_b16 v[164:165], v206 offset:45056\n"
    "v_mfma_f32_16x16x32_bf16 v[112:115], v[172:175], v[180:183], v[112:115]\n"
    "ds_read_b64_tr_b16 v[166:167], v206 offset:47104\n"
    "v_mfma_f32_16x16x32_bf16 v[128:131], v[176:179], v[180:183], v[128:131]\n"
    "buffer_load_dwordx4 v213, s[20:23], s41 offen offset:1024 lds\n"
    "v_mfma_f32_16x16x32_bf16 v[100:103], v[168:171], v[184:187], v[100:103]\n"
    "ds_read_b64_tr_b16 v[180:181], v208 offset:45056\n"
    "v_mfma_f32_16x16x32_bf16 v[116:119], v[172:175], v[184:187], v[116:119]\n"
    "ds_read_b64_tr_b16 v[182:183], v208 offset:47104\n"
    "v_mfma_f32_16x16x32_bf16 v[132:135], v[176:179], v[184:187], v[132:135]\n"
    "v_add_u32 v208, 90112, v208\n"
    "v_mfma_f32_16x16x32_bf16 v[104:107], v[168:171], v[188:191], v[104:107]\n"
    "ds_read_b64_tr_b16 v[184:185], v209 offset:45056\n"
    "v_mfma_f32_16x16x32_bf16 v[120:123], v[172:175], v[188:191], v[120:123]\n"
    "ds_read_b64_tr_b16 v[186:187], v209 offset:47104\n"
    "v_mfma_f32_16x16x32_bf16 v[136:139], v[176:179], v[188:191], v[136:139]\n"
    "buffer_load_dwordx4 v213, s[20:23], s42 offen offset:2048 lds\n"
    "v_mfma_f32_16x16x32_bf16 v[108:111], v[168:171], v[192:195], v[108:111]\n"
    "ds_read_b64_tr_b16 v[188:189], v210 offset:45056\n"
    "v_mfma_f32_16x16x32_bf16 v[124:127], v[172:175], v[192:195], v[124:127]\n"
    "ds_read_b64_tr_b16 v[190:191], v210 offset:47104\n"
    "v_mfma_f32_16x16x32_bf16 v[140:143], v[176:179], v[192:195], v[140:143]\n"
    "buffer_load_dwordx4 v[228:231], v253, s[36:39], 0 offen sc1\n"
    "v_add_u32 v209, 90112, v209\n"
    "v_add_u32 v210, 90112, v210\n"
    "s_waitcnt lgkmcnt(4)\n"
    "v_mfma_f32_16x16x32_bf16 v[0:3], v[144:147], v[180:183], v[0:3]\n"
    "ds_read_b64_tr_b16 v[192:193], v211 offset:45056\n"
    "v_mfma_f32_16x16x32_bf16 v[16:19], v[148:151], v[180:183], v[16:19]\n"
    "ds_read_b64_tr_b16 v[194:195], v211 offset:47104\n"
    "v_mfma_f32_16x16x32_bf16 v[32:35], v[152:155], v[180:183], v[32:35]\n"
    "buffer_load_dwordx4 v213, s[20:23], s43 offen offset:3072 lds\n"
    "ds_read_b64_tr_b16 v[168:169], v206 offset:61440\n"
    "v_mfma_f32_16x16x32_bf16 v[196:199], v[200:203], v[180:183], v[196:199]\n"
    "ds_read_b64_tr_b16 v[170:171], v206 offset:63488\n"
    "s_waitcnt lgkmcnt(6)\n"
    "v_mfma_f32_16x16x32_bf16 v[4:7], v[144:147], v[184:187], v[4:7]\n"
    "ds_read_b64_tr_b16 v[172:173], v207 offset:45056\n"
    "v_mfma_f32_16x16x32_bf16 v[20:23], v[148:151], v[184:187], v[20:23]\n"
    "ds_read_b64_tr_b16 v[174:175], v207 offset:47104\n"
    "v_mfma_f32_16x16x32_bf16 v[36:39], v[152:155], v[184:187], v[36:39]\n"
    "ds_read_b64_tr_b16 v[176:177], v207 offset:61440\n"
    "s_waitcnt lgkmcnt(7)\n"
    "v_mfma_f32_16x16x32_bf16 v[8:11], v[144:147], v[188:191], v[8:11]\n"
    "ds_read_b64_tr_b16 v[178:179], v207 offset:63488\n"
    "s_add_u32 m0, s50, 0\n"
    "s_cmp_eq_u32 s53, 0\n"
    "v_mfma_f32_16x16x32_bf16 v[24:27], v[148:151], v[188:191], v[24:27]\n"
    "s_cbranch_scc1 Lm82_%=\n"
    "buffer_load_dwordx4 v214, s[20:23], s40 offen lds\n"
    "Lm82_%=:\n"
    "v_mfma_f32_16x16x32_bf16 v[40:43], v[152:155], v[188:191], v[40:43]\n"
    "s_add_u32 s20, s20, 294912\n"
    "s_addc_u32 s21, s21, 0\n"
    "v_add_u32 v206, 90112, v206\n"
    "s_waitcnt lgkmcnt(6)\n"
    "v_mfma_f32_16x16x32_bf16 v[12:15], v[144:147], v[192:195], v[12:15]\n"
    "v_add_u32 v207, 90112, v207\n"
    "v_mfma_f32_16x16x32_bf16 v[28:31], v[148:151], v[192:195], v[28:31]\n"
    "v_add_u32 v211, 90112, v211\n"
    "v_mfma_f32_16x16x32_bf16 v[44:47], v[152:155], v[192:195], v[44:47]\n"
    "s_waitcnt vmcnt(7) lgkmcnt(0)\n"
    "s_barrier\n"
    "v_mfma_f32_16x16x32_bf16 v[48:51], v[156:159], v[180:183], v[48:51]\n"
    "ds_read_b64_tr_b16 v[144:145], v204 offset:0\n"
    "v_mfma_f32_16x16x32_bf16 v[52:55], v[156:159], v[184:187], v[52:55]\n"
    "ds_read_b64_tr_b16 v[146:147], v204 offset:2048\n"
    "s_add_u32 m0, s51, 45056\n"
    "v_mfma_f32_16x16x32_bf16 v[56:59], v[156:159], v[188:191], v[56:59]\n"
    "buffer_load_dwordx4 v215, s[24:27], s44 offen sc1 lds\n"
    "ds_read_b64_tr_b16 v[148:149], v204 offset:16384\n"
    "v_mfma_f32_16x16x32_bf16 v[60:63], v[156:159], v[192:195], v[60:63]\n"
    "s_add_u32 s44, s44, 4096\n"
    "ds_read_b64_tr_b16 v[150:151], v204 offset:18432\n"
    "v_mfma_f32_16x16x32_bf16 v[64:67], v[160:163], v[180:183], v[64:67]\n"
    "ds_read_b64_tr_b16 v[152:153], v212 offset:0\n"
    "v_mfma_f32_16x16x32_bf16 v[68:71], v[160:163], v[184:187], v[68:71]\n"
    "ds_read_b64_tr_b16 v[154:155], v212 offset:2048\n"
    "v_mfma_f32_16x16x32_bf16 v[72:75], v[160:163], v[188:191], v[72:75]\n"
    "ds_read_b64_tr_b16 v[156:157], v205 offset:0\n"
    "s_cbranch_vccnz Lh83_%=\n"
    "buffer_store_dwordx4 v[180:183], v219, s[32:35], s76 offen offset:3584\n"
    "Lh83_%=:\n"
    "v_mfma_f32_16x16x32_bf16 v[76:79], v[160:163], v[192:195], v[76:79]\n"
    "ds_read_b64_tr_b16 v[158:159], v205 offset:2048\n"
    "s_add_u32 m0, s49, 45056\n"
    "v_mfma_f32_16x16x32_bf16 v[80:83], v[164:167], v[180:183], v[80:83]\n"
    "buffer_load_dwordx4 v213, s[20:23], s40 offen lds\n"
    "ds_read_b64_tr_b16 v[160:161], v205 offset:16384\n"
    "v_mfma_f32_16x16x32_bf16 v[84:87], v[164:167], v[184:187], v[84:87]\n"
    "ds_read_b64_tr_b16 v[162:163], v205 offset:18432\n"
    "v_mfma_f32_16x16x32_bf16 v[88:91], v[164:167], v[188:191], v[88:91]\n"
    "v_subrev_u32 v204, 90112, v204\n"
    "v_mfma_f32_16x16x32_bf16 v[92:95], v[164:167], v[192:195], v[92:95]\n"
    "buffer_load_dwordx4 v[232:235], v252, s[36:39], 0 offen offset:64 sc1\n"
    "v_subrev_u32 v212, 90112, v212\n"
    "v_subrev_u32 v205, 90112, v205\n"
    "v_mfma_f32_16x16x32_bf16 v[96:99], v[168:171], v[180:183], v[96:99]\n"
    "ds_read_b64_tr_b16 v[164:165], v206 offset:0\n"
    "v_mfma_f32_16x16x32_bf16 v[112:115], v[172:175], v[180:183], v[112:115]\n"
    "ds_read_b64_tr_b16 v[166:167], v206 offset:2048\n"
    "v_mfma_f32_16x16x32_bf16 v[128:131], v[176:179], v[180:183], v[128:131]\n"
    "buffer_load_dwordx4 v213, s[20:23], s41 offen offset:1024 lds\n"
    "v_mfma_f32_16x16x32_bf16 v[100:103], v[168:171], v[184:187], v[100:103]\n"
    "ds_read_b64_tr_b16 v[180:181], v208 offset:0\n"
    "v_mfma_f32_16x16x32_bf16 v[116:119], v[172:175], v[184:187], v[116:119]\n"
    "ds_read_b64_tr_b16 v[182:183], v208 offset:2048\n"
    "v_mfma_f32_16x16x32_bf16 v[132:135], v[176:179], v[184:187], v[132:135]\n"
    "v_subrev_u32 v208, 90112, v208\n"
    "v_mfma_f32_16x16x32_bf16 v[104:107], v[168:171], v[188:191], v[104:107]\n"
    "ds_read_b64_tr_b16 v[184:185], v209 offset:0\n"
    "v_mfma_f32_16x16x32_bf16 v[120:123], v[172:175], v[188:191], v[120:123]\n"
    "ds_read_b64_tr_b16 v[186:187], v209 offset:2048\n"
    "v_mfma_f32_16x16x32_bf16 v[136:139], v[176:179], v[188:191], v[136:139]\n"
    "buffer_load_dwordx4 v213, s[20:23], s42 offen offset:2048 lds\n"
    "v_mfma_f32_16x16x32_bf16 v[108:111], v[168:171], v[192:195], v[108:111]\n"
    "ds_read_b64_tr_b16 v[188:189], v210 offset:0\n"
    "v_mfma_f32_16x16x32_bf16 v[124:127], v[172:175], v[192:195], v[124:127]\n"
    "ds_read_b64_tr_b16 v[190:191], v210 offset:2048\n"
    "v_mfma_f32_16x16x32_bf16 v[140:143], v[176:179], v[192:195], v[140:143]\n"
    "buffer_load_dwordx4 v[236:239], v252, s[36:39], s59 offen offset:64 sc1\n"
    "v_subrev_u32 v209, 90112, v209\n"
    "v_subrev_u32 v210, 90112, v210\n"
    "s_waitcnt lgkmcnt(4)\n"
    "v_mfma_f32_16x16x32_bf16 v[0:3], v[144:147], v[180:183], v[0:3]\n"
    "ds_read_b64_tr_b16 v[192:193], v211 offset:0\n"
    "v_mfma_f32_16x16x32_bf16 v[16:19], v[148:151], v[180:183], v[16:19]\n"
    "ds_read_b64_tr_b16 v[194:195], v211 offset:2048\n"
    "v_mfma_f32_16x16x32_bf16 v[32:35], v[152:155], v[180:183], v[32:35]\n"
    "buffer_load_dwordx4 v213, s[20:23], s43 offen offset:3072 lds\n"
    "ds_read_b64_tr_b16 v[168:169], v206 offset:16384\n"
    "v_mfma_f32_16x16x32_bf16 v[196:199], v[200:203], v[180:183], v[196:199]\n"
    "ds_read_b64_tr_b16 v[170:171], v206 offset:18432\n"
    "s_waitcnt lgkmcnt(6)\n"
    "v_mfma_f32_16x16x32_bf16 v[4:7], v[144:147], v[184:187], v[4:7]\n"
    "ds_read_b64_tr_b16 v[172:173], v207 offset:0\n"
    "v_mfma_f32_16x16x32_bf16 v[20:23], v[148:151], v[184:187], v[20:23]\n"
    "ds_read_b64_tr_b16 v[174:175], v207 offset:2048\n"
    "v_mfma_f32_16x16x32_bf16 v[36:39], v[152:155], v[184:187], v[36:39]\n"
    "ds_read_b64_tr_b16 v[176:177], v207 offset:16384\n"
    "s_waitcnt lgkmcnt(7)\n"
    "v_mfma_f32_16x16x32_bf16 v[8:11], v[144:147], v[188:191], v[8:11]\n"
    "ds_read_b64_tr_b16 v[178:179], v207 offset:18432\n"
    "s_add_u32 m0, s50, 45056\n"
    "s_cmp_eq_u32 s53, 0\n"
    "v_mfma_f32_16x16x32_bf16 v[24:27], v[148:151], v[188:191], v[24:27]\n"
    "s_cbranch_scc1 Lm84_%=\n"
    "buffer_load_dwordx4 v214, s[20:23], s40 offen lds\n"
    "Lm84_%=:\n"
    "v_mfma_f32_16x16x32_bf16 v[40:43], v[152:155], v[188:191], v[40:43]\n"
    "s_add_u32 s20, s20, 294912\n"
    "s_addc_u32 s21, s21, 0\n"
    "v_subrev_u32 v206, 90112, v206\n"
    "s_waitcnt lgkmcnt(6)\n"
    "v_mfma_f32_16x16x32_bf16 v[12:15], v[144:147], v[192:195], v[12:15]\n"
    "v_subrev_u32 v207, 90112, v207\n"
    "v_mfma_f32_16x16x32_bf16 v[28:31], v[148:151], v[192:195], v[28:31]\n"
    "v_subrev_u32 v211, 90112, v211\n"
    "v_mfma_f32_16x16x32_bf16 v[44:47], v[152:155], v[192:195], v[44:47]\n"
    "s_waitcnt vmcnt(7) lgkmcnt(0)\n"
    "s_barrier\n"
    "v_mfma_f32_16x16x32_bf16 v[48:51], v[156:159], v[180:183], v[48:51]\n"
    "ds_read_b64_tr_b16 v[144:145], v204 offset:0\n"
    "v_mfma_f32_16x16x32_bf16 v[52:55], v[156:159], v[184:187], v[52:55]\n"
    "ds_read_b64_tr_b16 v[146:147], v204 offset:2048\n"
    "s_add_u32 m0, s51, 90112\n"
    "v_mfma_f32_16x16x32_bf16 v[56:59], v[156:159], v[188:191], v[56:59]\n"
    "buffer_load_dwordx4 v215, s[24:27], s44 offen sc1 lds\n"
    "ds_read_b64_tr_b16 v[148:149], v204 offset:16384\n"
    "v_mfma_f32_16x16x32_bf16 v[60:63], v[156:159], v[192:195], v[60:63]\n"
    "s_add_u32 s44, s44, 4096\n"
    "ds_read_b64_tr_b16 v[150:151], v204 offset:18432\n"
    "v_mfma_f32_16x16x32_bf16 v[64:67], v[160:163], v[180:183], v[64:67]\n"
    "ds_read_b64_tr_b16 v[152:153], v212 offset:0\n"
    "v_mfma_f32_16x16x32_bf16 v[68:71], v[160:163], v[184:187], v[68:71]\n"
    "ds_read_b64_tr_b16 v[154:155], v212 offset:2048\n"
    "v_mfma_f32_16x16x32_bf16 v[72:75], v[160:163], v[188:191], v[72:75]\n"
    "ds_read_b64_tr_b16 v[156:157], v205 offset:0\n"
    "s_cbranch_vccnz Lh85_%=\n"
    "buffer_store_dwordx4 v[180:183], v219, s[32:35], s76 offen offset:3840\n"
    "Lh85_%=:\n"
    "v_mfma_f32_16x16x32_bf16 v[76:79], v[160:163], v[192:195], v[76:79]\n"
    "ds_read_b64_tr_b16 v[158:159], v205 offset:2048\n"
    "s_add_u32 m0, s49, 90112\n"
    "v_mfma_f32_16x16x32_bf16 v[80:83], v[164:167], v[180:183], v[80:83]\n"
    "buffer_load_dwordx4 v213, s[20:23], s40 offen lds\n"
    "ds_read_b64_tr_b16 v[160:161], v205 offset:16384\n"
    "v_mfma_f32_16x16x32_bf16 v[84:87], v[164:167], v[184:187], v[84:87]\n"
    "ds_read_b64_tr_b16 v[162:163], v205 offset:18432\n"
    "v_mfma_f32_16x16x32_bf16 v[88:91], v[164:167], v[188:191], v[88:91]\n"
    "v_mfma_f32_16x16x32_bf16 v[92:95], v[164:167], v[192:195], v[92:95]\n"
    "buffer_load_dwordx4 v[240:243], v252, s[36:39], 0 offen offset:128 sc1\n"
    "v_mfma_f32_16x16x32_bf16 v[96:99], v[168:171], v[180:183], v[96:99]\n"
    "ds_read_b64_tr_b16 v[164:165], v206 offset:0\n"
    "v_mfma_f32_16x16x32_bf16 v[112:115], v[172:175], v[180:183], v[112:115]\n"
    "ds_read_b64_tr_b16 v[166:167], v206 offset:2048\n"
    "v_mfma_f32_16x16x32_bf16 v[128:131], v[176:179], v[180:183], v[128:131]\n"
    "buffer_load_dwordx4 v213, s[20:23], s41 offen offset:1024 lds\n"
    "v_mfma_f32_16x16x32_bf16 v[100:103], v[168:171], v[184:187], v[100:103]\n"
    "ds_read_b64_tr_b16 v[180:181], v208 offset:0\n"
    "v_mfma_f32_16x16x32_bf16 v[116:119], v[172:175], v[184:187], v[116:119]\n"
    "ds_read_b64_tr_b16 v[182:183], v208 offset:2048\n"
    "v_mfma_f32_16x16x32_bf16 v[132:135], v[176:179], v[184:187], v[132:135]\n"
    "v_mfma_f32_16x16x32_bf16 v[104:107], v[168:171], v[188:191], v[104:107]\n"
    "ds_read_b64_tr_b16 v[184:185], v209 offset:0\n"
    "v_mfma_f32_16x16x32_bf16 v[120:123], v[172:175], v[188:191], v[120:123]\n"
    "ds_read_b64_tr_b16 v[186:187], v209 offset:2048\n"
    "v_mfma_f32_16x16x32_bf16 v[136:139], v[176:179], v[188:191], v[136:139]\n"
    "buffer_load_dwordx4 v213, s[20:23], s42 offen offset:2048 lds\n"
    "v_mfma_f32_16x16x32_bf16 v[108:111], v[168:171], v[192:195], v[108:111]\n"
    "ds_read_b64_tr_b16 v[188:189], v210 offset:0\n"
    "v_mfma_f32_16x16x32_bf16 v[124:127], v[172:175], v[192:195], v[124:127]\n"
    "ds_read_b64_tr_b16 v[190:191], v210 offset:2048\n"
    "v_mfma_f32_16x16x32_bf16 v[140:143], v[176:179], v[192:195], v[140:143]\n"
    "buffer_load_dwordx4 v[244:247], v252, s[36:39], s59 offen offset:128 sc1\n"
    "s_waitcnt lgkmcnt(4)\n"
    "v_mfma_f32_16x16x32_bf16 v[0:3], v[144:147], v[180:183], v[0:3]\n"
    "ds_read_b64_tr_b16 v[192:193], v211 offset:0\n"
    "v_mfma_f32_16x16x32_bf16 v[16:19], v[148:151], v[180:183], v[16:19]\n"
    "ds_read_b64_tr_b16 v[194:195], v211 offset:2048\n"
    "v_mfma_f32_16x16x32_bf16 v[32:35], v[152:155], v[180:183], v[32:35]\n"
    "buffer_load_dwordx4 v213, s[20:23], s43 offen offset:3072 lds\n"
    "ds_read_b64_tr_b16 v[168:169], v206 offset:16384\n"
    "v_mfma_f32_16x16x32_bf16 v[196:199], v[200:203], v[180:183], v[196:199]\n"
    "ds_read_b64_tr_b16 v[170:171], v206 offset:18432\n"
    "s_waitcnt lgkmcnt(6)\n"
    "v_mfma_f32_16x16x32_bf16 v[4:7], v[144:147], v[184:187], v[4:7]\n"
    "ds_read_b64_tr_b16 v[172:173], v207 offset:0\n"
    "v_mfma_f32_16x16x32_bf16 v[20:23], v[148:151], v[184:187], v[20:23]\n"
    "ds_read_b64_tr_b16 v[174:175], v207 offset:2048\n"
    "v_mfma_f32_16x16x32_bf16 v[36:39], v[152:155], v[184:187], v[36:39]\n"
    "ds_read_b64_tr_b16 v[176:177], v207 offset:16384\n"
    "s_waitcnt lgkmcnt(7)\n"
    "v_mfma_f32_16x16x32_bf16 v[8:11], v[144:147], v[188:191], v[8:11]\n"
    "ds_read_b64_tr_b16 v[178:179], v207 offset:18432\n"
    "s_add_u32 m0, s50, 90112\n"
    "s_cmp_eq_u32 s53, 0\n"
    "v_mfma_f32_16x16x32_bf16 v[24:27], v[148:151], v[188:191], v[24:27]\n"
    "s_cbranch_scc1 Lm86_%=\n"
    "buffer_load_dwordx4 v214, s[20:23], s40 offen lds\n"
    "Lm86_%=:\n"
    "v_mfma_f32_16x16x32_bf16 v[40:43], v[152:155], v[188:191], v[40:43]\n"
    "s_add_u32 s20, s20, 294912\n"
    "s_addc_u32 s21, s21, 0\n"
    "s_waitcnt lgkmcnt(6)\n"
    "v_mfma_f32_16x16x32_bf16 v[12:15], v[144:147], v[192:195], v[12:15]\n"
    "v_mfma_f32_16x16x32_bf16 v[28:31], v[148:151], v[192:195], v[28:31]\n"
    "v_mfma_f32_16x16x32_bf16 v[44:47], v[152:155], v[192:195], v[44:47]\n"
    "s_waitcnt vmcnt(7) lgkmcnt(0)\n"
    "s_barrier\n"
    "s_add_u32 s76, s76, 4096\n"
    "v_mfma_f32_16x16x32_bf16 v[48:51], v[156:159], v[180:183], v[48:51]\n"
    "ds_read_b64_tr_b16 v[144:145], v204 offset:45056\n"
    "v_mfma_f32_16x16x32_bf16 v[52:55], v[156:159], v[184:187], v[52:55]\n"
    "ds_read_b64_tr_b16 v[146:147], v204 offset:47104\n"
    "s_add_u32 m0, s49, 0\n"
    "v_mfma_f32_16x16x32_bf16 v[56:59], v[156:159], v[188:191], v[56:59]\n"
    "buffer_load_dwordx4 v216, s[28:31], s45 offen lds\n"
    "ds_read_b64_tr_b16 v[148:149], v204 offset:61440\n"
    "v_mfma_f32_16x16x32_bf16 v[60:63], v[156:159], v[192:195], v[60:63]\n"
    "ds_read_b64_tr_b16 v[150:151], v204 offset:63488\n"
    "v_mfma_f32_16x16x32_bf16 v[64:67], v[160:163], v[180:183], v[64:67]\n"
    "ds_read_b64_tr_b16 v[152:153], v212 offset:45056\n"
    "v_mfma_f32_16x16x32_bf16 v[68:71], v[160:163], v[184:187], v[68:71]\n"
    "ds_read_b64_tr_b16 v[154:155], v212 offset:47104\n"
    "v_mfma_f32_16x16x32_bf16 v[72:75], v[160:163], v[188:191], v[72:75]\n"
    "ds_read_b64_tr_b16 v[156:157], v205 offset:45056\n"
    "s_cbranch_vccnz Lh87_%=\n"
    "buffer_store_dwordx4 v[180:183], v219, s[32:35], s76 offen offset:0\n"
    "Lh87_%=:\n"
    "v_mfma_f32_16x16x32_bf16 v[76:79], v[160:163], v[192:195], v[76:79]\n"
    "ds_read_b64_tr_b16 v[158:159], v205 offset:47104\n"
    "v_mfma_f32_16x16x32_bf16 v[80:83], v[164:167], v[180:183], v[80:83]\n"
    "buffer_load_dwordx4 v216, s[28:31], s46 offen offset:1024 lds\n"
    "ds_read_b64_tr_b16 v[160:161], v205 offset:61440\n"
    "v_mfma_f32_16x16x32_bf16 v[84:87], v[164:167], v[184:187], v[84:87]\n"
    "ds_read_b64_tr_b16 v[162:163], v205 offset:63488\n"
    "v_mfma_f32_16x16x32_bf16 v[88:91], v[164:167], v[188:191], v[88:91]\n"
    "v_add_u32 v204, 90112, v204\n"
    "v_mfma_f32_16x16x32_bf16 v[92:95], v[164:167], v[192:195], v[92:95]\n"
    "buffer_load_dwordx4 v[248:251], v252, s[36:39], 0 offen offset:192 sc1\n"
    "v_add_u32 v212, 90112, v212\n"
    "v_add_u32 v205, 90112, v205\n"
    "v_mfma_f32_16x16x32_bf16 v[96:99], v[168:171], v[180:183], v[96:99]\n"
    "ds_read_b64_tr_b16 v[164:165], v206 offset:45056\n"
    "v_mfma_f32_16x16x32_bf16 v[112:115], v[172:175], v[180:183], v[112:115]\n"
    "ds_read_b64_tr_b16 v[166:167], v206 offset:47104\n"
    "v_mfma_f32_16x16x32_bf16 v[128:131], v[176:179], v[180:183], v[128:131]\n"
    "buffer_load_dwordx4 v216, s[28:31], s47 offen offset:2048 lds\n"
    "v_mfma_f32_16x16x32_bf16 v[100:103], v[168:171], v[184:187], v[100:103]\n"
    "ds_read_b64_tr_b16 v[180:181], v208 offset:45056\n"
    "v_mfma_f32_16x16x32_bf16 v[116:119], v[172:175], v[184:187], v[116:119]\n"
    "ds_read_b64_tr_b16 v[182:183], v208 offset:47104\n"
    "v_mfma_f32_16x16x32_bf16 v[132:135], v[176:179], v[184:187], v[132:135]\n"
    "v_add_u32 v208, 90112, v208\n"
    "v_mfma_f32_16x16x32_bf16 v[104:107], v[168:171], v[188:191], v[104:107]\n"
    "ds_read_b64_tr_b16 v[184:185], v209 offset:45056\n"
    "v_mfma_f32_16x16x32_bf16 v[120:123], v[172:175], v[188:191], v[120:123]\n"
    "ds_read_b64_tr_b16 v[186:187], v209 offset:47104\n"
    "v_mfma_f32_16x16x32_bf16 v[136:139], v[176:179], v[188:191], v[136:139]\n"
    "buffer_load_dwordx4 v216, s[28:31], s48 offen offset:3072 lds\n"
    "v_mfma_f32_16x16x32_bf16 v[108:111], v[168:171], v[192:195], v[108:111]\n"
    "ds_read_b64_tr_b16 v[188:189], v210 offset:45056\n"
    "v_mfma_f32_16x16x32_bf16 v[124:127], v[172:175], v[192:195], v[124:127]\n"
    "ds_read_b64_tr_b16 v[190:191], v210 offset:47104\n"
    "v_mfma_f32_16x16x32_bf16 v[140:143], v[176:179], v[192:195], v[140:143]\n"
    "buffer_load_dwordx4 v[252:255], v252, s[36:39], s59 offen offset:192 sc1\n"
    "v_add_u32 v209, 90112, v209\n"
    "v_add_u32 v210, 90112, v210\n"
    "s_waitcnt lgkmcnt(4)\n"
    "v_mfma_f32_16x16x32_bf16 v[0:3], v[144:147], v[180:183], v[0:3]\n"
    "ds_read_b64_tr_b16 v[192:193], v211 offset:45056\n"
    "v_mfma_f32_16x16x32_bf16 v[16:19], v[148:151], v[180:183], v[16:19]\n"
    "ds_read_b64_tr_b16 v[194:195], v211 offset:47104\n"
    "s_add_u32 m0, s50, 0\n"
    "s_cmp_eq_u32 s53, 0\n"
    "v_mfma_f32_16x16x32_bf16 v[32:35], v[152:155], v[180:183], v[32:35]\n"
    "s_cbranch_scc1 Lm88_%=\n"
    "buffer_load_dwordx4 v217, s[28:31], s45 offen lds\n"
    "Lm88_%=:\n"
    "ds_read_b64_tr_b16 v[168:169], v206 offset:61440\n"
    "v_mfma_f32_16x16x32_bf16 v[196:199], v[200:203], v[180:183], v[196:199]\n"
    "s_add_u32 s28, s28, 128\n"
    "s_addc_u32 s29, s29, 0\n"
    "ds_read_b64_tr_b16 v[170:171], v206 offset:63488\n"
    "s_waitcnt lgkmcnt(6)\n"
    "v_mfma_f32_16x16x32_bf16 v[4:7], v[144:147], v[184:187], v[4:7]\n"
    "ds_read_b64_tr_b16 v[172:173], v207 offset:45056\n"
    "v_mfma_f32_16x16x32_bf16 v[20:23], v[148:151], v[184:187], v[20:23]\n"
    "ds_read_b64_tr_b16 v[174:175], v207 offset:47104\n"
    "v_mfma_f32_16x16x32_bf16 v[36:39], v[152:155], v[184:187], v[36:39]\n"
    "ds_read_b64_tr_b16 v[176:177], v207 offset:61440\n"
    "s_waitcnt lgkmcnt(7)\n"
    "v_mfma_f32_16x16x32_bf16 v[8:11], v[144:147], v[188:191], v[8:11]\n"
    "ds_read_b64_tr_b16 v[178:179], v207 offset:63488\n"
    "v_mfma_f32_16x16x32_bf16 v[24:27], v[148:151], v[188:191], v[24:27]\n"
    "v_mfma_f32_16x16x32_bf16 v[40:43], v[152:155], v[188:191], v[40:43]\n"
    "v_add_u32 v206, 90112, v206\n"
    "s_waitcnt lgkmcnt(6)\n"
    "v_mfma_f32_16x16x32_bf16 v[12:15], v[144:147], v[192:195], v[12:15]\n"
    "v_add_u32 v207, 90112, v207\n"
    "v_mfma_f32_16x16x32_bf16 v[28:31], v[148:151], v[192:195], v[28:31]\n"
    "v_add_u32 v211, 90112, v211\n"
    "v_mfma_f32_16x16x32_bf16 v[44:47], v[152:155], v[192:195], v[44:47]\n"
    "s_waitcnt vmcnt(6) lgkmcnt(0)\n"
    "s_barrier\n"
    "v_mfma_f32_16x16x32_bf16 v[48:51], v[156:159], v[180:183], v[48:51]\n"
    "ds_read_b64_tr_b16 v[144:145], v204 offset:0\n"
    "v_mfma_f32_16x16x32_bf16 v[52:55], v[156:159], v[184:187], v[52:55]\n"
    "ds_read_b64_tr_b16 v[146:147], v204 offset:2048\n"
    "s_add_u32 m0, s49, 45056\n"
    "v_mfma_f32_16x16x32_bf16 v[56:59], v[156:159], v[188:191], v[56:59]\n"
    "buffer_load_dwordx4 v216, s[28:31], s45 offen lds\n"
    "ds_read_b64_tr_b16 v[148:149], v204 offset:16384\n"
    "v_mfma_f32_16x16x32_bf16 v[60:63], v[156:159], v[192:195], v[60:63]\n"
    "ds_read_b64_tr_b16 v[150:151], v204 offset:18432\n"
    "v_mfma_f32_16x16x32_bf16 v[64:67], v[160:163], v[180:183], v[64:67]\n"
    "ds_read_b64_tr_b16 v[152:153], v212 offset:0\n"
    "v_mfma_f32_16x16x32_bf16 v[68:71], v[160:163], v[184:187], v[68:71]\n"
    "ds_read_b64_tr_b16 v[154:155], v212 offset:2048\n"
    "v_mfma_f32_16x16x32_bf16 v[72:75], v[160:163], v[188:191], v[72:75]\n"
    "ds_read_b64_tr_b16 v[156:157], v205 offset:0\n"
    "s_cbranch_vccnz Lh89_%=\n"
    "buffer_store_dwordx4 v[180:183], v219, s[32:35], s76 offen offset:256\n"
    "Lh89_%=:\n"
    "v_mfma_f32_16x16x32_bf16 v[76:79], v[160:163], v[192:195], v[76:79]\n"
    "ds_read_b64_tr_b16 v[158:159], v205 offset:2048\n"
    "v_mfma_f32_16x16x32_bf16 v[80:83], v[164:167], v[180:183], v[80:83]\n"
    "buffer_load_dwordx4 v216, s[28:31], s46 offen offset:1024 lds\n"
    "ds_read_b64_tr_b16 v[160:161], v205 offset:16384\n"
    "v_mfma_f32_16x16x32_bf16 v[84:87], v[164:167], v[184:187], v[84:87]\n"
    "ds_read_b64_tr_b16 v[162:163], v205 offset:18432\n"
    "v_mfma_f32_16x16x32_bf16 v[88:91], v[164:167], v[188:191], v[88:91]\n"
    "v_mfma_f32_16x16x32_bf16 v[92:95], v[164:167], v[192:195], v[92:95]\n"
    "v_mfma_f32_16x16x32_bf16 v[96:99], v[168:171], v[180:183], v[96:99]\n"
    "ds_read_b64_tr_b16 v[164:165], v206 offset:0\n"
    "v_mfma_f32_16x16x32_bf16 v[112:115], v[172:175], v[180:183], v[112:115]\n"
    "ds_read_b64_tr_b16 v[166:167], v206 offset:2048\n"
    "v_mfma_f32_16x16x32_bf16 v[128:131], v[176:179], v[180:183], v[128:131]\n"
    "buffer_load_dwordx4 v216, s[28:31], s47 offen offset:2048 lds\n"
    "v_mfma_f32_16x16x32_bf16 v[100:103], v[168:171], v[184:187], v[100:103]\n"
    "ds_read_b64_tr_b16 v[180:181], v208 offset:0\n"
    "v_mfma_f32_16x16x32_bf16 v[116:119], v[172:175], v[184:187], v[116:119]\n"
    "ds_read_b64_tr_b16 v[182:183], v208 offset:2048\n"
    "v_mfma_f32_16x16x32_bf16 v[132:135], v[176:179], v[184:187], v[132:135]\n"
    "v_mfma_f32_16x16x32_bf16 v[104:107], v[168:171], v[188:191], v[104:107]\n"
    "ds_read_b64_tr_b16 v[184:185], v209 offset:0\n"
    "v_mfma_f32_16x16x32_bf16 v[120:123], v[172:175], v[188:191], v[120:123]\n"
    "ds_read_b64_tr_b16 v[186:187], v209 offset:2048\n"
    "v_mfma_f32_16x16x32_bf16 v[136:139], v[176:179], v[188:191], v[136:139]\n"
    "buffer_load_dwordx4 v216, s[28:31], s48 offen offset:3072 lds\n"
    "v_mfma_f32_16x16x32_bf16 v[108:111], v[168:171], v[192:195], v[108:111]\n"
    "ds_read_b64_tr_b16 v[188:189], v210 offset:0\n"
    "v_mfma_f32_16x16x32_bf16 v[124:127], v[172:175], v[192:195], v[124:127]\n"
    "ds_read_b64_tr_b16 v[190:191], v210 offset:2048\n"
    "v_mfma_f32_16x16x32_bf16 v[140:143], v[176:179], v[192:195], v[140:143]\n"
    "s_waitcnt lgkmcnt(4)\n"
    "v_mfma_f32_16x16x32_bf16 v[196:199], v[200:203], v[180:183], v[196:199]\n"
    "ds_read_b64_tr_b16 v[192:193], v211 offset:0\n"
    "v_mfma_f32_16x16x32_bf16 v[0:3], v[144:147], v[180:183], v[0:3]\n"
    "ds_read_b64_tr_b16 v[194:195], v211 offset:2048\n"
    "s_add_u32 m0, s50, 45056\n"
    "s_cmp_eq_u32 s53, 0\n"
    "v_mfma_f32_16x16x32_bf16 v[16:19], v[148:151], v[180:183], v[16:19]\n"
    "s_cbranch_scc1 Lm90_%=\n"
    "buffer_load_dwordx4 v217, s[28:31], s45 offen lds\n"
    "Lm90_%=:\n"
    "ds_read_b64_tr_b16 v[168:169], v206 offset:16384\n"
    "v_mfma_f32_16x16x32_bf16 v[32:35], v[152:155], v[180:183], v[32:35]\n"
    "s_add_u32 s28, s28, 128\n"
    "s_addc_u32 s29, s29, 0\n"
    "ds_read_b64_tr_b16 v[170:171], v206 offset:18432\n"
    "s_waitcnt lgkmcnt(6)\n"
    "v_mfma_f32_16x16x32_bf16 v[4:7], v[144:147], v[184:187], v[4:7]\n"
    "ds_read_b64_tr_b16 v[172:173], v207 offset:0\n"
    "v_mfma_f32_16x16x32_bf16 v[20:23], v[148:151], v[184:187], v[20:23]\n"
    "ds_read_b64_tr_b16 v[174:175], v207 offset:2048\n"
    "v_mfma_f32_16x16x32_bf16 v[36:39], v[152:155], v[184:187], v[36:39]\n"
    "ds_read_b64_tr_b16 v[176:177], v207 offset:16384\n"
    "s_waitcnt lgkmcnt(7)\n"
    "v_mfma_f32_16x16x32_bf16 v[8:11], v[144:147], v[188:191], v[8:11]\n"
    "ds_read_b64_tr_b16 v[178:179], v207 offset:18432\n"
    "v_mfma_f32_16x16x32_bf16 v[24:27], v[148:151], v[188:191], v[24:27]\n"
    "v_mfma_f32_16x16x32_bf16 v[40:43], v[152:155], v[188:191], v[40:43]\n"
    "v_mul_f32 v213, 0x3ae38e39, v196\n"
    "v_mbcnt_lo_u32_b32 v214, -1, 0\n"
    "v_mbcnt_hi_u32_b32 v214, -1, v214\n"
    "v_cvt_pk_bf16_f32 v213, v213, v213\n"
    "v_and_b32 v215, 15, v214\n"
    "v_lshlrev_b32 v213, 16, v213\n"
    "s_lshl_b32 s56, s80, 6\n"
    "s_add_u32 s56, s56, 36864\n"
    "v_xor_b32 v213, 0x80000000, v213\n"
    "v_lshl_add_u32 v215, v215, 2, s56\n"
    "ds_write_b32 v215, v213\n"
    "s_waitcnt lgkmcnt(7)\n"
    "v_mfma_f32_16x16x32_bf16 v[12:15], v[144:147], v[192:195], v[12:15]\n"
    "v_mfma_f32_16x16x32_bf16 v[28:31], v[148:151], v[192:195], v[28:31]\n"
    "v_mfma_f32_16x16x32_bf16 v[44:47], v[152:155], v[192:195], v[44:47]\n"
    "s_waitcnt vmcnt(4) lgkmcnt(0)\n"
    "s_barrier\n"
    "ds_read_b32 v200, v215\n"
    "v_xor_b32 v209, 64, v215\n"
    "ds_read_b32 v201, v209\n"
    "v_xor_b32 v210, 128, v215\n"
    "ds_read_b32 v202, v210\n"
    "v_xor_b32 v211, 192, v215\n"
    "ds_read_b32 v203, v211\n"
    "v_mfma_f32_16x16x32_bf16 v[48:51], v[156:159], v[180:183], v[48:51]\n"
    "ds_read_b64_tr_b16 v[144:145], v218 offset:0\n"
    "s_cbranch_vccnz Lh91_%=\n"
    "buffer_store_dwordx4 v[180:183], v219, s[32:35], s76 offen offset:512\n"
    "Lh91_%=:\n"
    "v_mfma_f32_16x16x32_bf16 v[52:55], v[156:159], v[184:187], v[52:55]\n"
    "ds_read_b64_tr_b16 v[146:147], v218 offset:2048\n"
    "s_lshr_b32 s56, s80, 2\n"
    "s_and_b32 s57, s80, 3\n"
    "s_mul_i32 s70, s57, 0x4400\n"
    "v_mfma_f32_16x16x32_bf16 v[56:59], v[156:159], v[188:191], v[56:59]\n"
    "ds_read_b64_tr_b16 v[148:149], v218 offset:4096\n"
    "s_lshl_b32 s71, s56, 12\n"
    "s_add_i32 s70, s71, s70\n"
    "v_lshlrev_b32_e32 v213, 4, v214\n"
    "v_mfma_f32_16x16x32_bf16 v[60:63], v[156:159], v[192:195], v[60:63]\n"
    "ds_read_b64_tr_b16 v[150:151], v218 offset:6144\n"
    "v_add_u32_e32 v215, s70, v213\n"
    "v_add_u32_e32 v204, 0x16000, v215\n"
    "v_lshrrev_b32_e32 v215, 5, v214\n"
    "v_mfma_f32_16x16x32_bf16 v[64:67], v[160:163], v[180:183], v[64:67]\n"
    "ds_read_b64_tr_b16 v[152:153], v218 offset:8192\n"
    "v_bitop3_b32 v215, s80, v215, 3 bitop3:0x6c\n"
    "v_and_b32_e32 v196, 0xf0, v213\n"
    "v_lshl_or_b32 v215, v215, 10, v196\n"
    "v_mfma_f32_16x16x32_bf16 v[68:71], v[160:163], v[184:187], v[68:71]\n"
    "ds_read_b64_tr_b16 v[154:155], v218 offset:10240\n"
    "v_lshrrev_b32_e32 v196, 1, v214\n"
    "s_lshr_b32 s74, s81, 3\n"
    "s_lshl_b32 s57, s57, 8\n"
    "v_mfma_f32_16x16x32_bf16 v[72:75], v[160:163], v[188:191], v[72:75]\n"
    "ds_read_b64_tr_b16 v[156:157], v218 offset:12288\n"
    "v_and_b32_e32 v196, 8, v196\n"
    "s_and_b32 s74, s74, 30\n"
    "v_or3_b32 v215, v215, v196, s57\n"
    "v_mfma_f32_16x16x32_bf16 v[76:79], v[160:163], v[192:195], v[76:79]\n"
    "ds_read_b64_tr_b16 v[158:159], v218 offset:14336\n"
    "s_lshr_b32 s72, s77, s56\n"
    "s_or_b32 s56, s74, s56\n"
    "v_lshlrev_b32_e32 v196, 2, v214\n"
    "v_mfma_f32_16x16x32_bf16 v[80:83], v[164:167], v[180:183], v[80:83]\n"
    "ds_read_b64_tr_b16 v[160:161], v218 offset:16384\n"
    "v_and_b32_e32 v197, 48, v213\n"
    "s_movk_i32 s74, 0xffc0\n"
    "s_lshr_b32 s70, s77, 2\n"
    "v_mfma_f32_16x16x32_bf16 v[84:87], v[164:167], v[184:187], v[84:87]\n"
    "ds_read_b64_tr_b16 v[162:163], v218 offset:18432\n"
    "v_and_or_b32 v196, v196, s74, v197\n"
    "v_bfe_u32 v197, v214, 2, 2\n"
    "v_add_u32_e32 v197, s70, v197\n"
    "v_mfma_f32_16x16x32_bf16 v[88:91], v[164:167], v[188:191], v[88:91]\n"
    "v_and_b32_e32 v198, 3, v197\n"
    "v_lshrrev_b32_e32 v197, 2, v197\n"
    "v_xor_b32_e32 v197, s72, v197\n"
    "v_mfma_f32_16x16x32_bf16 v[92:95], v[164:167], v[192:195], v[92:95]\n"
    "s_lshl_b32 s72, s81, 12\n"
    "s_lshl_b32 s73, s81, 1\n"
    "s_lshl_b32 s56, s56, 21\n"
    "v_mfma_f32_16x16x32_bf16 v[96:99], v[168:171], v[180:183], v[96:99]\n"
    "ds_read_b64_tr_b16 v[164:165], v218 offset:20480\n"
    "s_and_b32 s72, s72, 0x4000\n"
    "s_bfe_u32 s75, s81, 0x10003\n"
    "s_lshl_b32 s74, s81, 9\n"
    "v_mfma_f32_16x16x32_bf16 v[112:115], v[172:175], v[180:183], v[112:115]\n"
    "ds_read_b64_tr_b16 v[166:167], v218 offset:22528\n"
    "s_or_b32 s56, s56, s72\n"
    "s_and_b32 s72, s73, 6\n"
    "s_and_b32 s74, s74, 0x400\n"
    "v_mfma_f32_16x16x32_bf16 v[128:131], v[176:179], v[180:183], v[128:131]\n"
    "s_or_b32 s72, s72, s75\n"
    "s_lshl_b32 s72, s72, 10\n"
    "v_or_b32_e32 v213, s74, v213\n"
    "v_mfma_f32_16x16x32_bf16 v[100:103], v[168:171], v[184:187], v[100:103]\n"
    "v_mul_u32_u24_e32 v198, 0x48000, v198\n"
    "v_lshlrev_b32_e32 v197, 13, v197\n"
    "s_movk_i32 s70, 0x2000\n"
    "v_mfma_f32_16x16x32_bf16 v[116:119], v[172:175], v[184:187], v[116:119]\n"
    "s_or_b32 s56, s56, s72\n"
    "v_or_b32_e32 v213, s71, v213\n"
    "v_or_b32_e32 v215, s71, v215\n"
    "v_mfma_f32_16x16x32_bf16 v[132:135], v[176:179], v[184:187], v[132:135]\n"
    "v_and_or_b32 v197, v197, s70, v198\n"
    "s_or_b32 s58, s56, s57\n"
    "s_movk_i32 s56, 0x800\n"
    "v_mfma_f32_16x16x32_bf16 v[104:107], v[168:171], v[188:191], v[104:107]\n"
    "v_or_b32_e32 v208, 0x16000, v213\n"
    "v_or_b32_e32 v209, 0x26000, v213\n"
    "v_mov_b32_e32 v213, 0x26000\n"
    "v_mfma_f32_16x16x32_bf16 v[120:123], v[172:175], v[188:191], v[120:123]\n"
    "v_or_b32_e32 v210, 0x26000, v215\n"
    "v_or_b32_e32 v219, v197, v196\n"
    "v_xor_b32_e32 v205, 0x400, v204\n"
    "v_mfma_f32_16x16x32_bf16 v[136:139], v[176:179], v[188:191], v[136:139]\n"
    "v_xor_b32_e32 v206, 0x800, v204\n"
    "v_xor_b32_e32 v207, 0xc00, v204\n"
    "v_bitop3_b32 v211, v215, s56, v213 bitop3:0x36\n"
    "v_mfma_f32_16x16x32_bf16 v[108:111], v[168:171], v[192:195], v[108:111]\n"
    "v_bitop3_b32 v212, v197, s70, v196 bitop3:0x36\n"
    "v_xor_b32 v198, 1024, v208\n"
    "v_xor_b32 v199, 1024, v209\n"
    "v_mfma_f32_16x16x32_bf16 v[124:127], v[172:175], v[192:195], v[124:127]\n"
    "v_mfma_f32_16x16x32_bf16 v[140:143], v[176:179], v[192:195], v[140:143]\n"
    "ds_read_b64_tr_b16 v[168:169], v218 offset:24576\n"
    "ds_read_b64_tr_b16 v[170:171], v218 offset:26624\n"
    "ds_read_b64_tr_b16 v[172:173], v218 offset:28672\n"
    "ds_read_b64_tr_b16 v[174:175], v218 offset:30720\n"
    "ds_read_b64_tr_b16 v[176:177], v218 offset:32768\n"
    "ds_read_b64_tr_b16 v[178:179], v218 offset:34816\n"
    "s_waitcnt lgkmcnt(15)\n"
    "v_pk_fma_f32 v[32:33], v[200:201], v[228:229], v[32:33] op_sel:[0,0,0] op_sel_hi:[0,1,1]\n"
    "v_pk_fma_f32 v[34:35], v[200:201], v[230:231], v[34:35] op_sel:[0,0,0] op_sel_hi:[0,1,1]\n"
    "v_pk_fma_f32 v[36:37], v[200:201], v[228:229], v[36:37] op_sel:[1,0,0] op_sel_hi:[1,1,1]\n"
    "v_pk_fma_f32 v[38:39], v[200:201], v[230:231], v[38:39] op_sel:[1,0,0] op_sel_hi:[1,1,1]\n"
    "s_nop 0\n"
    "v_permlane32_swap_b32 v32, v36\n"
    "v_permlane32_swap_b32 v33, v37\n"
    "v_permlane32_swap_b32 v34, v38\n"
    "v_permlane32_swap_b32 v35, v39\n"
    "s_nop 1\n"
    "v_pk_mul_f32 v[32:33], v[32:33], v[36:37]\n"
    "v_pk_mul_f32 v[34:35], v[34:35], v[38:39]\n"
    "v_cvt_pk_bf16_f32 v36, v32, v33\n"
    "v_cvt_pk_bf16_f32 v37, v34, v35\n"
    "ds_write_b64 v210, v[36:37]\n"
    "v_pk_fma_f32 v[40:41], v[202:203], v[228:229], v[40:41] op_sel:[0,0,0] op_sel_hi:[0,1,1]\n"
    "v_pk_fma_f32 v[42:43], v[202:203], v[230:231], v[42:43] op_sel:[0,0,0] op_sel_hi:[0,1,1]\n"
    "v_pk_fma_f32 v[44:45], v[202:203], v[228:229], v[44:45] op_sel:[1,0,0] op_sel_hi:[1,1,1]\n"
    "v_pk_fma_f32 v[46:47], v[202:203], v[230:231], v[46:47] op_sel:[1,0,0] op_sel_hi:[1,1,1]\n"
    "s_nop 0\n"
    "v_permlane32_swap_b32 v40, v44\n"
    "v_permlane32_swap_b32 v41, v45\n"
    "v_permlane32_swap_b32 v42, v46\n"
    "v_permlane32_swap_b32 v43, v47\n"
    "s_nop 1\n"
    "v_pk_mul_f32 v[40:41], v[40:41], v[44:45]\n"
    "v_pk_mul_f32 v[42:43], v[42:43], v[46:47]\n"
    "v_cvt_pk_bf16_f32 v44, v40, v41\n"
    "v_cvt_pk_bf16_f32 v45, v42, v43\n"
    "ds_write_b64 v211, v[44:45]\n"
    "v_pk_fma_f32 v[0:1], v[200:201], v[220:221], v[0:1] op_sel:[0,0,0] op_sel_hi:[0,1,1]\n"
    "v_pk_fma_f32 v[2:3], v[200:201], v[222:223], v[2:3] op_sel:[0,0,0] op_sel_hi:[0,1,1]\n"
    "v_pk_fma_f32 v[16:17], v[200:201], v[224:225], v[16:17] op_sel:[0,0,0] op_sel_hi:[0,1,1]\n"
    "v_pk_fma_f32 v[18:19], v[200:201], v[226:227], v[18:19] op_sel:[0,0,0] op_sel_hi:[0,1,1]\n"
    "v_pk_mul_f32 v[0:1], v[0:1], v[16:17]\n"
    "v_pk_mul_f32 v[2:3], v[2:3], v[18:19]\n"
    "v_cvt_pk_bf16_f32 v16, v0, v1\n"
    "v_cvt_pk_bf16_f32 v17, v2, v3\n"
    "v_pk_fma_f32 v[48:49], v[200:201], v[232:233], v[48:49] op_sel:[0,0,0] op_sel_hi:[0,1,1]\n"
    "v_pk_fma_f32 v[50:51], v[200:201], v[234:235], v[50:51] op_sel:[0,0,0] op_sel_hi:[0,1,1]\n"
    "v_pk_fma_f32 v[64:65], v[200:201], v[236:237], v[64:65] op_sel:[0,0,0] op_sel_hi:[0,1,1]\n"
    "v_pk_fma_f32 v[66:67], v[200:201], v[238:239], v[66:67] op_sel:[0,0,0] op_sel_hi:[0,1,1]\n"
    "v_pk_mul_f32 v[48:49], v[48:49], v[64:65]\n"
    "v_pk_mul_f32 v[50:51], v[50:51], v[66:67]\n"
    "v_cvt_pk_bf16_f32 v18, v48, v49\n"
    "v_cvt_pk_bf16_f32 v19, v50, v51\n"
    "ds_write_b128 v204, v[16:19] offset:0\n"
    "v_pk_fma_f32 v[4:5], v[200:201], v[220:221], v[4:5] op_sel:[1,0,0] op_sel_hi:[1,1,1]\n"
    "v_pk_fma_f32 v[6:7], v[200:201], v[222:223], v[6:7] op_sel:[1,0,0] op_sel_hi:[1,1,1]\n"
    "v_pk_fma_f32 v[20:21], v[200:201], v[224:225], v[20:21] op_sel:[1,0,0] op_sel_hi:[1,1,1]\n"
    "v_pk_fma_f32 v[22:23], v[200:201], v[226:227], v[22:23] op_sel:[1,0,0] op_sel_hi:[1,1,1]\n"
    "v_pk_mul_f32 v[4:5], v[4:5], v[20:21]\n"
    "v_pk_mul_f32 v[6:7], v[6:7], v[22:23]\n"
    "v_cvt_pk_bf16_f32 v20, v4, v5\n"
    "v_cvt_pk_bf16_f32 v21, v6, v7\n"
    "v_pk_fma_f32 v[52:53], v[200:201], v[232:233], v[52:53] op_sel:[1,0,0] op_sel_hi:[1,1,1]\n"
    "v_pk_fma_f32 v[54:55], v[200:201], v[234:235], v[54:55] op_sel:[1,0,0] op_sel_hi:[1,1,1]\n"
    "v_pk_fma_f32 v[68:69], v[200:201], v[236:237], v[68:69] op_sel:[1,0,0] op_sel_hi:[1,1,1]\n"
    "v_pk_fma_f32 v[70:71], v[200:201], v[238:239], v[70:71] op_sel:[1,0,0] op_sel_hi:[1,1,1]\n"
    "v_pk_mul_f32 v[52:53], v[52:53], v[68:69]\n"
    "v_pk_mul_f32 v[54:55], v[54:55], v[70:71]\n"
    "v_cvt_pk_bf16_f32 v22, v52, v53\n"
    "v_cvt_pk_bf16_f32 v23, v54, v55\n"
    "ds_write_b128 v205, v[20:23] offset:0\n"
    "v_pk_fma_f32 v[8:9], v[202:203], v[220:221], v[8:9] op_sel:[0,0,0] op_sel_hi:[0,1,1]\n"
    "v_pk_fma_f32 v[10:11], v[202:203], v[222:223], v[10:11] op_sel:[0,0,0] op_sel_hi:[0,1,1]\n"
    "v_pk_fma_f32 v[24:25], v[202:203], v[224:225], v[24:25] op_sel:[0,0,0] op_sel_hi:[0,1,1]\n"
    "v_pk_fma_f32 v[26:27], v[202:203], v[226:227], v[26:27] op_sel:[0,0,0] op_sel_hi:[0,1,1]\n"
    "v_pk_mul_f32 v[8:9], v[8:9], v[24:25]\n"
    "v_pk_mul_f32 v[10:11], v[10:11], v[26:27]\n"
    "v_cvt_pk_bf16_f32 v24, v8, v9\n"
    "v_cvt_pk_bf16_f32 v25, v10, v11\n"
    "v_pk_fma_f32 v[56:57], v[202:203], v[232:233], v[56:57] op_sel:[0,0,0] op_sel_hi:[0,1,1]\n"
    "v_pk_fma_f32 v[58:59], v[202:203], v[234:235], v[58:59] op_sel:[0,0,0] op_sel_hi:[0,1,1]\n"
    "v_pk_fma_f32 v[72:73], v[202:203], v[236:237], v[72:73] op_sel:[0,0,0] op_sel_hi:[0,1,1]\n"
    "v_pk_fma_f32 v[74:75], v[202:203], v[238:239], v[74:75] op_sel:[0,0,0] op_sel_hi:[0,1,1]\n"
    "v_pk_mul_f32 v[56:57], v[56:57], v[72:73]\n"
    "v_pk_mul_f32 v[58:59], v[58:59], v[74:75]\n"
    "v_cvt_pk_bf16_f32 v26, v56, v57\n"
    "v_cvt_pk_bf16_f32 v27, v58, v59\n"
    "ds_write_b128 v206, v[24:27] offset:0\n"
    "v_pk_fma_f32 v[12:13], v[202:203], v[220:221], v[12:13] op_sel:[1,0,0] op_sel_hi:[1,1,1]\n"
    "v_pk_fma_f32 v[14:15], v[202:203], v[222:223], v[14:15] op_sel:[1,0,0] op_sel_hi:[1,1,1]\n"
    "v_pk_fma_f32 v[28:29], v[202:203], v[224:225], v[28:29] op_sel:[1,0,0] op_sel_hi:[1,1,1]\n"
    "v_pk_fma_f32 v[30:31], v[202:203], v[226:227], v[30:31] op_sel:[1,0,0] op_sel_hi:[1,1,1]\n"
    "v_pk_mul_f32 v[12:13], v[12:13], v[28:29]\n"
    "v_pk_mul_f32 v[14:15], v[14:15], v[30:31]\n"
    "v_cvt_pk_bf16_f32 v28, v12, v13\n"
    "v_cvt_pk_bf16_f32 v29, v14, v15\n"
    "v_pk_fma_f32 v[60:61], v[202:203], v[232:233], v[60:61] op_sel:[1,0,0] op_sel_hi:[1,1,1]\n"
    "v_pk_fma_f32 v[62:63], v[202:203], v[234:235], v[62:63] op_sel:[1,0,0] op_sel_hi:[1,1,1]\n"
    "v_pk_fma_f32 v[76:77], v[202:203], v[236:237], v[76:77] op_sel:[1,0,0] op_sel_hi:[1,1,1]\n"
    "v_pk_fma_f32 v[78:79], v[202:203], v[238:239], v[78:79] op_sel:[1,0,0] op_sel_hi:[1,1,1]\n"
    "v_pk_mul_f32 v[60:61], v[60:61], v[76:77]\n"
    "v_pk_mul_f32 v[62:63], v[62:63], v[78:79]\n"
    "v_cvt_pk_bf16_f32 v30, v60, v61\n"
    "v_cvt_pk_bf16_f32 v31, v62, v63\n"
    "ds_write_b128 v207, v[28:31] offset:0\n"
    "v_pk_fma_f32 v[80:81], v[200:201], v[240:241], v[80:81] op_sel:[0,0,0] op_sel_hi:[0,1,1]\n"
    "v_pk_fma_f32 v[82:83], v[200:201], v[242:243], v[82:83] op_sel:[0,0,0] op_sel_hi:[0,1,1]\n"
    "v_pk_fma_f32 v[96:97], v[200:201], v[244:245], v[96:97] op_sel:[0,0,0] op_sel_hi:[0,1,1]\n"
    "v_pk_fma_f32 v[98:99], v[200:201], v[246:247], v[98:99] op_sel:[0,0,0] op_sel_hi:[0,1,1]\n"
    "v_pk_mul_f32 v[80:81], v[80:81], v[96:97]\n"
    "v_pk_mul_f32 v[82:83], v[82:83], v[98:99]\n"
    "v_cvt_pk_bf16_f32 v96, v80, v81\n"
    "v_cvt_pk_bf16_f32 v97, v82, v83\n"
    "v_pk_fma_f32 v[112:113], v[200:201], v[248:249], v[112:113] op_sel:[0,0,0] op_sel_hi:[0,1,1]\n"
    "v_pk_fma_f32 v[114:115], v[200:201], v[250:251], v[114:115] op_sel:[0,0,0] op_sel_hi:[0,1,1]\n"
    "v_pk_fma_f32 v[128:129], v[200:201], v[252:253], v[128:129] op_sel:[0,0,0] op_sel_hi:[0,1,1]\n"
    "v_pk_fma_f32 v[130:131], v[200:201], v[254:255], v[130:131] op_sel:[0,0,0] op_sel_hi:[0,1,1]\n"
    "v_pk_mul_f32 v[112:113], v[112:113], v[128:129]\n"
    "v_pk_mul_f32 v[114:115], v[114:115], v[130:131]\n"
    "v_cvt_pk_bf16_f32 v98, v112, v113\n"
    "v_cvt_pk_bf16_f32 v99, v114, v115\n"
    "ds_write_b128 v204, v[96:99] offset:8192\n"
    "v_pk_fma_f32 v[84:85], v[200:201], v[240:241], v[84:85] op_sel:[1,0,0] op_sel_hi:[1,1,1]\n"
    "v_pk_fma_f32 v[86:87], v[200:201], v[242:243], v[86:87] op_sel:[1,0,0] op_sel_hi:[1,1,1]\n"
    "v_pk_fma_f32 v[100:101], v[200:201], v[244:245], v[100:101] op_sel:[1,0,0] op_sel_hi:[1,1,1]\n"
    "v_pk_fma_f32 v[102:103], v[200:201], v[246:247], v[102:103] op_sel:[1,0,0] op_sel_hi:[1,1,1]\n"
    "v_pk_mul_f32 v[84:85], v[84:85], v[100:101]\n"
    "v_pk_mul_f32 v[86:87], v[86:87], v[102:103]\n"
    "v_cvt_pk_bf16_f32 v100, v84, v85\n"
    "v_cvt_pk_bf16_f32 v101, v86, v87\n"
    "v_pk_fma_f32 v[116:117], v[200:201], v[248:249], v[116:117] op_sel:[1,0,0] op_sel_hi:[1,1,1]\n"
    "v_pk_fma_f32 v[118:119], v[200:201], v[250:251], v[118:119] op_sel:[1,0,0] op_sel_hi:[1,1,1]\n"
    "v_pk_fma_f32 v[132:133], v[200:201], v[252:253], v[132:133] op_sel:[1,0,0] op_sel_hi:[1,1,1]\n"
    "v_pk_fma_f32 v[134:135], v[200:201], v[254:255], v[134:135] op_sel:[1,0,0] op_sel_hi:[1,1,1]\n"
    "v_pk_mul_f32 v[116:117], v[116:117], v[132:133]\n"
    "v_pk_mul_f32 v[118:119], v[118:119], v[134:135]\n"
    "v_cvt_pk_bf16_f32 v102, v116, v117\n"
    "v_cvt_pk_bf16_f32 v103, v118, v119\n"
    "ds_write_b128 v205, v[100:103] offset:8192\n"
    "v_pk_fma_f32 v[88:89], v[202:203], v[240:241], v[88:89] op_sel:[0,0,0] op_sel_hi:[0,1,1]\n"
    "v_pk_fma_f32 v[90:91], v[202:203], v[242:243], v[90:91] op_sel:[0,0,0] op_sel_hi:[0,1,1]\n"
    "v_pk_fma_f32 v[104:105], v[202:203], v[244:245], v[104:105] op_sel:[0,0,0] op_sel_hi:[0,1,1]\n"
    "v_pk_fma_f32 v[106:107], v[202:203], v[246:247], v[106:107] op_sel:[0,0,0] op_sel_hi:[0,1,1]\n"
    "v_pk_mul_f32 v[88:89], v[88:89], v[104:105]\n"
    "v_pk_mul_f32 v[90:91], v[90:91], v[106:107]\n"
    "v_cvt_pk_bf16_f32 v104, v88, v89\n"
    "v_cvt_pk_bf16_f32 v105, v90, v91\n"
    "v_pk_fma_f32 v[120:121], v[202:203], v[248:249], v[120:121] op_sel:[0,0,0] op_sel_hi:[0,1,1]\n"
    "v_pk_fma_f32 v[122:123], v[202:203], v[250:251], v[122:123] op_sel:[0,0,0] op_sel_hi:[0,1,1]\n"
    "v_pk_fma_f32 v[136:137], v[202:203], v[252:253], v[136:137] op_sel:[0,0,0] op_sel_hi:[0,1,1]\n"
    "v_pk_fma_f32 v[138:139], v[202:203], v[254:255], v[138:139] op_sel:[0,0,0] op_sel_hi:[0,1,1]\n"
    "v_pk_mul_f32 v[120:121], v[120:121], v[136:137]\n"
    "v_pk_mul_f32 v[122:123], v[122:123], v[138:139]\n"
    "v_cvt_pk_bf16_f32 v106, v120, v121\n"
    "v_cvt_pk_bf16_f32 v107, v122, v123\n"
    "ds_write_b128 v206, v[104:107] offset:8192\n"
    "v_pk_fma_f32 v[92:93], v[202:203], v[240:241], v[92:93] op_sel:[1,0,0] op_sel_hi:[1,1,1]\n"
    "v_pk_fma_f32 v[94:95], v[202:203], v[242:243], v[94:95] op_sel:[1,0,0] op_sel_hi:[1,1,1]\n"
    "v_pk_fma_f32 v[108:109], v[202:203], v[244:245], v[108:109] op_sel:[1,0,0] op_sel_hi:[1,1,1]\n"
    "v_pk_fma_f32 v[110:111], v[202:203], v[246:247], v[110:111] op_sel:[1,0,0] op_sel_hi:[1,1,1]\n"
    "v_pk_mul_f32 v[92:93], v[92:93], v[108:109]\n"
    "v_pk_mul_f32 v[94:95], v[94:95], v[110:111]\n"
    "v_cvt_pk_bf16_f32 v108, v92, v93\n"
    "v_cvt_pk_bf16_f32 v109, v94, v95\n"
    "v_pk_fma_f32 v[124:125], v[202:203], v[248:249], v[124:125] op_sel:[1,0,0] op_sel_hi:[1,1,1]\n"
    "v_pk_fma_f32 v[126:127], v[202:203], v[250:251], v[126:127] op_sel:[1,0,0] op_sel_hi:[1,1,1]\n"
    "v_pk_fma_f32 v[140:141], v[202:203], v[252:253], v[140:141] op_sel:[1,0,0] op_sel_hi:[1,1,1]\n"
    "v_pk_fma_f32 v[142:143], v[202:203], v[254:255], v[142:143] op_sel:[1,0,0] op_sel_hi:[1,1,1]\n"
    "v_pk_mul_f32 v[124:125], v[124:125], v[140:141]\n"
    "v_pk_mul_f32 v[126:127], v[126:127], v[142:143]\n"
    "v_cvt_pk_bf16_f32 v110, v124, v125\n"
    "v_cvt_pk_bf16_f32 v111, v126, v127\n"
    "ds_write_b128 v207, v[108:111] offset:8192\n"
    "s_waitcnt vmcnt(0) lgkmcnt(0)\n"
    "s_barrier\n"
    "v_add_u32 v218, 45056, v218\n"
    "ds_read_b128 v[0:3], v208 offset:0\n"
    "ds_read_b128 v[4:7], v198 offset:0\n"
    "ds_read_b128 v[8:11], v208 offset:2048\n"
    "ds_read_b128 v[12:15], v198 offset:2048\n"
    "ds_read_b128 v[16:19], v208 offset:8192\n"
    "ds_read_b128 v[20:23], v198 offset:8192\n"
    "ds_read_b128 v[24:27], v208 offset:10240\n"
    "ds_read_b128 v[28:31], v198 offset:10240\n"
    "s_waitcnt lgkmcnt(7)\n"
    "v_mfma_f32_16x16x32_bf16 v[180:183], v[144:147], v[0:3], 0\n"
    "ds_read_b128 v[32:35], v208 offset:16384\n"
    "ds_read_b128 v[36:39], v198 offset:16384\n"
    "s_waitcnt lgkmcnt(8)\n"
    "v_mfma_f32_16x16x32_bf16 v[184:187], v[144:147], v[4:7], 0\n"
    "ds_read_b128 v[40:43], v208 offset:18432\n"
    "s_waitcnt lgkmcnt(8)\n"
    "v_mfma_f32_16x16x32_bf16 v[188:191], v[144:147], v[8:11], 0\n"
    "ds_read_b128 v[44:47], v198 offset:18432\n"
    "s_add_u32 m0, s49, 0\n"
    "s_waitcnt lgkmcnt(8)\n"
    "v_mfma_f32_16x16x32_bf16 v[192:195], v[144:147], v[12:15], 0\n"
    "buffer_load_dwordx4 v216, s[28:31], s45 offen lds\n"
    "ds_read_b128 v[48:51], v208 offset:24576\n"
    "s_waitcnt lgkmcnt(8)\n"
    "v_mfma_f32_16x16x32_bf16 v[180:183], v[148:151], v[16:19], v[180:183]\n"
    "ds_read_b128 v[52:55], v198 offset:24576\n"
    "ds_read_b128 v[56:59], v208 offset:26624\n"
    "s_waitcnt lgkmcnt(9)\n"
    "v_mfma_f32_16x16x32_bf16 v[184:187], v[148:151], v[20:23], v[184:187]\n"
    "ds_read_b128 v[60:63], v198 offset:26624\n"
    "s_waitcnt lgkmcnt(9)\n"
    "v_mfma_f32_16x16x32_bf16 v[188:191], v[148:151], v[24:27], v[188:191]\n"
    "ds_read_b128 v[64:67], v208 offset:32768\n"
    "s_waitcnt lgkmcnt(9)\n"
    "v_mfma_f32_16x16x32_bf16 v[192:195], v[148:151], v[28:31], v[192:195]\n"
    "buffer_load_dwordx4 v216, s[28:31], s46 offen offset:1024 lds\n"
    "ds_read_b128 v[68:71], v198 offset:32768\n"
    "s_waitcnt lgkmcnt(9)\n"
    "v_mfma_f32_16x16x32_bf16 v[180:183], v[152:155], v[32:35], v[180:183]\n"
    "ds_read_b128 v[72:75], v208 offset:34816\n"
    "ds_read_b128 v[76:79], v198 offset:34816\n"
    "s_waitcnt lgkmcnt(10)\n"
    "v_mfma_f32_16x16x32_bf16 v[184:187], v[152:155], v[36:39], v[184:187]\n"
    "ds_read_b128 v[80:83], v208 offset:40960\n"
    "s_waitcnt lgkmcnt(10)\n"
    "v_mfma_f32_16x16x32_bf16 v[188:191], v[152:155], v[40:43], v[188:191]\n"
    "ds_read_b128 v[84:87], v198 offset:40960\n"
    "s_waitcnt lgkmcnt(10)\n"
    "v_mfma_f32_16x16x32_bf16 v[192:195], v[152:155], v[44:47], v[192:195]\n"
    "buffer_load_dwordx4 v216, s[28:31], s47 offen offset:2048 lds\n"
    "ds_read_b128 v[88:91], v208 offset:43008\n"
    "s_waitcnt lgkmcnt(10)\n"
    "v_mfma_f32_16x16x32_bf16 v[180:183], v[156:159], v[48:51], v[180:183]\n"
    "ds_read_b128 v[92:95], v198 offset:43008\n"
    "ds_read_b128 v[96:99], v208 offset:49152\n"
    "s_waitcnt lgkmcnt(11)\n"
    "v_mfma_f32_16x16x32_bf16 v[184:187], v[156:159], v[52:55], v[184:187]\n"
    "ds_read_b128 v[100:103], v198 offset:49152\n"
    "s_waitcnt lgkmcnt(11)\n"
    "v_mfma_f32_16x16x32_bf16 v[188:191], v[156:159], v[56:59], v[188:191]\n"
    "ds_read_b128 v[104:107], v208 offset:51200\n"
    "s_waitcnt lgkmcnt(11)\n"
    "v_mfma_f32_16x16x32_bf16 v[192:195], v[156:159], v[60:63], v[192:195]\n"
    "buffer_load_dwordx4 v216, s[28:31], s48 offen offset:3072 lds\n"
    "ds_read_b128 v[108:111], v198 offset:51200\n"
    "s_waitcnt lgkmcnt(11)\n"
    "v_mfma_f32_16x16x32_bf16 v[180:183], v[160:163], v[64:67], v[180:183]\n"
    "ds_read_b128 v[112:115], v208 offset:57344\n"
    "ds_read_b128 v[116:119], v198 offset:57344\n"
    "s_waitcnt lgkmcnt(12)\n"
    "v_mfma_f32_16x16x32_bf16 v[184:187], v[160:163], v[68:71], v[184:187]\n"
    "ds_read_b128 v[120:123], v208 offset:59392\n"
    "s_waitcnt lgkmcnt(12)\n"
    "v_mfma_f32_16x16x32_bf16 v[188:191], v[160:163], v[72:75], v[188:191]\n"
    "ds_read_b128 v[124:127], v198 offset:59392\n"
    "s_add_u32 m0, s50, 0\n"
    "s_cmp_eq_u32 s53, 0\n"
    "s_waitcnt lgkmcnt(12)\n"
    "v_mfma_f32_16x16x32_bf16 v[192:195], v[160:163], v[76:79], v[192:195]\n"
    "s_cbranch_scc1 Lm92_%=\n"
    "buffer_load_dwordx4 v217, s[28:31], s45 offen lds\n"
    "Lm92_%=:\n"
    "ds_read_b128 v[128:131], v209 offset:0\n"
    "s_waitcnt lgkmcnt(12)\n"
    "v_mfma_f32_16x16x32_bf16 v[180:183], v[164:167], v[80:83], v[180:183]\n"
    "s_add_u32 s28, s28, 128\n"
    "s_addc_u32 s29, s29, 0\n"
    "ds_read_b128 v[132:135], v199 offset:0\n"
    "ds_read_b128 v[136:139], v209 offset:2048\n"
    "s_waitcnt lgkmcnt(13)\n"
    "v_mfma_f32_16x16x32_bf16 v[184:187], v[164:167], v[84:87], v[184:187]\n"
    "ds_read_b128 v[140:143], v199 offset:2048\n"
    "s_waitcnt lgkmcnt(13)\n"
    "v_mfma_f32_16x16x32_bf16 v[188:191], v[164:167], v[88:91], v[188:191]\n"
    "ds_read_b64_tr_b16 v[144:145], v218 offset:0\n"
    "s_waitcnt lgkmcnt(13)\n"
    "v_mfma_f32_16x16x32_bf16 v[192:195], v[164:167], v[92:95], v[192:195]\n"
    "ds_read_b64_tr_b16 v[146:147], v218 offset:2048\n"
    "s_waitcnt lgkmcnt(13)\n"
    "v_mfma_f32_16x16x32_bf16 v[180:183], v[168:171], v[96:99], v[180:183]\n"
    "ds_read_b64_tr_b16 v[148:149], v218 offset:4096\n"
    "s_waitcnt lgkmcnt(13)\n"
    "v_mfma_f32_16x16x32_bf16 v[184:187], v[168:171], v[100:103], v[184:187]\n"
    "ds_read_b64_tr_b16 v[150:151], v218 offset:6144\n"
    "s_waitcnt lgkmcnt(13)\n"
    "v_mfma_f32_16x16x32_bf16 v[188:191], v[168:171], v[104:107], v[188:191]\n"
    "ds_read_b64_tr_b16 v[152:153], v218 offset:8192\n"
    "s_waitcnt lgkmcnt(13)\n"
    "v_mfma_f32_16x16x32_bf16 v[192:195], v[168:171], v[108:111], v[192:195]\n"
    "s_waitcnt lgkmcnt(5)\n"
    "s_barrier\n"
    "ds_read_b64_tr_b16 v[154:155], v218 offset:10240\n"
    "v_mfma_f32_16x16x32_bf16 v[180:183], v[172:175], v[112:115], v[180:183]\n"
    "ds_read_b64_tr_b16 v[156:157], v218 offset:12288\n"
    "s_lshr_b32 s21, s80, 2\n"
    "s_add_u32 m0, s49, 90112\n"
    "v_mfma_f32_16x16x32_bf16 v[184:187], v[172:175], v[116:119], v[184:187]\n"
    "buffer_load_dwordx4 v216, s[28:31], s45 offen lds\n"
    "ds_read_b64_tr_b16 v[158:159], v218 offset:14336\n"
    "s_mul_i32 s20, s81, 0x2400\n"
    "v_mfma_f32_16x16x32_bf16 v[188:191], v[172:175], v[120:123], v[188:191]\n"
    "ds_read_b64_tr_b16 v[160:161], v218 offset:16384\n"
    "s_mulk_i32 s21, 0x1200\n"
    "v_mfma_f32_16x16x32_bf16 v[192:195], v[172:175], v[124:127], v[192:195]\n"
    "buffer_load_dwordx4 v216, s[28:31], s46 offen offset:1024 lds\n"
    "ds_read_b64_tr_b16 v[162:163], v218 offset:18432\n"
    "s_add_i32 s21, s21, s20\n"
    "v_mfma_f32_16x16x32_bf16 v[180:183], v[176:179], v[128:131], v[180:183]\n"
    "ds_read_b64_tr_b16 v[164:165], v218 offset:20480\n"
    "s_lshl_b32 s20, s80, 4\n"
    "v_mfma_f32_16x16x32_bf16 v[184:187], v[176:179], v[132:135], v[184:187]\n"
    "ds_read_b64_tr_b16 v[166:167], v218 offset:22528\n"
    "s_and_b32 s20, s20, 48\n"
    "v_mfma_f32_16x16x32_bf16 v[188:191], v[176:179], v[136:139], v[188:191]\n"
    "buffer_load_dwordx4 v216, s[28:31], s47 offen offset:2048 lds\n"
    "ds_read_b64_tr_b16 v[168:169], v218 offset:24576\n"
    "v_lshlrev_b32_e32 v252, 7, v214\n"
    "v_mfma_f32_16x16x32_bf16 v[192:195], v[176:179], v[140:143], v[192:195]\n"
    "ds_read_b64_tr_b16 v[170:171], v218 offset:26624\n"
    "v_lshlrev_b32_e32 v253, 2, v214\n"
    "s_waitcnt lgkmcnt(12)\n"
    "v_mfma_f32_16x16x32_bf16 v[220:223], v[144:147], v[0:3], 0\n"
    "ds_read_b64_tr_b16 v[172:173], v218 offset:28672\n"
    "v_lshlrev_b32_e32 v254, 3, v214\n"
    "v_mfma_f32_16x16x32_bf16 v[224:227], v[144:147], v[4:7], 0\n"
    "buffer_load_dwordx4 v216, s[28:31], s48 offen offset:3072 lds\n"
    "ds_read_b64_tr_b16 v[174:175], v218 offset:30720\n"
    "v_and_b32_e32 v252, 0x700, v252\n"
    "v_mfma_f32_16x16x32_bf16 v[228:231], v[144:147], v[8:11], 0\n"
    "ds_read_b64_tr_b16 v[176:177], v218 offset:32768\n"
    "v_and_b32_e32 v253, 0xc0, v253\n"
    "v_mfma_f32_16x16x32_bf16 v[232:235], v[144:147], v[12:15], 0\n"
    "ds_read_b64_tr_b16 v[178:179], v218 offset:34816\n"
    "v_subrev_u32 v218, 45056, v218\n"
    "v_and_b32_e32 v254, 8, v254\n"
    "s_add_u32 m0, s50, 90112\n"
    "s_cmp_eq_u32 s53, 0\n"
    "s_waitcnt lgkmcnt(14)\n"
    "v_mfma_f32_16x16x32_bf16 v[220:223], v[148:151], v[16:19], v[220:223]\n"
    "s_cbranch_scc1 Lm93_%=\n"
    "buffer_load_dwordx4 v217, s[28:31], s45 offen lds\n"
    "Lm93_%=:\n"
    "s_or_b32 s76, s21, s20\n"
    "v_mfma_f32_16x16x32_bf16 v[224:227], v[148:151], v[20:23], v[224:227]\n"
    "s_add_u32 s28, s28, 128\n"
    "s_addc_u32 s29, s29, 0\n"
    "v_or3_b32 v250, v252, v253, v254\n"
    "v_mfma_f32_16x16x32_bf16 v[228:231], v[148:151], v[24:27], v[228:231]\n"
    "s_add_i32 s76, s76, 0x4000000\n"
    "v_mfma_f32_16x16x32_bf16 v[232:235], v[148:151], v[28:31], v[232:235]\n"
    "s_waitcnt vmcnt(4) lgkmcnt(0)\n"
    "s_barrier\n"
    "v_mfma_f32_16x16x32_bf16 v[220:223], v[152:155], v[32:35], v[220:223]\n"
    "ds_read_b64_tr_b16 v[144:145], v218 offset:0\n"
    "v_mfma_f32_16x16x32_bf16 v[224:227], v[152:155], v[36:39], v[224:227]\n"
    "ds_read_b64_tr_b16 v[146:147], v218 offset:2048\n"
    "s_add_u32 m0, s49, 126976\n"
    "v_mfma_f32_16x16x32_bf16 v[228:231], v[152:155], v[40:43], v[228:231]\n"
    "buffer_load_dwordx4 v216, s[28:31], s45 offen lds\n"
    "ds_read_b64_tr_b16 v[148:149], v218 offset:4096\n"
    "v_mfma_f32_16x16x32_bf16 v[232:235], v[152:155], v[44:47], v[232:235]\n"
    "ds_read_b64_tr_b16 v[150:151], v218 offset:6144\n"
    "v_mfma_f32_16x16x32_bf16 v[220:223], v[156:159], v[48:51], v[220:223]\n"
    "ds_read_b64_tr_b16 v[152:153], v218 offset:8192\n"
    "v_mfma_f32_16x16x32_bf16 v[224:227], v[156:159], v[52:55], v[224:227]\n"
    "ds_read_b64_tr_b16 v[154:155], v218 offset:10240\n"
    "buffer_load_dwordx2 v[244:245], v250, s[32:35], s76 offen\n"
    "v_mov_b32 v248, 0\n"
    "s_add_u32 s76, s76, 2048\n"
    "v_mfma_f32_16x16x32_bf16 v[228:231], v[156:159], v[56:59], v[228:231]\n"
    "v_cvt_pk_bf16_f32 v236, v180, v181\n"
    "s_lshl_b32 s22, s80, 1\n"
    "v_mfma_f32_16x16x32_bf16 v[232:235], v[156:159], v[60:63], v[232:235]\n"
    "ds_read_b64_tr_b16 v[156:157], v218 offset:12288\n"
    "s_bfe_u32 s40, s81, 0x10003\n"
    "v_mfma_f32_16x16x32_bf16 v[220:223], v[160:163], v[64:67], v[220:223]\n"
    "buffer_load_dwordx4 v216, s[28:31], s46 offen offset:1024 lds\n"
    "ds_read_b64_tr_b16 v[158:159], v218 offset:14336\n"
    "v_bitop3_b32 v252, s22, v214, 2 bitop3:0x6c\n"
    "v_mfma_f32_16x16x32_bf16 v[224:227], v[160:163], v[68:71], v[224:227]\n"
    "v_cvt_pk_bf16_f32 v237, v182, v183\n"
    "v_mfma_f32_16x16x32_bf16 v[228:231], v[160:163], v[72:75], v[228:231]\n"
    "v_cvt_pk_bf16_f32 v238, v184, v185\n"
    "s_lshr_b32 s22, s77, 2\n"
    "v_mfma_f32_16x16x32_bf16 v[232:235], v[160:163], v[76:79], v[232:235]\n"
    "ds_read_b64_tr_b16 v[160:161], v218 offset:16384\n"
    "s_add_i32 s22, s22, s81\n"
    "buffer_load_dwordx2 v[246:247], v250, s[32:35], s76 offen\n"
    "v_mov_b32 v249, 0\n"
    "v_mfma_f32_16x16x32_bf16 v[220:223], v[164:167], v[80:83], v[220:223]\n"
    "ds_read_b64_tr_b16 v[162:163], v218 offset:18432\n"
    "v_mfma_f32_16x16x32_bf16 v[224:227], v[164:167], v[84:87], v[224:227]\n"
    "v_cvt_pk_bf16_f32 v239, v186, v187\n"
    "s_lshr_b32 s40, s77, s40\n"
    "v_mfma_f32_16x16x32_bf16 v[228:231], v[164:167], v[88:91], v[228:231]\n"
    "buffer_load_dwordx4 v216, s[28:31], s47 offen offset:2048 lds\n"
    "v_cvt_pk_bf16_f32 v240, v188, v189\n"
    "s_and_b32 s41, s22, 3\n"
    "v_mfma_f32_16x16x32_bf16 v[232:235], v[164:167], v[92:95], v[232:235]\n"
    "ds_read_b64_tr_b16 v[164:165], v218 offset:20480\n"
    "v_mfma_f32_16x16x32_bf16 v[220:223], v[168:171], v[96:99], v[220:223]\n"
    "ds_read_b64_tr_b16 v[166:167], v218 offset:22528\n"
    "s_lshr_b32 s22, s22, 2\n"
    "v_mfma_f32_16x16x32_bf16 v[224:227], v[168:171], v[100:103], v[224:227]\n"
    "buffer_store_dwordx4 v[236:239], v219, s[32:35], s58 offen sc1\n"
    "v_lshlrev_b32_e32 v197, 4, v252\n"
    "v_mfma_f32_16x16x32_bf16 v[228:231], v[168:171], v[104:107], v[228:231]\n"
    "v_cvt_pk_bf16_f32 v241, v190, v191\n"
    "s_lshr_b32 s23, s81, 3\n"
    "v_mfma_f32_16x16x32_bf16 v[232:235], v[168:171], v[108:111], v[232:235]\n"
    "ds_read_b64_tr_b16 v[168:169], v218 offset:24576\n"
    "v_mfma_f32_16x16x32_bf16 v[220:223], v[172:175], v[112:115], v[220:223]\n"
    "buffer_load_dwordx4 v216, s[28:31], s48 offen offset:3072 lds\n"
    "ds_read_b64_tr_b16 v[170:171], v218 offset:26624\n"
    "s_xor_b32 s22, s22, s40\n"
    "v_mfma_f32_16x16x32_bf16 v[224:227], v[172:175], v[116:119], v[224:227]\n"
    "v_cvt_pk_bf16_f32 v242, v192, v193\n"
    "v_lshrrev_b32_e32 v252, 2, v214\n"
    "v_mfma_f32_16x16x32_bf16 v[228:231], v[172:175], v[120:123], v[228:231]\n"
    "v_cvt_pk_bf16_f32 v243, v194, v195\n"
    "v_mfma_f32_16x16x32_bf16 v[232:235], v[172:175], v[124:127], v[232:235]\n"
    "ds_read_b64_tr_b16 v[172:173], v218 offset:28672\n"
    "s_lshl_b32 s23, s23, 21\n"
    "s_mov_b32 exec_lo, 0x000f000f\n"
    "s_mov_b32 exec_hi, 0x000f000f\n"
    "buffer_load_dwordx2 v[248:249], v250, s[32:35], s76 offen offset:2048\n"
    "s_mov_b64 exec, -1\n"
    "v_mfma_f32_16x16x32_bf16 v[220:223], v[176:179], v[128:131], v[220:223]\n"
    "ds_read_b64_tr_b16 v[174:175], v218 offset:30720\n"
    "s_mul_i32 s41, s41, 0x48000\n"
    "v_mfma_f32_16x16x32_bf16 v[224:227], v[176:179], v[132:135], v[224:227]\n"
    "s_add_u32 m0, s50, 126976\n"
    "s_cmp_eq_u32 s53, 0\n"
    "v_mfma_f32_16x16x32_bf16 v[228:231], v[176:179], v[136:139], v[228:231]\n"
    "s_cbranch_scc1 Lm94_%=\n"
    "buffer_load_dwordx4 v217, s[28:31], s45 offen lds\n"
    "Lm94_%=:\n"
    "s_lshl_b32 s22, s22, 13\n"
    "v_mfma_f32_16x16x32_bf16 v[232:235], v[176:179], v[140:143], v[232:235]\n"
    "s_add_u32 s28, s28, 128\n"
    "s_addc_u32 s29, s29, 0\n"
    "ds_read_b64_tr_b16 v[176:177], v218 offset:32768\n"
    "v_lshlrev_b32_e32 v253, 10, v252\n"
    "s_waitcnt lgkmcnt(15)\n"
    "v_mfma_f32_16x16x32_bf16 v[180:183], v[144:147], v[0:3], 0\n"
    "ds_read_b64_tr_b16 v[178:179], v218 offset:34816\n"
    "v_add_u32 v218, 90112, v218\n"
    "v_xor_b32_e32 v252, s80, v252\n"
    "v_mfma_f32_16x16x32_bf16 v[184:187], v[144:147], v[4:7], 0\n"
    "buffer_store_dwordx4 v[240:243], v212, s[32:35], s58 offen sc1\n"
    "s_add_u32 s58, s58, 32768\n"
    "v_mfma_f32_16x16x32_bf16 v[188:191], v[144:147], v[8:11], 0\n"
    "s_lshr_b32 s20, s80, 2\n"
    "v_mfma_f32_16x16x32_bf16 v[192:195], v[144:147], v[12:15], 0\n"
    "s_or_b32 s23, s41, s23\n"
    "s_waitcnt lgkmcnt(14)\n"
    "v_mfma_f32_16x16x32_bf16 v[180:183], v[148:151], v[16:19], v[180:183]\n"
    "v_mfma_f32_16x16x32_bf16 v[184:187], v[148:151], v[20:23], v[184:187]\n"
    "s_and_b32 s22, s22, 0x2000\n"
    "v_mfma_f32_16x16x32_bf16 v[188:191], v[148:151], v[24:27], v[188:191]\n"
    "v_lshlrev_b32_e32 v252, 4, v252\n"
    "v_mfma_f32_16x16x32_bf16 v[192:195], v[148:151], v[28:31], v[192:195]\n"
    "s_waitcnt vmcnt(9) lgkmcnt(0)\n"
    "s_barrier\n"
    "v_mfma_f32_16x16x32_bf16 v[180:183], v[152:155], v[32:35], v[180:183]\n"
    "ds_read_b64_tr_b16 v[144:145], v218 offset:0\n"
    "v_mfma_f32_16x16x32_bf16 v[184:187], v[152:155], v[36:39], v[184:187]\n"
    "ds_read_b64_tr_b16 v[146:147], v218 offset:2048\n"
    "s_add_u32 m0, s49, 45056\n"
    "v_mfma_f32_16x16x32_bf16 v[188:191], v[152:155], v[40:43], v[188:191]\n"
    "buffer_load_dwordx4 v216, s[28:31], s45 offen lds\n"
    "ds_read_b64_tr_b16 v[148:149], v218 offset:4096\n"
    "v_mfma_f32_16x16x32_bf16 v[192:195], v[152:155], v[44:47], v[192:195]\n"
    "ds_read_b64_tr_b16 v[150:151], v218 offset:6144\n"
    "v_mfma_f32_16x16x32_bf16 v[180:183], v[156:159], v[48:51], v[180:183]\n"
    "ds_read_b64_tr_b16 v[152:153], v218 offset:8192\n"
    "v_mfma_f32_16x16x32_bf16 v[184:187], v[156:159], v[52:55], v[184:187]\n"
    "ds_read_b64_tr_b16 v[154:155], v218 offset:10240\n"
    "v_mfma_f32_16x16x32_bf16 v[188:191], v[156:159], v[56:59], v[188:191]\n"
    "v_cvt_pk_bf16_f32 v236, v220, v221\n"
    "v_lshrrev_b32_e32 v254, 4, v214\n"
    "v_mfma_f32_16x16x32_bf16 v[192:195], v[156:159], v[60:63], v[192:195]\n"
    "ds_read_b64_tr_b16 v[156:157], v218 offset:12288\n"
    "s_and_b32 s21, s80, 3\n"
    "v_mfma_f32_16x16x32_bf16 v[180:183], v[160:163], v[64:67], v[180:183]\n"
    "buffer_load_dwordx4 v216, s[28:31], s46 offen offset:1024 lds\n"
    "ds_read_b64_tr_b16 v[158:159], v218 offset:14336\n"
    "s_or_b32 s22, s22, s23\n"
    "v_mfma_f32_16x16x32_bf16 v[184:187], v[160:163], v[68:71], v[184:187]\n"
    "v_cvt_pk_bf16_f32 v237, v222, v223\n"
    "v_mfma_f32_16x16x32_bf16 v[188:191], v[160:163], v[72:75], v[188:191]\n"
    "v_cvt_pk_bf16_f32 v238, v224, v225\n"
    "s_lshl_b32 s23, s20, 14\n"
    "v_mfma_f32_16x16x32_bf16 v[192:195], v[160:163], v[76:79], v[192:195]\n"
    "ds_read_b64_tr_b16 v[160:161], v218 offset:16384\n"
    "v_and_or_b32 v252, v252, 48, v253\n"
    "v_mfma_f32_16x16x32_bf16 v[180:183], v[164:167], v[80:83], v[180:183]\n"
    "ds_read_b64_tr_b16 v[162:163], v218 offset:18432\n"
    "v_mfma_f32_16x16x32_bf16 v[184:187], v[164:167], v[84:87], v[184:187]\n"
    "v_cvt_pk_bf16_f32 v239, v226, v227\n"
    "v_lshlrev_b32_e32 v253, 6, v214\n"
    "v_mfma_f32_16x16x32_bf16 v[188:191], v[164:167], v[88:91], v[188:191]\n"
    "buffer_load_dwordx4 v216, s[28:31], s47 offen offset:2048 lds\n"
    "v_cvt_pk_bf16_f32 v240, v228, v229\n"
    "v_bitop3_b32 v255, v254, s20, 1 bitop3:0x6c\n"
    "v_mfma_f32_16x16x32_bf16 v[192:195], v[164:167], v[92:95], v[192:195]\n"
    "ds_read_b64_tr_b16 v[164:165], v218 offset:20480\n"
    "v_mfma_f32_16x16x32_bf16 v[180:183], v[168:171], v[96:99], v[180:183]\n"
    "ds_read_b64_tr_b16 v[166:167], v218 offset:22528\n"
    "s_or_b32 s22, s22, s23\n"
    "v_mfma_f32_16x16x32_bf16 v[184:187], v[168:171], v[100:103], v[184:187]\n"
    "buffer_store_dwordx4 v[236:239], v219, s[32:35], s58 offen sc1\n"
    "s_lshl_b32 s23, s21, 11\n"
    "v_mfma_f32_16x16x32_bf16 v[188:191], v[168:171], v[104:107], v[188:191]\n"
    "v_cvt_pk_bf16_f32 v241, v230, v231\n"
    "v_and_b32_e32 v253, 0xc0, v253\n"
    "v_mfma_f32_16x16x32_bf16 v[192:195], v[168:171], v[108:111], v[192:195]\n"
    "ds_read_b64_tr_b16 v[168:169], v218 offset:24576\n"
    "v_mfma_f32_16x16x32_bf16 v[180:183], v[172:175], v[112:115], v[180:183]\n"
    "buffer_load_dwordx4 v216, s[28:31], s48 offen offset:3072 lds\n"
    "ds_read_b64_tr_b16 v[170:171], v218 offset:26624\n"
    "v_lshlrev_b32_e32 v255, 3, v255\n"
    "v_mfma_f32_16x16x32_bf16 v[184:187], v[172:175], v[116:119], v[184:187]\n"
    "v_cvt_pk_bf16_f32 v242, v232, v233\n"
    "s_or_b32 s72, s22, s23\n"
    "v_mfma_f32_16x16x32_bf16 v[188:191], v[172:175], v[120:123], v[188:191]\n"
    "v_cvt_pk_bf16_f32 v243, v234, v235\n"
    "v_mfma_f32_16x16x32_bf16 v[192:195], v[172:175], v[124:127], v[192:195]\n"
    "ds_read_b64_tr_b16 v[172:173], v218 offset:28672\n"
    "v_or3_b32 v204, v252, v253, v255\n"
    "v_mfma_f32_16x16x32_bf16 v[180:183], v[176:179], v[128:131], v[180:183]\n"
    "ds_read_b64_tr_b16 v[174:175], v218 offset:30720\n"
    "s_and_b32 s22, s81, 0xf0\n"
    "v_mfma_f32_16x16x32_bf16 v[184:187], v[176:179], v[132:135], v[184:187]\n"
    "s_add_u32 m0, s50, 45056\n"
    "s_cmp_eq_u32 s53, 0\n"
    "v_mfma_f32_16x16x32_bf16 v[188:191], v[176:179], v[136:139], v[188:191]\n"
    "s_cbranch_scc1 Lm95_%=\n"
    "buffer_load_dwordx4 v217, s[28:31], s45 offen lds\n"
    "Lm95_%=:\n"
    "v_lshlrev_b32_e32 v252, 3, v214\n"
    "v_mfma_f32_16x16x32_bf16 v[192:195], v[176:179], v[140:143], v[192:195]\n"
    "s_add_u32 s28, s28, 128\n"
    "s_addc_u32 s29, s29, 0\n"
    "ds_read_b64_tr_b16 v[176:177], v218 offset:32768\n"
    "v_and_or_b32 v252, v252, 8, s22\n"
    "s_waitcnt lgkmcnt(15)\n"
    "v_mfma_f32_16x16x32_bf16 v[220:223], v[144:147], v[0:3], 0\n"
    "ds_read_b64_tr_b16 v[178:179], v218 offset:34816\n"
    "v_add_u32 v218, 36864, v218\n"
    "v_or_b32_e32 v252, s80, v252\n"
    "v_mfma_f32_16x16x32_bf16 v[224:227], v[144:147], v[4:7], 0\n"
    "buffer_store_dwordx4 v[240:243], v212, s[32:35], s58 offen sc1\n"
    "s_add_u32 s58, s58, 32768\n"
    "v_mfma_f32_16x16x32_bf16 v[228:231], v[144:147], v[8:11], 0\n"
    "v_mov_b32_e32 v253, 0x1a000\n"
    "v_mfma_f32_16x16x32_bf16 v[232:235], v[144:147], v[12:15], 0\n"
    "v_lshl_add_u32 v196, v252, 7, v253\n"
    "s_waitcnt lgkmcnt(14)\n"
    "v_mfma_f32_16x16x32_bf16 v[220:223], v[148:151], v[16:19], v[220:223]\n"
    "v_mfma_f32_16x16x32_bf16 v[224:227], v[148:151], v[20:23], v[224:227]\n"
    "v_lshlrev_b32_e32 v252, 5, v214\n"
    "v_mfma_f32_16x16x32_bf16 v[228:231], v[148:151], v[24:27], v[228:231]\n"
    "v_lshlrev_b32_e32 v253, 3, v254\n"
    "v_mfma_f32_16x16x32_bf16 v[232:235], v[148:151], v[28:31], v[232:235]\n"
    "s_waitcnt vmcnt(7) lgkmcnt(0)\n"
    "s_barrier\n"
    "v_mfma_f32_16x16x32_bf16 v[220:223], v[152:155], v[32:35], v[220:223]\n"
    "ds_read_b64_tr_b16 v[144:145], v218 offset:0\n"
    "v_mfma_f32_16x16x32_bf16 v[224:227], v[152:155], v[36:39], v[224:227]\n"
    "ds_read_b64_tr_b16 v[146:147], v218 offset:2048\n"
    "s_add_u32 m0, s49, 0\n"
    "v_mfma_f32_16x16x32_bf16 v[228:231], v[152:155], v[40:43], v[228:231]\n"
    "buffer_load_dwordx4 v216, s[28:31], s45 offen lds\n"
    "ds_read_b64_tr_b16 v[148:149], v218 offset:4096\n"
    "v_mfma_f32_16x16x32_bf16 v[232:235], v[152:155], v[44:47], v[232:235]\n"
    "ds_read_b64_tr_b16 v[150:151], v218 offset:6144\n"
    "v_mfma_f32_16x16x32_bf16 v[220:223], v[156:159], v[48:51], v[220:223]\n"
    "ds_read_b64_tr_b16 v[152:153], v218 offset:8192\n"
    "v_mfma_f32_16x16x32_bf16 v[224:227], v[156:159], v[52:55], v[224:227]\n"
    "ds_read_b64_tr_b16 v[154:155], v218 offset:10240\n"
    "v_mfma_f32_16x16x32_bf16 v[228:231], v[156:159], v[56:59], v[228:231]\n"
    "v_cvt_pk_bf16_f32 v236, v180, v181\n"
    "s_movk_i32 s22, 0x1e0\n"
    "v_mfma_f32_16x16x32_bf16 v[232:235], v[156:159], v[60:63], v[232:235]\n"
    "ds_read_b64_tr_b16 v[156:157], v218 offset:12288\n"
    "v_and_or_b32 v207, v252, s22, v253\n"
    "v_mfma_f32_16x16x32_bf16 v[220:223], v[160:163], v[64:67], v[220:223]\n"
    "buffer_load_dwordx4 v216, s[28:31], s46 offen offset:1024 lds\n"
    "ds_read_b64_tr_b16 v[158:159], v218 offset:14336\n"
    "s_lshl_b32 s22, s81, 3\n"
    "v_mfma_f32_16x16x32_bf16 v[224:227], v[160:163], v[68:71], v[224:227]\n"
    "v_cvt_pk_bf16_f32 v237, v182, v183\n"
    "v_mfma_f32_16x16x32_bf16 v[228:231], v[160:163], v[72:75], v[228:231]\n"
    "v_cvt_pk_bf16_f32 v238, v184, v185\n"
    "s_lshl_b32 s23, s81, 2\n"
    "v_mfma_f32_16x16x32_bf16 v[232:235], v[160:163], v[76:79], v[232:235]\n"
    "ds_read_b64_tr_b16 v[160:161], v218 offset:16384\n"
    "s_and_b32 s22, s22, 0x7e0\n"
    "v_mfma_f32_16x16x32_bf16 v[220:223], v[164:167], v[80:83], v[220:223]\n"
    "ds_read_b64_tr_b16 v[162:163], v218 offset:18432\n"
    "v_mfma_f32_16x16x32_bf16 v[224:227], v[164:167], v[84:87], v[224:227]\n"
    "v_cvt_pk_bf16_f32 v239, v186, v187\n"
    "s_and_b32 s23, s23, 12\n"
    "v_mfma_f32_16x16x32_bf16 v[228:231], v[164:167], v[88:91], v[228:231]\n"
    "buffer_load_dwordx4 v216, s[28:31], s47 offen offset:2048 lds\n"
    "v_cvt_pk_bf16_f32 v240, v188, v189\n"
    "s_or_b32 s22, s22, s23\n"
    "v_mfma_f32_16x16x32_bf16 v[232:235], v[164:167], v[92:95], v[232:235]\n"
    "ds_read_b64_tr_b16 v[164:165], v218 offset:20480\n"
    "v_mfma_f32_16x16x32_bf16 v[220:223], v[168:171], v[96:99], v[220:223]\n"
    "ds_read_b64_tr_b16 v[166:167], v218 offset:22528\n"
    "s_lshl_b32 s20, s20, 4\n"
    "v_mfma_f32_16x16x32_bf16 v[224:227], v[168:171], v[100:103], v[224:227]\n"
    "buffer_store_dwordx4 v[236:239], v219, s[32:35], s58 offen sc1\n"
    "s_or_b32 s20, s22, s20\n"
    "v_mfma_f32_16x16x32_bf16 v[228:231], v[168:171], v[104:107], v[228:231]\n"
    "v_cvt_pk_bf16_f32 v241, v190, v191\n"
    "s_or_b32 s20, s20, s21\n"
    "v_mfma_f32_16x16x32_bf16 v[232:235], v[168:171], v[108:111], v[232:235]\n"
    "ds_read_b64_tr_b16 v[168:169], v218 offset:24576\n"
    "v_mfma_f32_16x16x32_bf16 v[220:223], v[172:175], v[112:115], v[220:223]\n"
    "buffer_load_dwordx4 v216, s[28:31], s48 offen offset:3072 lds\n"
    "ds_read_b64_tr_b16 v[170:171], v218 offset:26624\n"
    "s_lshl_b32 s78, s81, 7\n"
    "v_mfma_f32_16x16x32_bf16 v[224:227], v[172:175], v[116:119], v[224:227]\n"
    "v_cvt_pk_bf16_f32 v242, v192, v193\n"
    "s_xor_b32 s79, s20, 0x780\n"
    "v_mfma_f32_16x16x32_bf16 v[228:231], v[172:175], v[120:123], v[228:231]\n"
    "v_cvt_pk_bf16_f32 v243, v194, v195\n"
    "v_mfma_f32_16x16x32_bf16 v[232:235], v[172:175], v[124:127], v[232:235]\n"
    "ds_read_b64_tr_b16 v[172:173], v218 offset:28672\n"
    "v_xor_b32_e32 v198, 16, v197\n"
    "v_mfma_f32_16x16x32_bf16 v[220:223], v[176:179], v[128:131], v[220:223]\n"
    "ds_read_b64_tr_b16 v[174:175], v218 offset:30720\n"
    "s_lshl_b32 s74, s80, 11\n"
    "v_mfma_f32_16x16x32_bf16 v[224:227], v[176:179], v[132:135], v[224:227]\n"
    "s_add_u32 m0, s50, 0\n"
    "s_cmp_eq_u32 s53, 0\n"
    "v_mfma_f32_16x16x32_bf16 v[228:231], v[176:179], v[136:139], v[228:231]\n"
    "s_cbranch_scc1 Lm96_%=\n"
    "buffer_load_dwordx4 v217, s[28:31], s45 offen lds\n"
    "Lm96_%=:\n"
    "s_add_i32 s78, s78, 0x1a000\n"
    "v_mfma_f32_16x16x32_bf16 v[232:235], v[176:179], v[140:143], v[232:235]\n"
    "s_add_u32 s28, s28, 128\n"
    "s_addc_u32 s29, s29, 0\n"
    "ds_read_b64_tr_b16 v[176:177], v218 offset:32768\n"
    "v_or_b32_e32 v205, 0x10000, v204\n"
    "s_waitcnt lgkmcnt(15)\n"
    "v_mfma_f32_16x16x32_bf16 v[180:183], v[144:147], v[0:3], 0\n"
    "ds_read_b64_tr_b16 v[178:179], v218 offset:34816\n"
    "v_subrev_u32 v218, 81920, v218\n"
    "v_or_b32_e32 v206, 0x20000, v204\n"
    "v_mfma_f32_16x16x32_bf16 v[184:187], v[144:147], v[4:7], 0\n"
    "buffer_store_dwordx4 v[240:243], v212, s[32:35], s58 offen sc1\n"
    "s_add_u32 s58, s58, 32768\n"
    "v_mfma_f32_16x16x32_bf16 v[188:191], v[144:147], v[8:11], 0\n"
    "s_mulk_i32 s79, 0x480\n"
    "v_mfma_f32_16x16x32_bf16 v[192:195], v[144:147], v[12:15], 0\n"
    "s_mov_b32 s70, 0\n"
    "s_waitcnt lgkmcnt(14)\n"
    "v_mfma_f32_16x16x32_bf16 v[180:183], v[148:151], v[16:19], v[180:183]\n"
    "v_mfma_f32_16x16x32_bf16 v[184:187], v[148:151], v[20:23], v[184:187]\n"
    "s_lshl_b32 s71, s83, 4\n"
    "v_mfma_f32_16x16x32_bf16 v[188:191], v[148:151], v[24:27], v[188:191]\n"
    "s_add_u32 s71, s71, 1\n"
    "v_mfma_f32_16x16x32_bf16 v[192:195], v[148:151], v[28:31], v[192:195]\n"
    "s_waitcnt vmcnt(7) lgkmcnt(0)\n"
    "s_barrier\n"
    "v_mfma_f32_16x16x32_bf16 v[180:183], v[152:155], v[32:35], v[180:183]\n"
    "ds_read_b64_tr_b16 v[144:145], v218 offset:0\n"
    "v_mfma_f32_16x16x32_bf16 v[184:187], v[152:155], v[36:39], v[184:187]\n"
    "ds_read_b64_tr_b16 v[146:147], v218 offset:2048\n"
    "s_add_u32 m0, s49, 90112\n"
    "v_mfma_f32_16x16x32_bf16 v[188:191], v[152:155], v[40:43], v[188:191]\n"
    "buffer_load_dwordx4 v216, s[28:31], s45 offen lds\n"
    "ds_read_b64_tr_b16 v[148:149], v218 offset:4096\n"
    "v_mfma_f32_16x16x32_bf16 v[192:195], v[152:155], v[44:47], v[192:195]\n"
    "ds_read_b64_tr_b16 v[150:151], v218 offset:6144\n"
    "v_mfma_f32_16x16x32_bf16 v[180:183], v[156:159], v[48:51], v[180:183]\n"
    "ds_read_b64_tr_b16 v[152:153], v218 offset:8192\n"
    "v_mfma_f32_16x16x32_bf16 v[184:187], v[156:159], v[52:55], v[184:187]\n"
    "ds_read_b64_tr_b16 v[154:155], v218 offset:10240\n"
    "v_mfma_f32_16x16x32_bf16 v[188:191], v[156:159], v[56:59], v[188:191]\n"
    "v_cvt_pk_bf16_f32 v236, v220, v221\n"
    "v_mfma_f32_16x16x32_bf16 v[192:195], v[156:159], v[60:63], v[192:195]\n"
    "ds_read_b64_tr_b16 v[156:157], v218 offset:12288\n"
    "v_mfma_f32_16x16x32_bf16 v[180:183], v[160:163], v[64:67], v[180:183]\n"
    "buffer_load_dwordx4 v216, s[28:31], s46 offen offset:1024 lds\n"
    "ds_read_b64_tr_b16 v[158:159], v218 offset:14336\n"
    "v_mfma_f32_16x16x32_bf16 v[184:187], v[160:163], v[68:71], v[184:187]\n"
    "v_cvt_pk_bf16_f32 v237, v222, v223\n"
    "v_mfma_f32_16x16x32_bf16 v[188:191], v[160:163], v[72:75], v[188:191]\n"
    "v_cvt_pk_bf16_f32 v238, v224, v225\n"
    "v_mfma_f32_16x16x32_bf16 v[192:195], v[160:163], v[76:79], v[192:195]\n"
    "ds_read_b64_tr_b16 v[160:161], v218 offset:16384\n"
    "v_mfma_f32_16x16x32_bf16 v[180:183], v[164:167], v[80:83], v[180:183]\n"
    "ds_read_b64_tr_b16 v[162:163], v218 offset:18432\n"
    "v_mfma_f32_16x16x32_bf16 v[184:187], v[164:167], v[84:87], v[184:187]\n"
    "v_cvt_pk_bf16_f32 v239, v226, v227\n"
    "v_mfma_f32_16x16x32_bf16 v[188:191], v[164:167], v[88:91], v[188:191]\n"
    "buffer_load_dwordx4 v216, s[28:31], s47 offen offset:2048 lds\n"
    "v_cvt_pk_bf16_f32 v240, v228, v229\n"
    "v_mfma_f32_16x16x32_bf16 v[192:195], v[164:167], v[92:95], v[192:195]\n"
    "ds_read_b64_tr_b16 v[164:165], v218 offset:20480\n"
    "v_mfma_f32_16x16x32_bf16 v[180:183], v[168:171], v[96:99], v[180:183]\n"
    "ds_read_b64_tr_b16 v[166:167], v218 offset:22528\n"
    "v_mfma_f32_16x16x32_bf16 v[184:187], v[168:171], v[100:103], v[184:187]\n"
    "buffer_store_dwordx4 v[236:239], v219, s[32:35], s58 offen sc1\n"
    "v_mfma_f32_16x16x32_bf16 v[188:191], v[168:171], v[104:107], v[188:191]\n"
    "v_cvt_pk_bf16_f32 v241, v230, v231\n"
    "v_mfma_f32_16x16x32_bf16 v[192:195], v[168:171], v[108:111], v[192:195]\n"
    "ds_read_b64_tr_b16 v[168:169], v218 offset:24576\n"
    "v_mfma_f32_16x16x32_bf16 v[180:183], v[172:175], v[112:115], v[180:183]\n"
    "buffer_load_dwordx4 v216, s[28:31], s48 offen offset:3072 lds\n"
    "ds_read_b64_tr_b16 v[170:171], v218 offset:26624\n"
    "v_mfma_f32_16x16x32_bf16 v[184:187], v[172:175], v[116:119], v[184:187]\n"
    "v_cvt_pk_bf16_f32 v242, v232, v233\n"
    "v_mfma_f32_16x16x32_bf16 v[188:191], v[172:175], v[120:123], v[188:191]\n"
    "v_cvt_pk_bf16_f32 v243, v234, v235\n"
    "v_mfma_f32_16x16x32_bf16 v[192:195], v[172:175], v[124:127], v[192:195]\n"
    "ds_read_b64_tr_b16 v[172:173], v218 offset:28672\n"
    "v_mfma_f32_16x16x32_bf16 v[180:183], v[176:179], v[128:131], v[180:183]\n"
    "ds_read_b64_tr_b16 v[174:175], v218 offset:30720\n"
    "v_mfma_f32_16x16x32_bf16 v[184:187], v[176:179], v[132:135], v[184:187]\n"
    "s_add_u32 m0, s50, 90112\n"
    "s_cmp_eq_u32 s53, 0\n"
    "v_mfma_f32_16x16x32_bf16 v[188:191], v[176:179], v[136:139], v[188:191]\n"
    "s_cbranch_scc1 Lm97_%=\n"
    "buffer_load_dwordx4 v217, s[28:31], s45 offen lds\n"
    "Lm97_%=:\n"
    "v_mfma_f32_16x16x32_bf16 v[192:195], v[176:179], v[140:143], v[192:195]\n"
    "s_add_u32 s28, s28, 128\n"
    "s_addc_u32 s29, s29, 0\n"
    "ds_read_b64_tr_b16 v[176:177], v218 offset:32768\n"
    "s_waitcnt lgkmcnt(15)\n"
    "v_mfma_f32_16x16x32_bf16 v[220:223], v[144:147], v[0:3], 0\n"
    "ds_read_b64_tr_b16 v[178:179], v218 offset:34816\n"
    "v_subrev_u32 v218, 45056, v218\n"
    "v_mfma_f32_16x16x32_bf16 v[224:227], v[144:147], v[4:7], 0\n"
    "buffer_store_dwordx4 v[240:243], v212, s[32:35], s58 offen sc1\n"
    "s_add_u32 s58, s58, 32768\n"
    "v_mfma_f32_16x16x32_bf16 v[228:231], v[144:147], v[8:11], 0\n"
    "v_mfma_f32_16x16x32_bf16 v[232:235], v[144:147], v[12:15], 0\n"
    "s_waitcnt lgkmcnt(14)\n"
    "v_mfma_f32_16x16x32_bf16 v[220:223], v[148:151], v[16:19], v[220:223]\n"
    "v_mfma_f32_16x16x32_bf16 v[224:227], v[148:151], v[20:23], v[224:227]\n"
    "v_mfma_f32_16x16x32_bf16 v[228:231], v[148:151], v[24:27], v[228:231]\n"
    "v_mfma_f32_16x16x32_bf16 v[232:235], v[148:151], v[28:31], v[232:235]\n"
    "s_waitcnt vmcnt(7) lgkmcnt(0)\n"
    "s_barrier\n"
    "v_mfma_f32_16x16x32_bf16 v[220:223], v[152:155], v[32:35], v[220:223]\n"
    "ds_read_b64_tr_b16 v[144:145], v218 offset:0\n"
    "v_mfma_f32_16x16x32_bf16 v[224:227], v[152:155], v[36:39], v[224:227]\n"
    "ds_read_b64_tr_b16 v[146:147], v218 offset:2048\n"
    "s_add_u32 m0, s49, 126976\n"
    "v_mfma_f32_16x16x32_bf16 v[228:231], v[152:155], v[40:43], v[228:231]\n"
    "buffer_load_dwordx4 v216, s[28:31], s45 offen lds\n"
    "ds_read_b64_tr_b16 v[148:149], v218 offset:4096\n"
    "v_mfma_f32_16x16x32_bf16 v[232:235], v[152:155], v[44:47], v[232:235]\n"
    "ds_read_b64_tr_b16 v[150:151], v218 offset:6144\n"
    "v_mfma_f32_16x16x32_bf16 v[220:223], v[156:159], v[48:51], v[220:223]\n"
    "ds_read_b64_tr_b16 v[152:153], v218 offset:8192\n"
    "v_mfma_f32_16x16x32_bf16 v[224:227], v[156:159], v[52:55], v[224:227]\n"
    "ds_read_b64_tr_b16 v[154:155], v218 offset:10240\n"
    "v_mfma_f32_16x16x32_bf16 v[228:231], v[156:159], v[56:59], v[228:231]\n"
    "v_cvt_pk_bf16_f32 v236, v180, v181\n"
    "v_mfma_f32_16x16x32_bf16 v[232:235], v[156:159], v[60:63], v[232:235]\n"
    "ds_read_b64_tr_b16 v[156:157], v218 offset:12288\n"
    "v_mfma_f32_16x16x32_bf16 v[220:223], v[160:163], v[64:67], v[220:223]\n"
    "buffer_load_dwordx4 v216, s[28:31], s46 offen offset:1024 lds\n"
    "ds_read_b64_tr_b16 v[158:159], v218 offset:14336\n"
    "v_mfma_f32_16x16x32_bf16 v[224:227], v[160:163], v[68:71], v[224:227]\n"
    "v_cvt_pk_bf16_f32 v237, v182, v183\n"
    "v_mfma_f32_16x16x32_bf16 v[228:231], v[160:163], v[72:75], v[228:231]\n"
    "v_cvt_pk_bf16_f32 v238, v184, v185\n"
    "v_mfma_f32_16x16x32_bf16 v[232:235], v[160:163], v[76:79], v[232:235]\n"
    "ds_read_b64_tr_b16 v[160:161], v218 offset:16384\n"
    "v_mfma_f32_16x16x32_bf16 v[220:223], v[164:167], v[80:83], v[220:223]\n"
    "ds_read_b64_tr_b16 v[162:163], v218 offset:18432\n"
    "v_mfma_f32_16x16x32_bf16 v[224:227], v[164:167], v[84:87], v[224:227]\n"
    "v_cvt_pk_bf16_f32 v239, v186, v187\n"
    "v_mfma_f32_16x16x32_bf16 v[228:231], v[164:167], v[88:91], v[228:231]\n"
    "buffer_load_dwordx4 v216, s[28:31], s47 offen offset:2048 lds\n"
    "v_cvt_pk_bf16_f32 v240, v188, v189\n"
    "v_mfma_f32_16x16x32_bf16 v[232:235], v[164:167], v[92:95], v[232:235]\n"
    "ds_read_b64_tr_b16 v[164:165], v218 offset:20480\n"
    "v_mfma_f32_16x16x32_bf16 v[220:223], v[168:171], v[96:99], v[220:223]\n"
    "ds_read_b64_tr_b16 v[166:167], v218 offset:22528\n"
    "v_mfma_f32_16x16x32_bf16 v[224:227], v[168:171], v[100:103], v[224:227]\n"
    "buffer_store_dwordx4 v[236:239], v219, s[32:35], s58 offen sc1\n"
    "v_mfma_f32_16x16x32_bf16 v[228:231], v[168:171], v[104:107], v[228:231]\n"
    "v_cvt_pk_bf16_f32 v241, v190, v191\n"
    "v_mfma_f32_16x16x32_bf16 v[232:235], v[168:171], v[108:111], v[232:235]\n"
    "ds_read_b64_tr_b16 v[168:169], v218 offset:24576\n"
    "v_mfma_f32_16x16x32_bf16 v[220:223], v[172:175], v[112:115], v[220:223]\n"
    "buffer_load_dwordx4 v216, s[28:31], s48 offen offset:3072 lds\n"
    "ds_read_b64_tr_b16 v[170:171], v218 offset:26624\n"
    "v_mfma_f32_16x16x32_bf16 v[224:227], v[172:175], v[116:119], v[224:227]\n"
    "v_cvt_pk_bf16_f32 v242, v192, v193\n"
    "v_mfma_f32_16x16x32_bf16 v[228:231], v[172:175], v[120:123], v[228:231]\n"
    "v_cvt_pk_bf16_f32 v243, v194, v195\n"
    "v_mfma_f32_16x16x32_bf16 v[232:235], v[172:175], v[124:127], v[232:235]\n"
    "ds_read_b64_tr_b16 v[172:173], v218 offset:28672\n"
    "v_mfma_f32_16x16x32_bf16 v[220:223], v[176:179], v[128:131], v[220:223]\n"
    "ds_read_b64_tr_b16 v[174:175], v218 offset:30720\n"
    "v_mfma_f32_16x16x32_bf16 v[224:227], v[176:179], v[132:135], v[224:227]\n"
    "s_add_u32 m0, s50, 126976\n"
    "s_cmp_eq_u32 s53, 0\n"
    "v_mfma_f32_16x16x32_bf16 v[228:231], v[176:179], v[136:139], v[228:231]\n"
    "s_cbranch_scc1 Lm98_%=\n"
    "buffer_load_dwordx4 v217, s[28:31], s45 offen lds\n"
    "Lm98_%=:\n"
    "v_mfma_f32_16x16x32_bf16 v[232:235], v[176:179], v[140:143], v[232:235]\n"
    "s_add_u32 s28, s28, 128\n"
    "s_addc_u32 s29, s29, 0\n"
    "ds_read_b64_tr_b16 v[176:177], v218 offset:32768\n"
    "s_waitcnt lgkmcnt(15)\n"
    "v_mfma_f32_16x16x32_bf16 v[180:183], v[144:147], v[0:3], 0\n"
    "ds_read_b64_tr_b16 v[178:179], v218 offset:34816\n"
    "v_add_u32 v218, 90112, v218\n"
    "v_mfma_f32_16x16x32_bf16 v[184:187], v[144:147], v[4:7], 0\n"
    "buffer_store_dwordx4 v[240:243], v212, s[32:35], s58 offen sc1\n"
    "s_add_u32 s58, s58, 32768\n"
    "v_mfma_f32_16x16x32_bf16 v[188:191], v[144:147], v[8:11], 0\n"
    "v_mfma_f32_16x16x32_bf16 v[192:195], v[144:147], v[12:15], 0\n"
    "s_waitcnt lgkmcnt(14)\n"
    "v_mfma_f32_16x16x32_bf16 v[180:183], v[148:151], v[16:19], v[180:183]\n"
    "v_mfma_f32_16x16x32_bf16 v[184:187], v[148:151], v[20:23], v[184:187]\n"
    "v_mfma_f32_16x16x32_bf16 v[188:191], v[148:151], v[24:27], v[188:191]\n"
    "v_mfma_f32_16x16x32_bf16 v[192:195], v[148:151], v[28:31], v[192:195]\n"
    "s_waitcnt vmcnt(7) lgkmcnt(0)\n"
    "s_barrier\n"
    "v_mfma_f32_16x16x32_bf16 v[180:183], v[152:155], v[32:35], v[180:183]\n"
    "ds_read_b64_tr_b16 v[144:145], v218 offset:0\n"
    "v_cvt_pk_bf16_f32 v236, v220, v221\n"
    "v_mfma_f32_16x16x32_bf16 v[184:187], v[152:155], v[36:39], v[184:187]\n"
    "ds_read_b64_tr_b16 v[146:147], v218 offset:2048\n"
    "v_cvt_pk_bf16_f32 v237, v222, v223\n"
    "v_mfma_f32_16x16x32_bf16 v[188:191], v[152:155], v[40:43], v[188:191]\n"
    "ds_read_b64_tr_b16 v[148:149], v218 offset:4096\n"
    "v_cvt_pk_bf16_f32 v238, v224, v225\n"
    "v_mfma_f32_16x16x32_bf16 v[192:195], v[152:155], v[44:47], v[192:195]\n"
    "ds_read_b64_tr_b16 v[150:151], v218 offset:6144\n"
    "v_cvt_pk_bf16_f32 v239, v226, v227\n"
    "v_mfma_f32_16x16x32_bf16 v[180:183], v[156:159], v[48:51], v[180:183]\n"
    "ds_read_b64_tr_b16 v[152:153], v218 offset:8192\n"
    "v_cvt_pk_bf16_f32 v240, v228, v229\n"
    "v_mfma_f32_16x16x32_bf16 v[184:187], v[156:159], v[52:55], v[184:187]\n"
    "ds_read_b64_tr_b16 v[154:155], v218 offset:10240\n"
    "v_cvt_pk_bf16_f32 v241, v230, v231\n"
    "v_mfma_f32_16x16x32_bf16 v[188:191], v[156:159], v[56:59], v[188:191]\n"
    "buffer_store_dwordx4 v[236:239], v219, s[32:35], s58 offen sc1\n"
    "v_mfma_f32_16x16x32_bf16 v[192:195], v[156:159], v[60:63], v[192:195]\n"
    "ds_read_b64_tr_b16 v[156:157], v218 offset:12288\n"
    "v_cvt_pk_bf16_f32 v242, v232, v233\n"
    "v_mfma_f32_16x16x32_bf16 v[180:183], v[160:163], v[64:67], v[180:183]\n"
    "ds_read_b64_tr_b16 v[158:159], v218 offset:14336\n"
    "v_cvt_pk_bf16_f32 v243, v234, v235\n"
    "v_mfma_f32_16x16x32_bf16 v[184:187], v[160:163], v[68:71], v[184:187]\n"
    "v_mfma_f32_16x16x32_bf16 v[188:191], v[160:163], v[72:75], v[188:191]\n"
    "buffer_store_dwordx4 v[240:243], v212, s[32:35], s58 offen sc1\n"
    "s_add_u32 s58, s58, 32768\n"
    "v_mfma_f32_16x16x32_bf16 v[192:195], v[160:163], v[76:79], v[192:195]\n"
    "ds_read_b64_tr_b16 v[160:161], v218 offset:16384\n"
    "v_mfma_f32_16x16x32_bf16 v[180:183], v[164:167], v[80:83], v[180:183]\n"
    "ds_read_b64_tr_b16 v[162:163], v218 offset:18432\n"
    "v_mfma_f32_16x16x32_bf16 v[184:187], v[164:167], v[84:87], v[184:187]\n"
    "v_mfma_f32_16x16x32_bf16 v[188:191], v[164:167], v[88:91], v[188:191]\n"
    "v_mfma_f32_16x16x32_bf16 v[192:195], v[164:167], v[92:95], v[192:195]\n"
    "ds_read_b64_tr_b16 v[164:165], v218 offset:20480\n"
    "v_mfma_f32_16x16x32_bf16 v[180:183], v[168:171], v[96:99], v[180:183]\n"
    "ds_read_b64_tr_b16 v[166:167], v218 offset:22528\n"
    "v_mfma_f32_16x16x32_bf16 v[184:187], v[168:171], v[100:103], v[184:187]\n"
    "v_mfma_f32_16x16x32_bf16 v[188:191], v[168:171], v[104:107], v[188:191]\n"
    "v_mfma_f32_16x16x32_bf16 v[192:195], v[168:171], v[108:111], v[192:195]\n"
    "ds_read_b64_tr_b16 v[168:169], v218 offset:24576\n"
    "v_mfma_f32_16x16x32_bf16 v[180:183], v[172:175], v[112:115], v[180:183]\n"
    "ds_read_b64_tr_b16 v[170:171], v218 offset:26624\n"
    "v_mfma_f32_16x16x32_bf16 v[184:187], v[172:175], v[116:119], v[184:187]\n"
    "v_mfma_f32_16x16x32_bf16 v[188:191], v[172:175], v[120:123], v[188:191]\n"
    "v_mfma_f32_16x16x32_bf16 v[192:195], v[172:175], v[124:127], v[192:195]\n"
    "ds_read_b64_tr_b16 v[172:173], v218 offset:28672\n"
    "v_mfma_f32_16x16x32_bf16 v[180:183], v[176:179], v[128:131], v[180:183]\n"
    "ds_read_b64_tr_b16 v[174:175], v218 offset:30720\n"
    "v_mfma_f32_16x16x32_bf16 v[184:187], v[176:179], v[132:135], v[184:187]\n"
    "v_mfma_f32_16x16x32_bf16 v[188:191], v[176:179], v[136:139], v[188:191]\n"
    "v_mfma_f32_16x16x32_bf16 v[192:195], v[176:179], v[140:143], v[192:195]\n"
    "ds_read_b64_tr_b16 v[176:177], v218 offset:32768\n"
    "s_waitcnt lgkmcnt(15)\n"
    "v_mfma_f32_16x16x32_bf16 v[220:223], v[144:147], v[0:3], 0\n"
    "ds_read_b64_tr_b16 v[178:179], v218 offset:34816\n"
    "v_add_u32 v218, 36864, v218\n"
    "v_mfma_f32_16x16x32_bf16 v[224:227], v[144:147], v[4:7], 0\n"
    "v_mfma_f32_16x16x32_bf16 v[228:231], v[144:147], v[8:11], 0\n"
    "v_mfma_f32_16x16x32_bf16 v[232:235], v[144:147], v[12:15], 0\n"
    "s_waitcnt lgkmcnt(14)\n"
    "v_mfma_f32_16x16x32_bf16 v[220:223], v[148:151], v[16:19], v[220:223]\n"
    "v_mfma_f32_16x16x32_bf16 v[224:227], v[148:151], v[20:23], v[224:227]\n"
    "v_mfma_f32_16x16x32_bf16 v[228:231], v[148:151], v[24:27], v[228:231]\n"
    "v_mfma_f32_16x16x32_bf16 v[232:235], v[148:151], v[28:31], v[232:235]\n"
    "s_waitcnt vmcnt(3) lgkmcnt(0)\n"
    "s_barrier\n"
    "s_cmp_lg_u32 s80, 0\n"
    "s_cbranch_scc1 Lpb99_%=\n"
    "s_lshl_b32 s56, s83, 4\n"
    "s_add_u32 s56, s56, 4\n"
    "v_mov_b32 v200, s56\n"
    "v_mov_b32 v201, 0\n"
    "s_mov_b64 exec, 1\n"
    "buffer_atomic_swap v200, v201, s[36:39], s78 offen sc1\n"
    "s_mov_b64 exec, -1\n"
    "Lpb99_%=:\n"
    "v_mfma_f32_16x16x32_bf16 v[220:223], v[152:155], v[32:35], v[220:223]\n"
    "ds_read_b64_tr_b16 v[144:145], v218 offset:0\n"
    "v_cvt_pk_bf16_f32 v236, v180, v181\n"
    "v_mfma_f32_16x16x32_bf16 v[224:227], v[152:155], v[36:39], v[224:227]\n"
    "ds_read_b64_tr_b16 v[146:147], v218 offset:2048\n"
    "v_cvt_pk_bf16_f32 v237, v182, v183\n"
    "v_mfma_f32_16x16x32_bf16 v[228:231], v[152:155], v[40:43], v[228:231]\n"
    "ds_read_b64_tr_b16 v[148:149], v218 offset:4096\n"
    "v_cvt_pk_bf16_f32 v238, v184, v185\n"
    "v_mfma_f32_16x16x32_bf16 v[232:235], v[152:155], v[44:47], v[232:235]\n"
    "ds_read_b64_tr_b16 v[150:151], v218 offset:6144\n"
    "v_cvt_pk_bf16_f32 v239, v186, v187\n"
    "v_mfma_f32_16x16x32_bf16 v[220:223], v[156:159], v[48:51], v[220:223]\n"
    "ds_read_b64_tr_b16 v[152:153], v218 offset:8192\n"
    "v_cvt_pk_bf16_f32 v240, v188, v189\n"
    "v_mfma_f32_16x16x32_bf16 v[224:227], v[156:159], v[52:55], v[224:227]\n"
    "ds_read_b64_tr_b16 v[154:155], v218 offset:10240\n"
    "v_cvt_pk_bf16_f32 v241, v190, v191\n"
    "v_mfma_f32_16x16x32_bf16 v[228:231], v[156:159], v[56:59], v[228:231]\n"
    "buffer_store_dwordx4 v[236:239], v219, s[32:35], s58 offen sc1\n"
    "v_mfma_f32_16x16x32_bf16 v[232:235], v[156:159], v[60:63], v[232:235]\n"
    "ds_read_b64_tr_b16 v[156:157], v218 offset:12288\n"
    "v_cvt_pk_bf16_f32 v242, v192, v193\n"
    "v_mfma_f32_16x16x32_bf16 v[220:223], v[160:163], v[64:67], v[220:223]\n"
    "ds_read_b64_tr_b16 v[158:159], v218 offset:14336\n"
    "v_cvt_pk_bf16_f32 v243, v194, v195\n"
    "v_mfma_f32_16x16x32_bf16 v[224:227], v[160:163], v[68:71], v[224:227]\n"
    "v_mfma_f32_16x16x32_bf16 v[228:231], v[160:163], v[72:75], v[228:231]\n"
    "buffer_store_dwordx4 v[240:243], v212, s[32:35], s58 offen sc1\n"
    "s_add_u32 s58, s58, 32768\n"
    "v_mfma_f32_16x16x32_bf16 v[232:235], v[160:163], v[76:79], v[232:235]\n"
    "ds_read_b64_tr_b16 v[160:161], v218 offset:16384\n"
    "v_mfma_f32_16x16x32_bf16 v[220:223], v[164:167], v[80:83], v[220:223]\n"
    "ds_read_b64_tr_b16 v[162:163], v218 offset:18432\n"
    "v_mfma_f32_16x16x32_bf16 v[224:227], v[164:167], v[84:87], v[224:227]\n"
    "v_mfma_f32_16x16x32_bf16 v[228:231], v[164:167], v[88:91], v[228:231]\n"
    "v_mfma_f32_16x16x32_bf16 v[232:235], v[164:167], v[92:95], v[232:235]\n"
    "ds_read_b64_tr_b16 v[164:165], v218 offset:20480\n"
    "v_mfma_f32_16x16x32_bf16 v[220:223], v[168:171], v[96:99], v[220:223]\n"
    "ds_read_b64_tr_b16 v[166:167], v218 offset:22528\n"
    "v_mfma_f32_16x16x32_bf16 v[224:227], v[168:171], v[100:103], v[224:227]\n"
    "v_mfma_f32_16x16x32_bf16 v[228:231], v[168:171], v[104:107], v[228:231]\n"
    "v_mfma_f32_16x16x32_bf16 v[232:235], v[168:171], v[108:111], v[232:235]\n"
    "ds_read_b64_tr_b16 v[168:169], v218 offset:24576\n"
    "v_mfma_f32_16x16x32_bf16 v[220:223], v[172:175], v[112:115], v[220:223]\n"
    "ds_read_b64_tr_b16 v[170:171], v218 offset:26624\n"
    "v_mfma_f32_16x16x32_bf16 v[224:227], v[172:175], v[116:119], v[224:227]\n"
    "v_mfma_f32_16x16x32_bf16 v[228:231], v[172:175], v[120:123], v[228:231]\n"
    "v_mfma_f32_16x16x32_bf16 v[232:235], v[172:175], v[124:127], v[232:235]\n"
    "ds_read_b64_tr_b16 v[172:173], v218 offset:28672\n"
    "v_mfma_f32_16x16x32_bf16 v[220:223], v[176:179], v[128:131], v[220:223]\n"
    "ds_read_b64_tr_b16 v[174:175], v218 offset:30720\n"
    "v_mfma_f32_16x16x32_bf16 v[224:227], v[176:179], v[132:135], v[224:227]\n"
    "v_mfma_f32_16x16x32_bf16 v[228:231], v[176:179], v[136:139], v[228:231]\n"
    "v_mfma_f32_16x16x32_bf16 v[232:235], v[176:179], v[140:143], v[232:235]\n"
    "ds_read_b64_tr_b16 v[176:177], v218 offset:32768\n"
    "s_waitcnt lgkmcnt(15)\n"
    "v_mfma_f32_16x16x32_bf16 v[180:183], v[144:147], v[0:3], 0\n"
    "ds_read_b64_tr_b16 v[178:179], v218 offset:34816\n"
    "v_mfma_f32_16x16x32_bf16 v[184:187], v[144:147], v[4:7], 0\n"
    "v_mfma_f32_16x16x32_bf16 v[188:191], v[144:147], v[8:11], 0\n"
    "v_mfma_f32_16x16x32_bf16 v[192:195], v[144:147], v[12:15], 0\n"
    "s_waitcnt lgkmcnt(14)\n"
    "v_mfma_f32_16x16x32_bf16 v[180:183], v[148:151], v[16:19], v[180:183]\n"
    "v_mfma_f32_16x16x32_bf16 v[184:187], v[148:151], v[20:23], v[184:187]\n"
    "v_mfma_f32_16x16x32_bf16 v[188:191], v[148:151], v[24:27], v[188:191]\n"
    "v_mfma_f32_16x16x32_bf16 v[192:195], v[148:151], v[28:31], v[192:195]\n"
    "s_waitcnt lgkmcnt(0)\n"
    "s_barrier\n"
    "v_mov_b32 v202, 0\n"
    "s_mov_b64 exec, 3\n"
    "buffer_atomic_or v202, v196, s[36:39], 0 offen sc0 sc1\n"
    "s_mov_b64 exec, -1\n"
    "v_mfma_f32_16x16x32_bf16 v[180:183], v[152:155], v[32:35], v[180:183]\n"
    "v_cvt_pk_bf16_f32 v236, v220, v221\n"
    "v_mfma_f32_16x16x32_bf16 v[184:187], v[152:155], v[36:39], v[184:187]\n"
    "v_cvt_pk_bf16_f32 v237, v222, v223\n"
    "v_mfma_f32_16x16x32_bf16 v[188:191], v[152:155], v[40:43], v[188:191]\n"
    "v_cvt_pk_bf16_f32 v238, v224, v225\n"
    "v_mfma_f32_16x16x32_bf16 v[192:195], v[152:155], v[44:47], v[192:195]\n"
    "v_cvt_pk_bf16_f32 v239, v226, v227\n"
    "v_mfma_f32_16x16x32_bf16 v[180:183], v[156:159], v[48:51], v[180:183]\n"
    "v_cvt_pk_bf16_f32 v240, v228, v229\n"
    "v_mfma_f32_16x16x32_bf16 v[184:187], v[156:159], v[52:55], v[184:187]\n"
    "v_cvt_pk_bf16_f32 v241, v230, v231\n"
    "v_mfma_f32_16x16x32_bf16 v[188:191], v[156:159], v[56:59], v[188:191]\n"
    "buffer_store_dwordx4 v[236:239], v219, s[32:35], s58 offen sc1\n"
    "v_mfma_f32_16x16x32_bf16 v[192:195], v[156:159], v[60:63], v[192:195]\n"
    "v_cvt_pk_bf16_f32 v242, v232, v233\n"
    "v_mfma_f32_16x16x32_bf16 v[180:183], v[160:163], v[64:67], v[180:183]\n"
    "v_cvt_pk_bf16_f32 v243, v234, v235\n"
    "v_mfma_f32_16x16x32_bf16 v[184:187], v[160:163], v[68:71], v[184:187]\n"
    "v_mfma_f32_16x16x32_bf16 v[188:191], v[160:163], v[72:75], v[188:191]\n"
    "buffer_store_dwordx4 v[240:243], v212, s[32:35], s58 offen sc1\n"
    "s_add_u32 s58, s58, 32768\n"
    "v_mfma_f32_16x16x32_bf16 v[192:195], v[160:163], v[76:79], v[192:195]\n"
    "v_mfma_f32_16x16x32_bf16 v[180:183], v[164:167], v[80:83], v[180:183]\n"
    "v_mfma_f32_16x16x32_bf16 v[184:187], v[164:167], v[84:87], v[184:187]\n"
    "v_mfma_f32_16x16x32_bf16 v[188:191], v[164:167], v[88:91], v[188:191]\n"
    "v_mfma_f32_16x16x32_bf16 v[192:195], v[164:167], v[92:95], v[192:195]\n"
    "v_mfma_f32_16x16x32_bf16 v[180:183], v[168:171], v[96:99], v[180:183]\n"
    "v_mfma_f32_16x16x32_bf16 v[184:187], v[168:171], v[100:103], v[184:187]\n"
    "v_mfma_f32_16x16x32_bf16 v[188:191], v[168:171], v[104:107], v[188:191]\n"
    "v_mfma_f32_16x16x32_bf16 v[192:195], v[168:171], v[108:111], v[192:195]\n"
    "v_mfma_f32_16x16x32_bf16 v[180:183], v[172:175], v[112:115], v[180:183]\n"
    "v_mfma_f32_16x16x32_bf16 v[184:187], v[172:175], v[116:119], v[184:187]\n"
    "v_mfma_f32_16x16x32_bf16 v[188:191], v[172:175], v[120:123], v[188:191]\n"
    "v_mfma_f32_16x16x32_bf16 v[192:195], v[172:175], v[124:127], v[192:195]\n"
    "v_mfma_f32_16x16x32_bf16 v[180:183], v[176:179], v[128:131], v[180:183]\n"
    "v_mfma_f32_16x16x32_bf16 v[184:187], v[176:179], v[132:135], v[184:187]\n"
    "v_mfma_f32_16x16x32_bf16 v[188:191], v[176:179], v[136:139], v[188:191]\n"
    "v_mfma_f32_16x16x32_bf16 v[192:195], v[176:179], v[140:143], v[192:195]\n"
    "s_nop 4\n"
    "v_cvt_pk_bf16_f32 v236, v180, v181\n"
    "v_cvt_pk_bf16_f32 v237, v182, v183\n"
    "v_cvt_pk_bf16_f32 v238, v184, v185\n"
    "v_cvt_pk_bf16_f32 v239, v186, v187\n"
    "buffer_store_dwordx4 v[236:239], v219, s[32:35], s58 offen sc1\n"
    "v_cvt_pk_bf16_f32 v240, v188, v189\n"
    "v_cvt_pk_bf16_f32 v241, v190, v191\n"
    "v_cvt_pk_bf16_f32 v242, v192, v193\n"
    "v_cvt_pk_bf16_f32 v243, v194, v195\n"
    "buffer_store_dwordx4 v[240:243], v212, s[32:35], s58 offen sc1\n"
    "s_add_u32 s58, s58, 32768\n"
    "s_mov_b32 s57, s70\n"
    "s_waitcnt vmcnt(4)\n"
    "v_subrev_u32 v199, s71, v202\n"
    "v_cmp_lt_u32 vcc, 15, v199\n"
    "s_nop 0\n"
    "s_and_b64 vcc, vcc, 3\n"
    "s_cmp_ge_u32 s70, 9\n"
    "s_cbranch_scc1 Lfw100_%=\n"
    "s_cbranch_vccnz Lfw100_%=\n"
    "s_mov_b32 m0, s74\n"
    "s_add_u32 s74, s74, 16384\n"
    "buffer_load_dwordx4 v197, s[32:35], s72 offen sc1 lds\n"
    "buffer_load_dwordx4 v198, s[32:35], s72 offen offset:1024 sc1 lds\n"
    "s_add_u32 s72, s72, 32768\n"
    "s_add_u32 s70, s70, 1\n"
    "s_add_u32 s71, s71, 1\n"
    "v_subrev_u32 v199, s71, v202\n"
    "v_cmp_lt_u32 vcc, 15, v199\n"
    "s_nop 0\n"
    "s_and_b64 vcc, vcc, 3\n"
    "s_cmp_ge_u32 s70, 9\n"
    "s_cbranch_scc1 Lfw100_%=\n"
    "s_cbranch_vccnz Lfw100_%=\n"
    "s_mov_b32 m0, s74\n"
    "s_add_u32 s74, s74, 16384\n"
    "buffer_load_dwordx4 v197, s[32:35], s72 offen sc1 lds\n"
    "buffer_load_dwordx4 v198, s[32:35], s72 offen offset:1024 sc1 lds\n"
    "s_add_u32 s72, s72, 32768\n"
    "s_add_u32 s70, s70, 1\n"
    "s_add_u32 s71, s71, 1\n"
    "v_subrev_u32 v199, s71, v202\n"
    "v_cmp_lt_u32 vcc, 15, v199\n"
    "s_nop 0\n"
    "s_and_b64 vcc, vcc, 3\n"
    "s_cmp_ge_u32 s70, 9\n"
    "s_cbranch_scc1 Lfw100_%=\n"
    "s_cbranch_vccnz Lfw100_%=\n"
    "s_mov_b32 m0, s74\n"
    "s_add_u32 s74, s74, 16384\n"
    "buffer_load_dwordx4 v197, s[32:35], s72 offen sc1 lds\n"
    "buffer_load_dwordx4 v198, s[32:35], s72 offen offset:1024 sc1 lds\n"
    "s_add_u32 s72, s72, 32768\n"
    "s_add_u32 s70, s70, 1\n"
    "s_add_u32 s71, s71, 1\n"
    "v_subrev_u32 v199, s71, v202\n"
    "v_cmp_lt_u32 vcc, 15, v199\n"
    "s_nop 0\n"
    "s_and_b64 vcc, vcc, 3\n"
    "s_cmp_ge_u32 s70, 9\n"
    "s_cbranch_scc1 Lfw100_%=\n"
    "s_cbranch_vccnz Lfw100_%=\n"
    "s_mov_b32 m0, s74\n"
    "s_add_u32 s74, s74, 16384\n"
    "buffer_load_dwordx4 v197, s[32:35], s72 offen sc1 lds\n"
    "buffer_load_dwordx4 v198, s[32:35], s72 offen offset:1024 sc1 lds\n"
    "s_add_u32 s72, s72, 32768\n"
    "s_add_u32 s70, s70, 1\n"
    "s_add_u32 s71, s71, 1\n"
    "Lfw100_%=:\n"
    "s_sub_u32 s57, s70, s57\n"
    "s_cmp_lg_u32 s57, 4\n"
    "s_cbranch_scc1 Lpn102_%=\n"
    "s_waitcnt vmcnt(8)\n"
    "s_branch Lpw101_%=\n"
    "Lpn102_%=:\n"
    "s_cmp_lg_u32 s57, 3\n"
    "s_cbranch_scc1 Lpn103_%=\n"
    "s_waitcnt vmcnt(6)\n"
    "s_branch Lpw101_%=\n"
    "Lpn103_%=:\n"
    "s_cmp_lg_u32 s57, 2\n"
    "s_cbranch_scc1 Lpn104_%=\n"
    "s_waitcnt vmcnt(4)\n"
    "s_branch Lpw101_%=\n"
    "Lpn104_%=:\n"
    "s_cmp_lg_u32 s57, 1\n"
    "s_cbranch_scc1 Lpn105_%=\n"
    "s_waitcnt vmcnt(2)\n"
    "s_branch Lpw101_%=\n"
    "Lpn105_%=:\n"
    "s_waitcnt vmcnt(0)\n"
    "Lpw101_%=:\n"
    "s_barrier\n"
    "s_cmp_lg_u32 s80, 0\n"
    "s_cbranch_scc1 Lpb106_%=\n"
    "s_lshl_b32 s56, s83, 4\n"
    "s_add_u32 s56, s56, 9\n"
    "v_mov_b32 v200, s56\n"
    "v_mov_b32 v201, 0\n"
    "s_mov_b64 exec, 1\n"
    "buffer_atomic_swap v200, v201, s[36:39], s78 offen sc1\n"
    "s_mov_b64 exec, -1\n"
    "Lpb106_%=:\n"
    "Lpa107_%=:\n"
    "v_subrev_u32 v199, s71, v202\n"
    "v_cmp_lt_u32 vcc, 15, v199\n"
    "s_nop 0\n"
    "s_and_b64 vcc, vcc, 3\n"
    "s_cmp_ge_u32 s70, 9\n"
    "s_cbranch_scc1 Lpb108_%=\n"
    "s_cbranch_vccnz Lpb108_%=\n"
    "s_mov_b32 m0, s74\n"
    "s_add_u32 s74, s74, 16384\n"
    "buffer_load_dwordx4 v197, s[32:35], s72 offen sc1 lds\n"
    "buffer_load_dwordx4 v198, s[32:35], s72 offen offset:1024 sc1 lds\n"
    "s_add_u32 s72, s72, 32768\n"
    "s_add_u32 s70, s70, 1\n"
    "s_add_u32 s71, s71, 1\n"
    "s_branch Lpa107_%=\n"
    "Lpb108_%=:\n"
    "v_mov_b32 v202, 0\n"
    "s_mov_b64 exec, 3\n"
    "buffer_atomic_or v202, v196, s[36:39], 0 offen sc0 sc1\n"
    "s_mov_b64 exec, -1\n"
    "v_lshlrev_b32 v48, 16, v244\n"
    "v_and_b32 v49, 0xffff0000, v244\n"
    "v_lshlrev_b32 v50, 16, v245\n"
    "v_and_b32 v51, 0xffff0000, v245\n"
    "v_lshlrev_b32 v52, 16, v246\n"
    "v_and_b32 v53, 0xffff0000, v246\n"
    "v_lshlrev_b32 v54, 16, v247\n"
    "v_and_b32 v55, 0xffff0000, v247\n"
    "v_lshlrev_b32 v56, 16, v248\n"
    "v_and_b32 v57, 0xffff0000, v248\n"
    "v_lshlrev_b32 v58, 16, v249\n"
    "v_and_b32 v59, 0xffff0000, v249\n"
    "v_pk_add_f32 v[60:61], v[48:49], v[50:51]\n"
    "v_pk_add_f32 v[60:61], v[60:61], v[52:53]\n"
    "v_pk_add_f32 v[60:61], v[60:61], v[54:55]\n"
    "v_pk_add_f32 v[60:61], v[60:61], v[56:57]\n"
    "v_pk_add_f32 v[60:61], v[60:61], v[58:59]\n"
    "v_add_f32 v60, v60, v61\n"
    "s_nop 1\n"
    "v_add_f32_dpp v60, v60, v60 quad_perm:[1,0,3,2] row_mask:0xf bank_mask:0xf\n"
    "v_mov_b64 v[0:1], 0\n"
    "v_mov_b64 v[2:3], 0\n"
    "v_add_f32_dpp v60, v60, v60 quad_perm:[2,3,0,1] row_mask:0xf bank_mask:0xf\n"
    "v_mov_b64 v[4:5], 0\n"
    "v_mov_b64 v[6:7], 0\n"
    "v_add_f32_dpp v60, v60, v60 row_half_mirror row_mask:0xf bank_mask:0xf\n"
    "v_mov_b64 v[8:9], 0\n"
    "v_mov_b64 v[10:11], 0\n"
    "v_add_f32_dpp v60, v60, v60 row_mirror row_mask:0xf bank_mask:0xf\n"
    "v_mov_b64 v[12:13], 0\n"
    "v_mov_b64 v[14:15], 0\n"
    "v_add_f32_dpp v60, v60, v60 row_bcast:15 row_mask:0xa bank_mask:0xf\n"
    "v_mov_b64 v[16:17], 0\n"
    "v_mov_b64 v[18:19], 0\n"
    "v_add_f32_dpp v60, v60, v60 row_bcast:31 row_mask:0xc bank_mask:0xf\n"
    "v_mov_b64 v[20:21], 0\n"
    "v_mov_b64 v[22:23], 0\n"
    "v_readlane_b32 s56, v60, 63\n"
    "v_mov_b32 v62, 0xbae38e39\n"
    "s_nop 0\n"
    "v_mul_f32 v62, s56, v62\n"
    "v_pk_add_f32 v[64:65], v[48:49], v[62:63] op_sel_hi:[1,0]\n"
    "v_pk_add_f32 v[66:67], v[50:51], v[62:63] op_sel_hi:[1,0]\n"
    "v_pk_add_f32 v[68:69], v[52:53], v[62:63] op_sel_hi:[1,0]\n"
    "v_pk_add_f32 v[70:71], v[54:55], v[62:63] op_sel_hi:[1,0]\n"
    "v_pk_add_f32 v[72:73], v[56:57], v[62:63] op_sel_hi:[1,0]\n"
    "v_pk_add_f32 v[74:75], v[58:59], v[62:63] op_sel_hi:[1,0]\n"
    "v_pk_mul_f32 v[60:61], v[64:65], v[64:65]\n"
    "v_pk_fma_f32 v[60:61], v[66:67], v[66:67], v[60:61]\n"
    "v_pk_fma_f32 v[60:61], v[68:69], v[68:69], v[60:61]\n"
    "v_pk_fma_f32 v[60:61], v[70:71], v[70:71], v[60:61]\n"
    "s_mov_b32 exec_lo, 0x000f000f\n"
    "s_mov_b32 exec_hi, 0x000f000f\n"
    "v_pk_fma_f32 v[60:61], v[72:73], v[72:73], v[60:61]\n"
    "v_pk_fma_f32 v[60:61], v[74:75], v[74:75], v[60:61]\n"
    "s_mov_b64 exec, -1\n"
    "v_add_f32 v60, v60, v61\n"
    "s_nop 1\n"
    "v_add_f32_dpp v60, v60, v60 quad_perm:[1,0,3,2] row_mask:0xf bank_mask:0xf\n"
    "v_mov_b64 v[24:25], 0\n"
    "v_mov_b64 v[26:27], 0\n"
    "v_add_f32_dpp v60, v60, v60 quad_perm:[2,3,0,1] row_mask:0xf bank_mask:0xf\n"
    "v_mov_b64 v[28:29], 0\n"
    "v_mov_b64 v[30:31], 0\n"
    "v_add_f32_dpp v60, v60, v60 row_half_mirror row_mask:0xf bank_mask:0xf\n"
    "v_mov_b64 v[32:33], 0\n"
    "v_mov_b64 v[34:35], 0\n"
    "v_add_f32_dpp v60, v60, v60 row_mirror row_mask:0xf bank_mask:0xf\n"
    "v_mov_b64 v[36:37], 0\n"
    "v_mov_b64 v[38:39], 0\n"
    "v_add_f32_dpp v60, v60, v60 row_bcast:15 row_mask:0xa bank_mask:0xf\n"
    "v_mov_b64 v[40:41], 0\n"
    "v_mov_b64 v[42:43], 0\n"
    "v_add_f32_dpp v60, v60, v60 row_bcast:31 row_mask:0xc bank_mask:0xf\n"
    "s_nop 0\n"
    "v_readlane_b32 s56, v60, 63\n"
    "v_mov_b32 v60, 0x3ae38e39\n"
    "s_nop 0\n"
    "v_mul_f32 v60, s56, v60\n"
    "v_cvt_pk_bf16_f32 v60, v60, v60\n"
    "v_lshlrev_b32 v60, 16, v60\n"
    "v_add_f32 v60, 0x3727c5ac, v60\n"
    "v_cvt_pk_bf16_f32 v60, v60, v60\n"
    "v_lshlrev_b32 v60, 16, v60\n"
    "v_sqrt_f32 v60, v60\n"
    "s_nop 0\n"
    "v_cvt_pk_bf16_f32 v60, v60, v60\n"
    "v_lshlrev_b32 v60, 16, v60\n"
    "v_mul_f32 v60, v60, v60\n"
    "v_rcp_f32 v60, v60\n"
    "v_mov_b32 v62, 0x3f803f80\n"
    "v_mov_b32 v63, 0x3f803f80\n"
    "s_mov_b32 exec_lo, 0x00010001\n"
    "s_mov_b32 exec_hi, 0x00010001\n"
    "v_mov_b64 v[0:1], v[62:63]\n"
    "s_lshl_b64 exec, exec, 1\n"
    "v_mov_b64 v[2:3], v[62:63]\n"
    "s_lshl_b64 exec, exec, 1\n"
    "v_mov_b64 v[4:5], v[62:63]\n"
    "s_lshl_b64 exec, exec, 1\n"
    "v_mov_b64 v[6:7], v[62:63]\n"
    "s_lshl_b64 exec, exec, 1\n"
    "v_mov_b64 v[8:9], v[62:63]\n"
    "s_lshl_b64 exec, exec, 1\n"
    "v_mov_b64 v[10:11], v[62:63]\n"
    "s_lshl_b64 exec, exec, 1\n"
    "v_mov_b64 v[12:13], v[62:63]\n"
    "s_lshl_b64 exec, exec, 1\n"
    "v_mov_b64 v[14:15], v[62:63]\n"
    "s_lshl_b64 exec, exec, 1\n"
    "v_mov_b64 v[16:17], v[62:63]\n"
    "s_lshl_b64 exec, exec, 1\n"
    "v_mov_b64 v[18:19], v[62:63]\n"
    "s_lshl_b64 exec, exec, 1\n"
    "v_mov_b64 v[20:21], v[62:63]\n"
    "s_lshl_b64 exec, exec, 1\n"
    "v_mov_b64 v[22:23], v[62:63]\n"
    "s_lshl_b64 exec, exec, 1\n"
    "v_mov_b64 v[24:25], v[62:63]\n"
    "s_lshl_b64 exec, exec, 1\n"
    "v_mov_b64 v[26:27], v[62:63]\n"
    "s_lshl_b64 exec, exec, 1\n"
    "v_mov_b64 v[28:29], v[62:63]\n"
    "s_lshl_b64 exec, exec, 1\n"
    "v_mov_b64 v[30:31], v[62:63]\n"
    "s_mov_b64 exec, -1\n"
    "s_waitcnt vmcnt(0)\n"
    "s_movk_i32 s57, 0x7fff\n"
    "Lga109_%=:\n"
    "v_subrev_u32 v199, s71, v202\n"
    "v_cmp_lt_u32 vcc, 15, v199\n"
    "s_nop 0\n"
    "s_and_b64 vcc, vcc, 3\n"
    "s_cmp_ge_u32 s70, 9\n"
    "s_cbranch_scc1 Lgc110_%=\n"
    "s_cbranch_vccnz Lgc110_%=\n"
    "s_mov_b32 m0, s74\n"
    "s_add_u32 s74, s74, 16384\n"
    "buffer_load_dwordx4 v197, s[32:35], s72 offen sc1 lds\n"
    "buffer_load_dwordx4 v198, s[32:35], s72 offen offset:1024 sc1 lds\n"
    "s_add_u32 s72, s72, 32768\n"
    "s_add_u32 s70, s70, 1\n"
    "s_add_u32 s71, s71, 1\n"
    "s_branch Lga109_%=\n"
    "Lgc110_%=:\n"
    "s_cmp_ge_u32 s70, 9\n"
    "s_cbranch_scc1 Lgd111_%=\n"
    "s_sub_u32 s57, s57, 1\n"
    "s_cbranch_scc1 Lgd111_%=\n"
    "v_mov_b32 v202, 0\n"
    "s_mov_b64 exec, 3\n"
    "buffer_atomic_or v202, v196, s[36:39], 0 offen sc0 sc1\n"
    "s_mov_b64 exec, -1\n"
    "s_waitcnt vmcnt(0)\n"
    "s_branch Lga109_%=\n"
    "Lgd111_%=:\n"
    "s_waitcnt vmcnt(10)\n"
    "s_barrier\n"
    "ds_read_b64_tr_b16 v[64:65], v204 offset:0\n"
    "ds_read_b64_tr_b16 v[66:67], v204 offset:256\n"
    "ds_read_b64_tr_b16 v[68:69], v204 offset:512\n"
    "ds_read_b64_tr_b16 v[70:71], v204 offset:768\n"
    "ds_read_b64_tr_b16 v[72:73], v204 offset:16384\n"
    "ds_read_b64_tr_b16 v[74:75], v204 offset:16640\n"
    "ds_read_b64_tr_b16 v[76:77], v204 offset:16896\n"
    "ds_read_b64_tr_b16 v[78:79], v204 offset:17152\n"
    "ds_read_b64_tr_b16 v[80:81], v204 offset:32768\n"
    "ds_read_b64_tr_b16 v[82:83], v204 offset:33024\n"
    "ds_read_b64_tr_b16 v[84:85], v204 offset:33280\n"
    "ds_read_b64_tr_b16 v[86:87], v204 offset:33536\n"
    "ds_read_b64_tr_b16 v[88:89], v204 offset:49152\n"
    "ds_read_b64_tr_b16 v[90:91], v204 offset:49408\n"
    "ds_read_b64_tr_b16 v[92:93], v204 offset:49664\n"
    "ds_read_b64_tr_b16 v[94:95], v204 offset:49920\n"
    "s_waitcnt lgkmcnt(14)\n"
    "v_mfma_f32_16x16x32_bf16 v[32:35], v[64:67], v[0:3], v[32:35]\n"
    "s_waitcnt lgkmcnt(12)\n"
    "v_mfma_f32_16x16x32_bf16 v[32:35], v[68:71], v[4:7], v[32:35]\n"
    "s_waitcnt lgkmcnt(10)\n"
    "v_mfma_f32_16x16x32_bf16 v[32:35], v[72:75], v[8:11], v[32:35]\n"
    "s_waitcnt lgkmcnt(8)\n"
    "v_mfma_f32_16x16x32_bf16 v[32:35], v[76:79], v[12:15], v[32:35]\n"
    "s_waitcnt lgkmcnt(6)\n"
    "v_mfma_f32_16x16x32_bf16 v[32:35], v[80:83], v[16:19], v[32:35]\n"
    "s_waitcnt lgkmcnt(4)\n"
    "v_mfma_f32_16x16x32_bf16 v[32:35], v[84:87], v[20:23], v[32:35]\n"
    "s_waitcnt lgkmcnt(2)\n"
    "v_mfma_f32_16x16x32_bf16 v[32:35], v[88:91], v[24:27], v[32:35]\n"
    "s_waitcnt lgkmcnt(0)\n"
    "v_mfma_f32_16x16x32_bf16 v[32:35], v[92:95], v[28:31], v[32:35]\n"
    "s_nop 7\n"
    "v_pk_fma_f32 v[32:33], v[32:33], v[60:61], v[48:49] op_sel_hi:[1,0,1]\n"
    "v_pk_fma_f32 v[34:35], v[34:35], v[60:61], v[50:51] op_sel_hi:[1,0,1]\n"
    "v_cvt_pk_bf16_f32 v32, v32, v33\n"
    "v_cvt_pk_bf16_f32 v33, v34, v35\n"
    "buffer_store_dwordx2 v[32:33], v207, s[84:87], s79 offen offset:0\n"
    "s_waitcnt vmcnt(1)\n"
    "s_barrier\n"
    "ds_read_b64_tr_b16 v[64:65], v205 offset:0\n"
    "ds_read_b64_tr_b16 v[66:67], v205 offset:256\n"
    "ds_read_b64_tr_b16 v[68:69], v205 offset:512\n"
    "ds_read_b64_tr_b16 v[70:71], v205 offset:768\n"
    "ds_read_b64_tr_b16 v[72:73], v205 offset:16384\n"
    "ds_read_b64_tr_b16 v[74:75], v205 offset:16640\n"
    "ds_read_b64_tr_b16 v[76:77], v205 offset:16896\n"
    "ds_read_b64_tr_b16 v[78:79], v205 offset:17152\n"
    "ds_read_b64_tr_b16 v[80:81], v205 offset:32768\n"
    "ds_read_b64_tr_b16 v[82:83], v205 offset:33024\n"
    "ds_read_b64_tr_b16 v[84:85], v205 offset:33280\n"
    "ds_read_b64_tr_b16 v[86:87], v205 offset:33536\n"
    "ds_read_b64_tr_b16 v[88:89], v205 offset:49152\n"
    "ds_read_b64_tr_b16 v[90:91], v205 offset:49408\n"
    "ds_read_b64_tr_b16 v[92:93], v205 offset:49664\n"
    "ds_read_b64_tr_b16 v[94:95], v205 offset:49920\n"
    "ds_read_b64_tr_b16 v[96:97], v206 offset:0\n"
    "ds_read_b64_tr_b16 v[98:99], v206 offset:256\n"
    "ds_read_b64_tr_b16 v[100:101], v206 offset:512\n"
    "ds_read_b64_tr_b16 v[102:103], v206 offset:768\n"
    "s_waitcnt lgkmcnt(15)\n"
    "v_mfma_f32_16x16x32_bf16 v[36:39], v[64:67], v[0:3], v[36:39]\n"
    "v_mfma_f32_16x16x32_bf16 v[36:39], v[68:71], v[4:7], v[36:39]\n"
    "s_waitcnt lgkmcnt(14)\n"
    "v_mfma_f32_16x16x32_bf16 v[36:39], v[72:75], v[8:11], v[36:39]\n"
    "s_waitcnt lgkmcnt(12)\n"
    "v_mfma_f32_16x16x32_bf16 v[36:39], v[76:79], v[12:15], v[36:39]\n"
    "s_waitcnt lgkmcnt(10)\n"
    "v_mfma_f32_16x16x32_bf16 v[36:39], v[80:83], v[16:19], v[36:39]\n"
    "s_waitcnt lgkmcnt(8)\n"
    "v_mfma_f32_16x16x32_bf16 v[36:39], v[84:87], v[20:23], v[36:39]\n"
    "s_waitcnt lgkmcnt(6)\n"
    "v_mfma_f32_16x16x32_bf16 v[36:39], v[88:91], v[24:27], v[36:39]\n"
    "s_waitcnt lgkmcnt(4)\n"
    "v_mfma_f32_16x16x32_bf16 v[36:39], v[92:95], v[28:31], v[36:39]\n"
    "s_waitcnt lgkmcnt(2)\n"
    "v_mfma_f32_16x16x32_bf16 v[40:43], v[96:99], v[0:3], v[40:43]\n"
    "s_waitcnt lgkmcnt(0)\n"
    "v_mfma_f32_16x16x32_bf16 v[40:43], v[100:103], v[4:7], v[40:43]\n"
    "s_nop 3\n"
    "v_pk_fma_f32 v[36:37], v[36:37], v[60:61], v[52:53] op_sel_hi:[1,0,1]\n"
    "v_pk_fma_f32 v[38:39], v[38:39], v[60:61], v[54:55] op_sel_hi:[1,0,1]\n"
    "v_cvt_pk_bf16_f32 v36, v36, v37\n"
    "v_cvt_pk_bf16_f32 v37, v38, v39\n"
    "buffer_store_dwordx2 v[36:37], v207, s[84:87], s79 offen offset:512\n"
    "v_pk_fma_f32 v[40:41], v[40:41], v[60:61], v[56:57] op_sel_hi:[1,0,1]\n"
    "v_pk_fma_f32 v[42:43], v[42:43], v[60:61], v[58:59] op_sel_hi:[1,0,1]\n"
    "v_cvt_pk_bf16_f32 v40, v40, v41\n"
    "v_cvt_pk_bf16_f32 v41, v42, v43\n"
    "s_mov_b32 exec_lo, 0x000f000f\n"
    "s_mov_b32 exec_hi, 0x000f000f\n"
    "buffer_store_dwordx2 v[40:41], v207, s[84:87], s79 offen offset:1024\n"
    "s_mov_b64 exec, -1\n"
    "s_branch Lend2_%=\n"
    "Llo13_%=:\n"
    "s_cmp_eq_u32 s82, 0\n"
    "s_cbranch_scc1 Lns112_%=\n"
    "s_setprio 3\n"
    "v_add_u32 v230, 4294888448, v230\n"
    "v_mov_b32 v30, 163776\n"
    "v_mov_b32 v31, 1\n"
    "s_lshl1_add_u32 s56, s83, 1\n"
    "v_mov_b32 v28, s56\n"
    "v_lshlrev_b32 v29, 10, v239\n"
    "s_barrier\n"
    "ds_read_b64 v[0:1], v230 offset:0\n"
    "ds_read_b64 v[2:3], v230 offset:2048\n"
    "ds_read_b64 v[4:5], v230 offset:4096\n"
    "ds_read_b64 v[6:7], v230 offset:6144\n"
    "s_waitcnt lgkmcnt(3)\n"
    "v_lshlrev_b32 v8, 16, v0\n"
    "v_and_b32 v9, 0xffff0000, v0\n"
    "v_lshlrev_b32 v10, 16, v1\n"
    "v_and_b32 v11, 0xffff0000, v1\n"
    "v_fma_f32 v8, v8, s68, v128\n"
    "v_fma_f32 v9, v9, s68, v129\n"
    "v_fma_f32 v10, v10, s68, v130\n"
    "v_fma_f32 v11, v11, s68, v131\n"
    "v_cvt_pk_bf16_f32 v16, v8, v9\n"
    "v_cvt_pk_bf16_f32 v17, v10, v11\n"
    "buffer_store_dwordx2 v[16:17], v235, s[24:27], s59 offen offset:0 sc1\n"
    "s_waitcnt lgkmcnt(2)\n"
    "v_lshlrev_b32 v8, 16, v2\n"
    "v_and_b32 v9, 0xffff0000, v2\n"
    "v_lshlrev_b32 v10, 16, v3\n"
    "v_and_b32 v11, 0xffff0000, v3\n"
    "v_fma_f32 v8, v8, s69, v132\n"
    "v_fma_f32 v9, v9, s69, v133\n"
    "v_fma_f32 v10, v10, s69, v134\n"
    "v_fma_f32 v11, v11, s69, v135\n"
    "v_cvt_pk_bf16_f32 v18, v8, v9\n"
    "v_cvt_pk_bf16_f32 v19, v10, v11\n"
    "buffer_store_dwordx2 v[18:19], v235, s[24:27], s59 offen offset:128 sc1\n"
    "s_waitcnt lgkmcnt(1)\n"
    "v_lshlrev_b32 v8, 16, v4\n"
    "v_and_b32 v9, 0xffff0000, v4\n"
    "v_lshlrev_b32 v10, 16, v5\n"
    "v_and_b32 v11, 0xffff0000, v5\n"
    "v_fma_f32 v8, v8, s70, v136\n"
    "v_fma_f32 v9, v9, s70, v137\n"
    "v_fma_f32 v10, v10, s70, v138\n"
    "v_fma_f32 v11, v11, s70, v139\n"
    "v_cvt_pk_bf16_f32 v20, v8, v9\n"
    "v_cvt_pk_bf16_f32 v21, v10, v11\n"
    "buffer_store_dwordx2 v[20:21], v235, s[24:27], s59 offen offset:256 sc1\n"
    "s_waitcnt lgkmcnt(0)\n"
    "v_lshlrev_b32 v8, 16, v6\n"
    "v_and_b32 v9, 0xffff0000, v6\n"
    "v_lshlrev_b32 v10, 16, v7\n"
    "v_and_b32 v11, 0xffff0000, v7\n"
    "v_fma_f32 v8, v8, s71, v140\n"
    "v_fma_f32 v9, v9, s71, v141\n"
    "v_fma_f32 v10, v10, s71, v142\n"
    "v_fma_f32 v11, v11, s71, v143\n"
    "v_cvt_pk_bf16_f32 v22, v8, v9\n"
    "v_cvt_pk_bf16_f32 v23, v10, v11\n"
    "buffer_store_dwordx2 v[22:23], v235, s[24:27], s59 offen offset:384 sc1\n"
    "Lns112_%=:\n"
    "s_setprio 0\n"
    "s_cmp_lt_u32 s80, 4\n"
    "s_cselect_b32 s20, s78, s28\n"
    "s_cselect_b32 s21, s79, s29\n"
    "s_and_b32 s21, s21, 0xffff\n"
    "s_mov_b32 s22, 5308416\n"
    "s_mov_b32 s23, 0x20000\n"
    "s_mov_b32 s28, s30\n"
    "s_and_b32 s29, s31, 0xffff\n"
    "s_mov_b32 s30, 5308416\n"
    "s_mov_b32 s31, 0x20000\n"
    "s_mov_b32 s32, s34\n"
    "s_and_b32 s33, s35, 0xffff\n"
    "s_mov_b32 s34, 69468160\n"
    "s_mov_b32 s35, 0x20000\n"
    "s_mov_b32 s40, 0\n"
    "s_mov_b32 s41, 72704\n"
    "s_mov_b32 s42, 145408\n"
    "s_mov_b32 s43, 218112\n"
    "s_mov_b32 s44, 0\n"
    "s_mov_b32 s45, 0\n"
    "s_mov_b32 s46, 8192\n"
    "s_mov_b32 s47, 16384\n"
    "s_mov_b32 s48, 24576\n"
    "s_and_b32 s85, s85, 0xffff\n"
    "s_mov_b32 s86, 2359296\n"
    "s_mov_b32 s87, 0x20000\n"
    "v_mov_b32 v200, 0x3f803f80\n"
    "v_mov_b32 v201, 0x3f803f80\n"
    "v_mov_b32 v202, 0x3f803f80\n"
    "v_mov_b32 v203, 0x3f803f80\n"
    "s_lshl_b32 s60, s81, 1\n"
    "s_and_b32 s60, s60, 14\n"
    "s_bfe_u32 s61, s81, 0x10003\n"
    "s_or_b32 s60, s60, s61\n"
    "s_lshl_b32 s63, s61, 8\n"
    "v_lshrrev_b32_e32 v152, 3, v239\n"
    "v_and_b32_e32 v153, 7, v239\n"
    "s_and_b32 s54, s80, 3\n"
    "s_mul_i32 s62, s60, 0x120\n"
    "s_xor_b32 s63, s63, 0x100\n"
    "v_bitop3_b32 v153, v152, v153, 6 bitop3:0x6c\n"
    "v_mul_u32_u24_e32 v152, 0x2400, v152\n"
    "s_add_i32 s62, s62, s63\n"
    "s_add_i32 s63, s80, 2\n"
    "v_lshl_or_b32 v152, s54, 7, v152\n"
    "s_lshr_b32 s64, s63, 3\n"
    "s_mulk_i32 s60, 0x240\n"
    "v_lshlrev_b32_e32 v153, 4, v153\n"
    "v_lshl_or_b32 v152, s61, 6, v152\n"
    "v_and_b32_e32 v150, 3, v239\n"
    "v_lshrrev_b32_e32 v151, 2, v239\n"
    "s_and_b32 s65, s80, 1\n"
    "v_add3_u32 v213, v152, s60, v153\n"
    "v_bfe_u32 v152, v239, 4, 1\n"
    "s_lshl_b32 s61, s64, 1\n"
    "v_lshl_or_b32 v151, s65, 4, v151\n"
    "v_bitop3_b32 v150, s61, v150, v152 bitop3:0x36\n"
    "v_mul_u32_u24_e32 v151, 0x2400, v151\n"
    "s_lshl_b32 s60, s62, 1\n"
    "v_lshlrev_b32_e32 v150, 4, v150\n"
    "v_add3_u32 v214, s60, v151, v150\n"
    "s_lshl_b32 s60, s65, 11\n"
    "s_lshl_b32 s61, s64, 10\n"
    "s_not_b32 s63, s63\n"
    "s_or_b32 s50, s61, s60\n"
    "s_lshl_b32 s51, s80, 10\n"
    "s_mul_i32 s76, s81, 0x2400\n"
    "s_lshr_b32 s60, s80, 1\n"
    "s_bfe_u32 s61, s81, 0x20002\n"
    "s_lshr_b32 s55, s80, 2\n"
    "s_bfe_u32 s53, s63, 0x10002\n"
    "s_lshl_b32 s49, s80, 12\n"
    "s_bitset1_b32 s50, 15\n"
    "s_add_i32 s51, s51, 0x9000\n"
    "s_bitset1_b32 s76, 26\n"
    "s_xor_b32 s77, s61, s60\n"
    "Lhs19_%=:\n"
    "s_mov_b32 s60, s78\n"
    "s_and_b32 s61, s79, 0xffff\n"
    "s_mov_b32 s62, s22\n"
    "s_mov_b32 s63, s23\n"
    "s_cmp_lt_u32 s80, 6\n"
    "s_cselect_b32 s72, s60, s20\n"
    "s_cselect_b32 s73, s61, s21\n"
    "s_mov_b32 s74, s62\n"
    "s_mov_b32 s75, s63\n"
    "s_setprio 3\n"
    "s_cmp_eq_u32 s82, 0\n"
    "s_cbranch_scc1 Lnoe15_%=\n"
    "s_waitcnt vmcnt(0)\n"
    "s_mov_b64 exec, 1\n"
    "ds_add_rtn_u32 v31, v30, v31\n"
    "s_mov_b64 exec, -1\n"
    "s_and_b32 s56, s81, 7\n"
    "s_lshl_b32 s56, s56, 7\n"
    "s_add_u32 s56, s56, 139264\n"
    "s_waitcnt lgkmcnt(0)\n"
    "v_readfirstlane_b32 s57, v31\n"
    "s_nop 0\n"
    "s_cmp_lg_u32 s57, 3\n"
    "s_cbranch_scc1 Lnl113_%=\n"
    "s_mov_b64 exec, 0xffff\n"
    "buffer_atomic_add v28, v29, s[36:39], s56 offen sc1\n"
    "s_mov_b64 exec, -1\n"
    "Lnl113_%=:\n"
    "s_movk_i32 s56, 0x7fff\n"
    "Llw114_%=:\n"
    "ds_read_b32 v183, v30 offset:0\n"
    "s_waitcnt lgkmcnt(0)\n"
    "v_readfirstlane_b32 s57, v183\n"
    "s_nop 0\n"
    "s_cmp_ge_u32 s57, 4\n"
    "s_cbranch_scc1 Llx115_%=\n"
    "s_sleep 1\n"
    "s_sub_u32 s56, s56, 1\n"
    "s_cbranch_scc0 Llw114_%=\n"
    "Llx115_%=:\n"
    "s_sleep 1\n"
    "Lnoe15_%=:\n"
    "v_min_u32 v26, 17, v239\n"
    "v_lshlrev_b32 v26, 7, v26\n"
    "s_mul_i32 s56, s54, 2304\n"
    "s_add_u32 s56, s56, s76\n"
    "buffer_load_dword v26, v26, s[32:35], s56 offen sc1\n"
    "v_mov_b32 v25, 106496\n"
    "s_lshl_b32 s56, s81, 7\n"
    "buffer_load_dword v25, v25, s[36:39], s56 offen sc1\n"
    "s_add_u32 m0, s49, 4294950912\n"
    "s_nop 0\n"
    "buffer_load_dwordx4 v213, s[60:63], s40 offen lds\n"
    "buffer_load_dwordx4 v213, s[60:63], s41 offen offset:1024 lds\n"
    "buffer_load_dwordx4 v213, s[60:63], s42 offen offset:2048 lds\n"
    "buffer_load_dwordx4 v213, s[60:63], s43 offen offset:3072 lds\n"
    "s_add_u32 m0, s49, 0\n"
    "s_nop 0\n"
    "buffer_load_dwordx4 v213, s[20:23], s40 offen lds\n"
    "buffer_load_dwordx4 v213, s[20:23], s41 offen offset:1024 lds\n"
    "buffer_load_dwordx4 v213, s[20:23], s42 offen offset:2048 lds\n"
    "buffer_load_dwordx4 v213, s[20:23], s43 offen offset:3072 lds\n"
    "s_add_u32 m0, s50, 0\n"
    "s_nop 0\n"
    "buffer_load_dwordx4 v214, s[72:75], s40 offen lds\n"
    "s_add_u32 s40, s40, 294912\n"
    "s_add_u32 s41, s41, 294912\n"
    "s_add_u32 s42, s42, 294912\n"
    "s_add_u32 s43, s43, 294912\n"
    "s_add_u32 m0, s49, 28672\n"
    "s_nop 0\n"
    "buffer_load_dwordx4 v213, s[60:63], s40 offen lds\n"
    "buffer_load_dwordx4 v213, s[60:63], s41 offen offset:1024 lds\n"
    "buffer_load_dwordx4 v213, s[60:63], s42 offen offset:2048 lds\n"
    "buffer_load_dwordx4 v213, s[60:63], s43 offen offset:3072 lds\n"
    "s_add_u32 m0, s49, 45056\n"
    "s_nop 0\n"
    "buffer_load_dwordx4 v213, s[20:23], s40 offen lds\n"
    "buffer_load_dwordx4 v213, s[20:23], s41 offen offset:1024 lds\n"
    "buffer_load_dwordx4 v213, s[20:23], s42 offen offset:2048 lds\n"
    "buffer_load_dwordx4 v213, s[20:23], s43 offen offset:3072 lds\n"
    "s_add_u32 m0, s50, 45056\n"
    "s_nop 0\n"
    "buffer_load_dwordx4 v214, s[72:75], s40 offen lds\n"
    "s_add_u32 s40, s40, 294912\n"
    "s_add_u32 s41, s41, 294912\n"
    "s_add_u32 s42, s42, 294912\n"
    "s_add_u32 s43, s43, 294912\n"
    "s_cmp_lg_u32 s82, 0\n"
    "s_cbranch_scc1 Lq3116_%=\n"
    "s_setprio 0\n"
    "s_add_u32 m0, s49, 73728\n"
    "s_nop 0\n"
    "buffer_load_dwordx4 v213, s[60:63], s40 offen lds\n"
    "buffer_load_dwordx4 v213, s[60:63], s41 offen offset:1024 lds\n"
    "buffer_load_dwordx4 v213, s[60:63], s42 offen offset:2048 lds\n"
    "buffer_load_dwordx4 v213, s[60:63], s43 offen offset:3072 lds\n"
    "s_add_u32 m0, s49, 90112\n"
    "s_nop 0\n"
    "buffer_load_dwordx4 v213, s[20:23], s40 offen lds\n"
    "buffer_load_dwordx4 v213, s[20:23], s41 offen offset:1024 lds\n"
    "buffer_load_dwordx4 v213, s[20:23], s42 offen offset:2048 lds\n"
    "buffer_load_dwordx4 v213, s[20:23], s43 offen offset:3072 lds\n"
    "s_add_u32 m0, s50, 90112\n"
    "s_nop 0\n"
    "buffer_load_dwordx4 v214, s[72:75], s40 offen lds\n"
    "s_add_u32 s40, s40, 294912\n"
    "s_add_u32 s41, s41, 294912\n"
    "s_add_u32 s42, s42, 294912\n"
    "s_add_u32 s43, s43, 294912\n"
    "s_waitcnt vmcnt(18)\n"
    "s_branch Lnp17_%=\n"
    "Lq3116_%=:\n"
    "s_setprio 0\n"
    "s_waitcnt vmcnt(0)\n"
    "s_movk_i32 s56, 0x7fff\n"
    "Llw117_%=:\n"
    "ds_read_b32 v183, v30 offset:8\n"
    "s_waitcnt lgkmcnt(0)\n"
    "v_readfirstlane_b32 s57, v183\n"
    "s_nop 0\n"
    "s_cmp_ge_u32 s57, 4\n"
    "s_cbranch_scc1 Llx118_%=\n"
    "s_sleep 2\n"
    "s_sub_u32 s56, s56, 1\n"
    "s_cbranch_scc0 Llw117_%=\n"
    "Llx118_%=:\n"
    "s_sleep 2\n"
    "s_add_u32 m0, s49, 73728\n"
    "s_nop 0\n"
    "buffer_load_dwordx4 v213, s[60:63], s40 offen lds\n"
    "buffer_load_dwordx4 v213, s[60:63], s41 offen offset:1024 lds\n"
    "buffer_load_dwordx4 v213, s[60:63], s42 offen offset:2048 lds\n"
    "buffer_load_dwordx4 v213, s[60:63], s43 offen offset:3072 lds\n"
    "s_add_u32 m0, s49, 90112\n"
    "s_nop 0\n"
    "buffer_load_dwordx4 v213, s[20:23], s40 offen lds\n"
    "buffer_load_dwordx4 v213, s[20:23], s41 offen offset:1024 lds\n"
    "buffer_load_dwordx4 v213, s[20:23], s42 offen offset:2048 lds\n"
    "buffer_load_dwordx4 v213, s[20:23], s43 offen offset:3072 lds\n"
    "s_add_u32 m0, s50, 90112\n"
    "s_nop 0\n"
    "buffer_load_dwordx4 v214, s[72:75], s40 offen lds\n"
    "s_add_u32 s40, s40, 294912\n"
    "s_add_u32 s41, s41, 294912\n"
    "s_add_u32 s42, s42, 294912\n"
    "s_add_u32 s43, s43, 294912\n"
    "s_lshl_b32 s60, s80, 1\n"
    "s_and_b32 s61, s60, 12\n"
    "v_lshrrev_b32_e32 v151, 2, v239\n"
    "s_and_b32 s62, s81, 15\n"
    "v_lshrrev_b32_e32 v150, 4, v239\n"
    "v_and_b32_e32 v152, 3, v239\n"
    "v_and_or_b32 v153, v151, 3, s61\n"
    "s_bitcmp1_b32 s80, 0\n"
    "s_cselect_b32 s63, 0x1200, 0\n"
    "v_lshlrev_b32_e32 v150, 6, v150\n"
    "v_lshlrev_b32_e32 v152, 4, v152\n"
    "v_cmp_eq_u32_e32 vcc, s62, v153\n"
    "v_bfe_u32 v153, v239, 1, 1\n"
    "v_or3_b32 v150, s63, v150, v152\n"
    "s_and_b32 s63, s80, 3\n"
    "v_and_or_b32 v154, v151, 6, v153\n"
    "s_and_b32 s65, s81, 7\n"
    "v_lshlrev_b32_e32 v154, 4, v154\n"
    "s_lshl_b32 s64, s63, 5\n"
    "s_lshl_b32 s66, s65, 1\n"
    "s_bfe_u32 s67, s81, 0x10003\n"
    "v_xor_b32_e32 v155, s64, v154\n"
    "s_add_i32 s60, s60, 4\n"
    "v_and_b32_e32 v156, 8, v151\n"
    "s_lshl_b32 s64, s80, 2\n"
    "s_or_b32 s66, s66, s67\n"
    "s_lshl_b32 s73, s67, 8\n"
    "s_and_b32 s60, s60, 16\n"
    "v_bfe_u32 v158, v239, 3, 2\n"
    "s_mul_i32 s72, s66, 0x120\n"
    "s_xor_b32 s73, s73, 0x100\n"
    "v_and_or_b32 v156, s64, 4, v156\n"
    "v_lshrrev_b32_e32 v157, 3, v239\n"
    "s_add_i32 s73, s72, s73\n"
    "v_or3_b32 v156, v156, v158, s60\n"
    "v_and_b32_e32 v158, 7, v239\n"
    "s_add_i32 s60, s67, s80\n"
    "v_or_b32_e32 v156, s73, v156\n"
    "v_bitop3_b32 v158, v157, v158, 6 bitop3:0x6c\n"
    "s_lshl_b32 s60, s60, 5\n"
    "v_mul_u32_u24_e32 v156, 0x480, v156\n"
    "v_lshlrev_b32_e32 v158, 4, v158\n"
    "s_add_i32 s60, s60, s72\n"
    "v_or_b32_e32 v217, v156, v158\n"
    "v_or_b32_e32 v156, s60, v157\n"
    "s_lshr_b32 s60, s81, 4\n"
    "s_lshl_b32 s64, s60, 1\n"
    "s_lshr_b32 s72, s80, 2\n"
    "s_or_b32 s64, s64, s72\n"
    "v_bfrev_b32_e32 v152, 1\n"
    "s_xor_b32 s64, s64, 30\n"
    "v_cndmask_b32_e32 v219, v152, v150, vcc\n"
    "v_lshlrev_b32_e32 v150, 3, v239\n"
    "s_mulk_i32 s64, 0x240\n"
    "s_lshl_b32 s74, s63, 3\n"
    "v_and_b32_e32 v150, 8, v150\n"
    "v_mul_u32_u24_e32 v156, 0x480, v156\n"
    "s_or_b32 s64, s64, s74\n"
    "v_lshl_or_b32 v152, v151, 7, v150\n"
    "v_or_b32_e32 v216, v156, v158\n"
    "v_or_b32_e32 v156, s64, v157\n"
    "v_lshlrev_b32_e32 v151, 6, v151\n"
    "v_lshl_or_b32 v215, v156, 7, v158\n"
    "v_lshl_or_b32 v151, v153, 10, v151\n"
    "v_bfe_u32 v153, v239, 4, 1\n"
    "v_and_b32_e32 v156, 2, v239\n"
    "v_bitop3_b32 v156, v153, s63, v156 bitop3:0x36\n"
    "v_lshl_or_b32 v151, v156, 4, v151\n"
    "s_mov_b32 s64, 0x8000\n"
    "v_or3_b32 v212, v151, v150, s64\n"
    "s_lshl_b32 s64, s72, 12\n"
    "s_add_i32 s64, s64, 0x9000\n"
    "v_or_b32_e32 v150, s64, v152\n"
    "s_movk_i32 s64, 0x60\n"
    "v_or_b32_e32 v208, v155, v150\n"
    "v_bitop3_b32 v211, v155, s64, v150 bitop3:0x36\n"
    "v_bitop3_b32 v210, v155, 64, v150 bitop3:0x36\n"
    "v_bitop3_b32 v209, v155, 32, v150 bitop3:0x36\n"
    "v_lshl_or_b32 v150, s63, 12, v152\n"
    "s_lshl_b32 s63, s63, 8\n"
    "v_or_b32_e32 v204, v154, v150\n"
    "v_bitop3_b32 v207, v154, s64, v150 bitop3:0x36\n"
    "v_bitop3_b32 v206, v154, 64, v150 bitop3:0x36\n"
    "v_bitop3_b32 v205, v154, 32, v150 bitop3:0x36\n"
    "v_and_or_b32 v150, v239, 48, s63\n"
    "s_mul_i32 s64, s66, 0x480\n"
    "v_lshl_or_b32 v150, s67, 7, v150\n"
    "v_add_u32_e32 v150, s64, v150\n"
    "s_movk_i32 s61, 0x1200\n"
    "v_add_u32_e32 v252, 0x8000, v150\n"
    "v_lshrrev_b32_e32 v150, 5, v239\n"
    "v_mov_b32_e32 v151, s73\n"
    "v_mad_u32_u24 v150, v150, s61, v151\n"
    "v_lshlrev_b32_e32 v151, 2, v153\n"
    "v_or3_b32 v150, v150, v151, s74\n"
    "v_mov_b32_e32 v151, 0x8000\n"
    "s_lshl_b32 s61, s65, 7\n"
    "v_lshl_add_u32 v253, v150, 2, v151\n"
    "v_lshl_or_b32 v150, v239, 10, s61\n"
    "s_lshl_b32 s61, s60, 8\n"
    "v_lshl_add_u32 v151, v239, 7, s61\n"
    "v_or_b32_e32 v218, v155, v152\n"
    "v_add_u32_e32 v151, 0x5700, v151\n"
    "v_mov_b32_e32 v152, 0x6000\n"
    "v_cmp_gt_u32_e32 vcc, 20, v239\n"
    "v_add_u32_e32 v150, 0x2a000, v150\n"
    "s_lshl_b32 s60, s60, 3\n"
    "v_cndmask_b32_e32 v151, v152, v151, vcc\n"
    "v_cmp_gt_u32_e32 vcc, 18, v239\n"
    "s_or_b32 s60, s60, s80\n"
    "s_mul_i32 s66, s66, 0x51000\n"
    "v_cndmask_b32_e32 v254, v151, v150, vcc\n"
    "v_min_u32_e32 v150, 20, v239\n"
    "v_mad_u64_u32 v[150:151], s[60:61], s60, 21, v[150:151]\n"
    "s_lshl_b32 s61, s62, 10\n"
    "s_lshl_b32 s62, s81, 7\n"
    "s_and_b32 s62, s62, 0x4000\n"
    "s_or_b32 s61, s61, s62\n"
    "s_mul_i32 s60, s80, 0x1c00\n"
    "s_xor_b32 s61, s61, 0x4000\n"
    "s_add_i32 s60, s60, 0x14c00\n"
    "s_add_i32 s61, s61, 0x22000\n"
    "s_cmp_eq_u32 s80, 0\n"
    "v_lshl_add_u32 v250, v150, 7, s66\n"
    "s_cselect_b32 s60, s61, s60\n"
    "v_min_u32_e32 v150, 55, v239\n"
    "v_lshl_add_u32 v249, v150, 7, s60\n"
    "s_branch Lnp17_%=\n"
    "Lrole4_%=:\n"
    "s_mov_b64 s[84:85], s[34:35]\n"
    "s_add_u32 s34, s32, 6291455\n"
    "s_addc_u32 s35, s33, 0\n"
    "s_and_b32 s34, s34, 0xffe00000\n"
    "s_mov_b32 s36, s32\n"
    "s_and_b32 s37, s33, 0xffff\n"
    "s_mov_b32 s38, 524288\n"
    "s_mov_b32 s39, 0x20000\n"
    "s_add_u32 s24, s36, 524288\n"
    "s_addc_u32 s25, s37, 0\n"
    "s_mov_b32 s26, 2359296\n"
    "s_mov_b32 s27, 0x20000\n"
    "s_lshl_b32 s57, s81, 7\n"
    "v_mov_b32 v45, 73728\n"
    "s_mov_b64 s[58:59], s[28:29]\n"
    "s_cmp_ge_u32 s81, 208\n"
    "s_cbranch_scc1 Lrw119_%=\n"
    "s_mov_b32 s40, 0x3f803f80\n"
    "s_waitcnt vmcnt(5)\n"
    "v_dot2_f32_bf16 v40, v0, s40, 0\n"
    "v_dot2_f32_bf16 v42, v0, v0, 0\n"
    "v_dot2_f32_bf16 v41, v1, s40, 0\n"
    "v_dot2_f32_bf16 v43, v1, v1, 0\n"
    "v_dot2_f32_bf16 v40, v2, s40, v40\n"
    "v_dot2_f32_bf16 v42, v2, v2, v42\n"
    "v_dot2_f32_bf16 v41, v3, s40, v41\n"
    "v_dot2_f32_bf16 v43, v3, v3, v43\n"
    "s_waitcnt vmcnt(4)\n"
    "v_dot2_f32_bf16 v40, v4, s40, v40\n"
    "v_dot2_f32_bf16 v42, v4, v4, v42\n"
    "v_dot2_f32_bf16 v41, v5, s40, v41\n"
    "v_dot2_f32_bf16 v43, v5, v5, v43\n"
    "v_dot2_f32_bf16 v40, v6, s40, v40\n"
    "v_dot2_f32_bf16 v42, v6, v6, v42\n"
    "v_dot2_f32_bf16 v41, v7, s40, v41\n"
    "v_dot2_f32_bf16 v43, v7, v7, v43\n"
    "s_waitcnt vmcnt(3)\n"
    "v_dot2_f32_bf16 v40, v8, s40, v40\n"
    "v_dot2_f32_bf16 v42, v8, v8, v42\n"
    "v_dot2_f32_bf16 v41, v9, s40, v41\n"
    "v_dot2_f32_bf16 v43, v9, v9, v43\n"
    "v_dot2_f32_bf16 v40, v10, s40, v40\n"
    "v_dot2_f32_bf16 v42, v10, v10, v42\n"
    "v_dot2_f32_bf16 v41, v11, s40, v41\n"
    "v_dot2_f32_bf16 v43, v11, v11, v43\n"
    "s_waitcnt vmcnt(2)\n"
    "v_dot2_f32_bf16 v40, v12, s40, v40\n"
    "v_dot2_f32_bf16 v42, v12, v12, v42\n"
    "v_dot2_f32_bf16 v41, v13, s40, v41\n"
    "v_dot2_f32_bf16 v43, v13, v13, v43\n"
    "v_dot2_f32_bf16 v40, v14, s40, v40\n"
    "v_dot2_f32_bf16 v42, v14, v14, v42\n"
    "v_dot2_f32_bf16 v41, v15, s40, v41\n"
    "v_dot2_f32_bf16 v43, v15, v15, v43\n"
    "s_waitcnt vmcnt(1)\n"
    "v_dot2_f32_bf16 v40, v16, s40, v40\n"
    "v_dot2_f32_bf16 v42, v16, v16, v42\n"
    "v_dot2_f32_bf16 v41, v17, s40, v41\n"
    "v_dot2_f32_bf16 v43, v17, v17, v43\n"
    "v_readfirstlane_b32 s83, v252\n"
    "s_nop 0\n"
    "v_add_f32 v40, v40, v41\n"
    "v_add_f32 v42, v42, v43\n"
    "s_add_u32 s83, s83, 1\n"
    "v_add_f32_dpp v40, v40, v40 quad_perm:[1,0,3,2] row_mask:0xf bank_mask:0xf\n"
    "v_add_f32_dpp v42, v42, v42 quad_perm:[1,0,3,2] row_mask:0xf bank_mask:0xf\n"
    "s_nop 0\n"
    "v_add_f32_dpp v40, v40, v40 quad_perm:[2,3,0,1] row_mask:0xf bank_mask:0xf\n"
    "v_add_f32_dpp v42, v42, v42 quad_perm:[2,3,0,1] row_mask:0xf bank_mask:0xf\n"
    "s_nop 0\n"
    "v_add_f32_dpp v40, v40, v40 row_half_mirror row_mask:0xf bank_mask:0xf\n"
    "v_add_f32_dpp v42, v42, v42 row_half_mirror row_mask:0xf bank_mask:0xf\n"
    "s_nop 0\n"
    "v_add_f32_dpp v40, v40, v40 row_mirror row_mask:0xf bank_mask:0xf\n"
    "v_add_f32_dpp v42, v42, v42 row_mirror row_mask:0xf bank_mask:0xf\n"
    "v_mul_f32 v40, 0x3ae38e39, v40\n"
    "v_mul_f32 v42, 0x3ae38e39, v42\n"
    "v_fma_f32 v42, -v40, v40, v42\n"
    "v_max_f32 v42, 0, v42\n"
    "v_cvt_pk_bf16_f32 v42, v42, v42\n"
    "v_lshlrev_b32 v42, 16, v42\n"
    "v_add_f32 v42, 0x3727c5ac, v42\n"
    "v_cvt_pk_bf16_f32 v42, v42, v42\n"
    "v_lshlrev_b32 v42, 16, v42\n"
    "v_sqrt_f32 v42, v42\n"
    "v_mov_b32 v41, s83\n"
    "v_mov_b32 v44, s83\n"
    "v_cvt_pk_bf16_f32 v40, v40, v42\n"
    "s_mov_b32 exec_lo, 0x00010001\n"
    "s_mov_b32 exec_hi, 0x00010001\n"
    "buffer_store_dwordx2 v[40:41], v251, s[36:39], 0 offen sc1\n"
    "s_mov_b64 exec, -1\n"
    "Lrg122_%=:\n"
    "s_branch Lrd120_%=\n"
    "Lrw119_%=:\n"
    "s_sub_u32 s44, s81, 248\n"
    "s_cmp_ge_u32 s44, 4\n"
    "s_cbranch_scc1 Lca123_%=\n"
    "s_lshl_b32 s44, s44, 3\n"
    "s_add_u32 s44, s44, s80\n"
    "s_lshl_b32 s45, s44, 21\n"
    "s_add_u32 s45, s45, 0x180000\n"
    "s_add_u32 s46, s45, 8192\n"
    "s_mov_b32 s40, s34\n"
    "s_and_b32 s41, s35, 0xffff\n"
    "s_mov_b32 s42, 67108864\n"
    "s_mov_b32 s43, 0x20000\n"
    "s_mov_b64 exec, 1\n"
    "v_mov_b32 v47, 0\n"
    "v_mov_b32 v48, 0\n"
    "buffer_atomic_add v47, v253, s[40:43], s45 offen sc0 sc1\n"
    "buffer_atomic_add v48, v253, s[40:43], s46 offen sc0 sc1\n"
    "s_mov_b32 s52, -1\n"
    "s_mov_b32 s53, -1\n"
    "s_waitcnt vmcnt(0)\n"
    "v_mov_b32 v47, 0\n"
    "s_memtime s[48:49]\n"
    "s_waitcnt lgkmcnt(0)\n"
    "buffer_atomic_add v47, v253, s[40:43], s45 offen sc0 sc1\n"
    "s_waitcnt vmcnt(0)\n"
    "s_memtime s[50:51]\n"
    "s_waitcnt lgkmcnt(0)\n"
    "s_sub_u32 s48, s50, s48\n"
    "s_min_u32 s52, s52, s48\n"
    "v_mov_b32 v47, 0\n"
    "s_memtime s[48:49]\n"
    "s_waitcnt lgkmcnt(0)\n"
    "buffer_atomic_add v47, v253, s[40:43], s46 offen sc0 sc1\n"
    "s_waitcnt vmcnt(0)\n"
    "s_memtime s[50:51]\n"
    "s_waitcnt lgkmcnt(0)\n"
    "s_sub_u32 s48, s50, s48\n"
    "s_min_u32 s53, s53, s48\n"
    "v_mov_b32 v47, 0\n"
    "s_memtime s[48:49]\n"
    "s_waitcnt lgkmcnt(0)\n"
    "buffer_atomic_add v47, v253, s[40:43], s45 offen sc0 sc1\n"
    "s_waitcnt vmcnt(0)\n"
    "s_memtime s[50:51]\n"
    "s_waitcnt lgkmcnt(0)\n"
    "s_sub_u32 s48, s50, s48\n"
    "s_min_u32 s52, s52, s48\n"
    "v_mov_b32 v47, 0\n"
    "s_memtime s[48:49]\n"
    "s_waitcnt lgkmcnt(0)\n"
    "buffer_atomic_add v47, v253, s[40:43], s46 offen sc0 sc1\n"
    "s_waitcnt vmcnt(0)\n"
    "s_memtime s[50:51]\n"
    "s_waitcnt lgkmcnt(0)\n"
    "s_sub_u32 s48, s50, s48\n"
    "s_min_u32 s53, s53, s48\n"
    "v_mov_b32 v47, 0\n"
    "s_memtime s[48:49]\n"
    "s_waitcnt lgkmcnt(0)\n"
    "buffer_atomic_add v47, v253, s[40:43], s45 offen sc0 sc1\n"
    "s_waitcnt vmcnt(0)\n"
    "s_memtime s[50:51]\n"
    "s_waitcnt lgkmcnt(0)\n"
    "s_sub_u32 s48, s50, s48\n"
    "s_min_u32 s52, s52, s48\n"
    "v_mov_b32 v47, 0\n"
    "s_memtime s[48:49]\n"
    "s_waitcnt lgkmcnt(0)\n"
    "buffer_atomic_add v47, v253, s[40:43], s46 offen sc0 sc1\n"
    "s_waitcnt vmcnt(0)\n"
    "s_memtime s[50:51]\n"
    "s_waitcnt lgkmcnt(0)\n"
    "s_sub_u32 s48, s50, s48\n"
    "s_min_u32 s53, s53, s48\n"
    "s_getreg_b32 s47, hwreg(HW_REG_XCC_ID)\n"
    "s_and_b32 s47, s47, 7\n"
    "s_cmp_gt_u32 s52, s53\n"
    "s_cselect_b32 s48, 1, 0\n"
    "s_lshr_b32 s49, s47, 2\n"
    "s_xor_b32 s48, s48, s49\n"
    "s_sub_u32 s47, s47, s81\n"
    "s_and_b32 s47, s47, 7\n"
    "s_lshl_b32 s47, s47, 1\n"
    "s_or_b32 s48, s48, s47\n"
    "v_add_u32 v47, 1, v252\n"
    "v_lshl_or_b32 v47, v47, 4, s48\n"
    "s_lshl_b32 s44, s44, 7\n"
    "s_add_u32 s44, s44, 24576\n"
    "buffer_store_dword v47, v253, s[36:39], s44 offen sc1\n"
    "s_mov_b64 exec, -1\n"
    "Lca123_%=:\n"
    "s_lshr_b32 s44, s81, 1\n"
    "s_and_b32 s44, s44, 0x7c\n"
    "s_lshr_b32 s45, s80, 1\n"
    "s_add_i32 s44, s44, s45\n"
    "v_min_u32_e32 v150, 62, v239\n"
    "v_mul_u32_u24_e32 v151, 57, v150\n"
    "s_mul_i32 s44, s44, 7\n"
    "v_lshrrev_b32_e32 v151, 9, v151\n"
    "s_addk_i32 s44, 0xfd28\n"
    "v_add_u32_e32 v152, s44, v151\n"
    "s_movk_i32 s44, 0x2400\n"
    "v_mul_lo_u32 v152, v152, s44\n"
    "s_and_b32 s44, s81, 7\n"
    "v_mul_i32_i24_e32 v151, -9, v151\n"
    "s_mulk_i32 s44, 0x480\n"
    "v_add_lshl_u32 v150, v151, v150, 7\n"
    "v_add3_u32 v46, v150, s44, v152\n"
    "s_bitcmp0_b32 s80, 0\n"
    "s_cselect_b32 s20, s78, s28\n"
    "s_cselect_b32 s21, s79, s29\n"
    "s_and_b32 s21, s21, 0xffff\n"
    "s_mov_b32 s22, 5308416\n"
    "s_mov_b32 s23, 0x20000\n"
    "s_barrier\n"
    "s_mov_b32 s41, 0\n"
    "buffer_load_dword v49, v46, s[20:23], s41 offen\n"
    "s_add_u32 s41, s41, 1548288\n"
    "s_waitcnt vmcnt(1)\n"
    "v_readfirstlane_b32 s83, v252\n"
    "s_nop 0\n"
    "s_add_u32 s83, s83, 1\n"
    "v_mov_b32 v44, s83\n"
    "Lrd120_%=:\n"
    "s_barrier\n"
    "s_cmp_lg_u32 s80, 0\n"
    "s_cbranch_scc1 Lrf124_%=\n"
    "s_mov_b64 exec, 1\n"
    "buffer_store_dword v44, v45, s[36:39], s57 offen sc1\n"
    "s_mov_b64 exec, -1\n"
    "Lrf124_%=:\n"
    "s_cmp_ge_u32 s81, 216\n"
    "s_cbranch_scc1 Lw2125_%=\n"
    "s_lshr_b32 s69, s81, 2\n"
    "s_and_b32 s74, s80, 3\n"
    "v_lshrrev_b32_e32 v150, 3, v239\n"
    "s_and_b32 s69, s69, 62\n"
    "v_lshl_or_b32 v150, s74, 3, v150\n"
    "s_sub_i32 s69, s69, 36\n"
    "s_lshr_b32 s70, s80, 2\n"
    "v_mul_u32_u24_e32 v150, 0x2400, v150\n"
    "v_lshlrev_b32_e32 v151, 4, v239\n"
    "s_movk_i32 s75, 0x70\n"
    "s_or_b32 s71, s69, s70\n"
    "v_and_or_b32 v46, v151, s75, v150\n"
    "v_lshlrev_b32_e32 v150, 5, v239\n"
    "s_add_i32 s72, s71, 7\n"
    "v_and_b32_e32 v150, 0x6e0, v150\n"
    "s_lshr_b32 s66, s72, 4\n"
    "s_and_b32 s72, s80, 1\n"
    "v_lshl_or_b32 v150, s74, 11, v150\n"
    "s_or_b32 s69, s69, s72\n"
    "v_lshl_add_u32 v150, s70, 8, v150\n"
    "s_and_b32 s68, s81, 7\n"
    "s_add_i32 s73, s69, 7\n"
    "s_mul_i32 s60, s66, 0x1fffff7\n"
    "v_add_u32_e32 v47, 0x21000, v150\n"
    "v_lshlrev_b32_e32 v150, 2, v239\n"
    "s_lshr_b32 s73, s73, 4\n"
    "s_add_i32 s60, s60, s71\n"
    "v_lshl_or_b32 v150, s72, 8, v150\n"
    "s_mul_i32 s71, s68, 0x240\n"
    "v_or_b32_e32 v48, 0x21000, v150\n"
    "s_mul_i32 s70, s73, 0x1200\n"
    "s_mul_i32 s72, s73, 0xfffff7\n"
    "v_or_b32_e32 v150, s71, v239\n"
    "s_mul_i32 s75, s68, 0x480\n"
    "s_add_i32 s72, s72, s69\n"
    "v_add_lshl_u32 v150, v150, s70, 2\n"
    "s_lshl_b32 s69, s69, 10\n"
    "s_lshl_b32 s68, s68, 7\n"
    "s_lshl_b32 s65, s60, 7\n"
    "v_lshl_add_u32 v150, s72, 8, v150\n"
    "s_or_b32 s67, s69, s68\n"
    "s_add_i32 s65, s65, s75\n"
    "v_add_u32_e32 v50, 0x8000, v150\n"
    "s_add_i32 s67, s67, 0x2a000\n"
    "s_cmp_eq_u32 s66, 0\n"
    "s_cselect_b32 s60, s78, s58\n"
    "s_cselect_b32 s61, s79, s59\n"
    "s_and_b32 s61, s61, 0xffff\n"
    "s_mov_b32 s62, 5308416\n"
    "s_mov_b32 s63, 0x20000\n"
    "v_mov_b32 v43, 0\n"
    "buffer_load_dword v51, v43, s[36:39], s67 offen sc1\n"
    "buffer_load_dwordx4 v[64:67], v46, s[60:63], s65 offen\n"
    "s_add_u32 s65, s65, 294912\n"
    "buffer_load_dwordx4 v[68:71], v46, s[60:63], s65 offen\n"
    "s_add_u32 s65, s65, 294912\n"
    "buffer_load_dwordx4 v[72:75], v46, s[60:63], s65 offen\n"
    "s_add_u32 s65, s65, 294912\n"
    "buffer_load_dwordx4 v[76:79], v46, s[60:63], s65 offen\n"
    "s_add_u32 s65, s65, 294912\n"
    "buffer_load_dwordx4 v[80:83], v46, s[60:63], s65 offen\n"
    "s_add_u32 s65, s65, 294912\n"
    "buffer_load_dwordx4 v[84:87], v46, s[60:63], s65 offen\n"
    "s_add_u32 s65, s65, 294912\n"
    "buffer_load_dwordx4 v[88:91], v46, s[60:63], s65 offen\n"
    "s_add_u32 s65, s65, 294912\n"
    "buffer_load_dwordx4 v[92:95], v46, s[60:63], s65 offen\n"
    "s_add_u32 s65, s65, 294912\n"
    "buffer_load_dwordx4 v[96:99], v46, s[60:63], s65 offen\n"
    "s_add_u32 s65, s65, 294912\n"
    "buffer_load_dwordx4 v[100:103], v46, s[60:63], s65 offen\n"
    "s_add_u32 s65, s65, 294912\n"
    "buffer_load_dwordx4 v[104:107], v46, s[60:63], s65 offen\n"
    "s_add_u32 s65, s65, 294912\n"
    "buffer_load_dwordx4 v[108:111], v46, s[60:63], s65 offen\n"
    "s_add_u32 s65, s65, 294912\n"
    "buffer_load_dwordx4 v[112:115], v46, s[60:63], s65 offen\n"
    "s_add_u32 s65, s65, 294912\n"
    "buffer_load_dwordx4 v[116:119], v46, s[60:63], s65 offen\n"
    "s_add_u32 s65, s65, 294912\n"
    "buffer_load_dwordx4 v[120:123], v46, s[60:63], s65 offen\n"
    "s_add_u32 s65, s65, 294912\n"
    "buffer_load_dwordx4 v[124:127], v46, s[60:63], s65 offen\n"
    "s_add_u32 s65, s65, 294912\n"
    "buffer_load_dwordx4 v[128:131], v46, s[60:63], s65 offen\n"
    "s_add_u32 s65, s65, 294912\n"
    "buffer_load_dwordx4 v[132:135], v46, s[60:63], s65 offen\n"
    "s_add_u32 s65, s65, 294912\n"
    "s_mov_b32 s70, s67\n"
    "s_cmp_lt_u32 s80, 4\n"
    "s_cselect_b32 s20, s78, s28\n"
    "s_cselect_b32 s21, s79, s29\n"
    "s_and_b32 s21, s21, 0xffff\n"
    "s_mov_b32 s22, 5308416\n"
    "s_mov_b32 s23, 0x20000\n"
    "s_mov_b32 s28, s30\n"
    "s_and_b32 s29, s31, 0xffff\n"
    "s_mov_b32 s30, 5308416\n"
    "s_mov_b32 s31, 0x20000\n"
    "s_mov_b32 s32, s34\n"
    "s_and_b32 s33, s35, 0xffff\n"
    "s_mov_b32 s34, 69468160\n"
    "s_mov_b32 s35, 0x20000\n"
    "s_cmp_lt_u32 s80, 4\n"
    "s_cselect_b32 s40, 884736, 0\n"
    "s_add_u32 s41, s40, 72704\n"
    "s_add_u32 s42, s40, 145408\n"
    "s_add_u32 s43, s40, 218112\n"
    "s_mov_b32 s44, 0\n"
    "s_mov_b32 s45, 0\n"
    "s_mov_b32 s46, 8192\n"
    "s_mov_b32 s47, 16384\n"
    "s_mov_b32 s48, 24576\n"
    "s_and_b32 s85, s85, 0xffff\n"
    "s_mov_b32 s86, 2359296\n"
    "s_mov_b32 s87, 0x20000\n"
    "v_mov_b32 v200, 0x3f803f80\n"
    "v_mov_b32 v201, 0x3f803f80\n"
    "v_mov_b32 v202, 0x3f803f80\n"
    "v_mov_b32 v203, 0x3f803f80\n"
    "s_lshl_b32 s60, s81, 1\n"
    "s_and_b32 s60, s60, 14\n"
    "s_bfe_u32 s61, s81, 0x10003\n"
    "s_or_b32 s60, s60, s61\n"
    "s_lshl_b32 s63, s61, 8\n"
    "v_lshrrev_b32_e32 v152, 3, v239\n"
    "v_and_b32_e32 v153, 7, v239\n"
    "s_and_b32 s54, s80, 3\n"
    "s_mul_i32 s62, s60, 0x120\n"
    "s_xor_b32 s63, s63, 0x100\n"
    "v_bitop3_b32 v153, v152, v153, 6 bitop3:0x6c\n"
    "v_mul_u32_u24_e32 v152, 0x2400, v152\n"
    "s_add_i32 s62, s62, s63\n"
    "s_add_i32 s63, s80, 2\n"
    "v_lshl_or_b32 v152, s54, 7, v152\n"
    "s_lshr_b32 s64, s63, 3\n"
    "s_mulk_i32 s60, 0x240\n"
    "v_lshlrev_b32_e32 v153, 4, v153\n"
    "v_lshl_or_b32 v152, s61, 6, v152\n"
    "v_and_b32_e32 v150, 3, v239\n"
    "v_lshrrev_b32_e32 v151, 2, v239\n"
    "s_and_b32 s65, s80, 1\n"
    "v_add3_u32 v213, v152, s60, v153\n"
    "v_bfe_u32 v152, v239, 4, 1\n"
    "s_lshl_b32 s61, s64, 1\n"
    "v_lshl_or_b32 v151, s65, 4, v151\n"
    "v_bitop3_b32 v150, s61, v150, v152 bitop3:0x36\n"
    "v_mul_u32_u24_e32 v151, 0x2400, v151\n"
    "s_lshl_b32 s60, s62, 1\n"
    "v_lshlrev_b32_e32 v150, 4, v150\n"
    "v_add3_u32 v214, s60, v151, v150\n"
    "s_lshl_b32 s60, s65, 11\n"
    "s_lshl_b32 s61, s64, 10\n"
    "s_not_b32 s63, s63\n"
    "s_or_b32 s50, s61, s60\n"
    "s_lshl_b32 s51, s80, 10\n"
    "s_mul_i32 s76, s81, 0x2400\n"
    "s_lshr_b32 s60, s80, 1\n"
    "s_bfe_u32 s61, s81, 0x20002\n"
    "s_lshr_b32 s55, s80, 2\n"
    "s_bfe_u32 s53, s63, 0x10002\n"
    "s_lshl_b32 s49, s80, 12\n"
    "s_bitset1_b32 s50, 15\n"
    "s_add_i32 s51, s51, 0x9000\n"
    "s_bitset1_b32 s76, 26\n"
    "s_xor_b32 s77, s61, s60\n"
    "s_lshl_b32 s60, s80, 1\n"
    "s_and_b32 s61, s60, 12\n"
    "v_lshrrev_b32_e32 v151, 2, v239\n"
    "s_and_b32 s62, s81, 15\n"
    "v_lshrrev_b32_e32 v150, 4, v239\n"
    "v_and_b32_e32 v152, 3, v239\n"
    "v_and_or_b32 v153, v151, 3, s61\n"
    "s_bitcmp1_b32 s80, 0\n"
    "s_cselect_b32 s63, 0x1200, 0\n"
    "v_lshlrev_b32_e32 v150, 6, v150\n"
    "v_lshlrev_b32_e32 v152, 4, v152\n"
    "v_cmp_eq_u32_e32 vcc, s62, v153\n"
    "v_bfe_u32 v153, v239, 1, 1\n"
    "v_or3_b32 v150, s63, v150, v152\n"
    "s_and_b32 s63, s80, 3\n"
    "v_and_or_b32 v154, v151, 6, v153\n"
    "s_and_b32 s65, s81, 7\n"
    "v_lshlrev_b32_e32 v154, 4, v154\n"
    "s_lshl_b32 s64, s63, 5\n"
    "s_lshl_b32 s66, s65, 1\n"
    "s_bfe_u32 s67, s81, 0x10003\n"
    "v_xor_b32_e32 v155, s64, v154\n"
    "s_add_i32 s60, s60, 4\n"
    "v_and_b32_e32 v156, 8, v151\n"
    "s_lshl_b32 s64, s80, 2\n"
    "s_or_b32 s66, s66, s67\n"
    "s_lshl_b32 s73, s67, 8\n"
    "s_and_b32 s60, s60, 16\n"
    "v_bfe_u32 v158, v239, 3, 2\n"
    "s_mul_i32 s72, s66, 0x120\n"
    "s_xor_b32 s73, s73, 0x100\n"
    "v_and_or_b32 v156, s64, 4, v156\n"
    "v_lshrrev_b32_e32 v157, 3, v239\n"
    "s_add_i32 s73, s72, s73\n"
    "v_or3_b32 v156, v156, v158, s60\n"
    "v_and_b32_e32 v158, 7, v239\n"
    "s_add_i32 s60, s67, s80\n"
    "v_or_b32_e32 v156, s73, v156\n"
    "v_bitop3_b32 v158, v157, v158, 6 bitop3:0x6c\n"
    "s_lshl_b32 s60, s60, 5\n"
    "v_mul_u32_u24_e32 v156, 0x480, v156\n"
    "v_lshlrev_b32_e32 v158, 4, v158\n"
    "s_add_i32 s60, s60, s72\n"
    "v_or_b32_e32 v217, v156, v158\n"
    "v_or_b32_e32 v156, s60, v157\n"
    "s_lshr_b32 s60, s81, 4\n"
    "s_lshl_b32 s64, s60, 1\n"
    "s_lshr_b32 s72, s80, 2\n"
    "s_or_b32 s64, s64, s72\n"
    "v_bfrev_b32_e32 v152, 1\n"
    "s_xor_b32 s64, s64, 30\n"
    "v_cndmask_b32_e32 v219, v152, v150, vcc\n"
    "v_lshlrev_b32_e32 v150, 3, v239\n"
    "s_mulk_i32 s64, 0x240\n"
    "s_lshl_b32 s74, s63, 3\n"
    "v_and_b32_e32 v150, 8, v150\n"
    "v_mul_u32_u24_e32 v156, 0x480, v156\n"
    "s_or_b32 s64, s64, s74\n"
    "v_lshl_or_b32 v152, v151, 7, v150\n"
    "v_or_b32_e32 v216, v156, v158\n"
    "v_or_b32_e32 v156, s64, v157\n"
    "v_lshlrev_b32_e32 v151, 6, v151\n"
    "v_lshl_or_b32 v215, v156, 7, v158\n"
    "v_lshl_or_b32 v151, v153, 10, v151\n"
    "v_bfe_u32 v153, v239, 4, 1\n"
    "v_and_b32_e32 v156, 2, v239\n"
    "v_bitop3_b32 v156, v153, s63, v156 bitop3:0x36\n"
    "v_lshl_or_b32 v151, v156, 4, v151\n"
    "s_mov_b32 s64, 0x8000\n"
    "v_or3_b32 v212, v151, v150, s64\n"
    "s_lshl_b32 s64, s72, 12\n"
    "s_add_i32 s64, s64, 0x9000\n"
    "v_or_b32_e32 v150, s64, v152\n"
    "s_movk_i32 s64, 0x60\n"
    "v_or_b32_e32 v208, v155, v150\n"
    "v_bitop3_b32 v211, v155, s64, v150 bitop3:0x36\n"
    "v_bitop3_b32 v210, v155, 64, v150 bitop3:0x36\n"
    "v_bitop3_b32 v209, v155, 32, v150 bitop3:0x36\n"
    "v_lshl_or_b32 v150, s63, 12, v152\n"
    "s_lshl_b32 s63, s63, 8\n"
    "v_or_b32_e32 v204, v154, v150\n"
    "v_bitop3_b32 v207, v154, s64, v150 bitop3:0x36\n"
    "v_bitop3_b32 v206, v154, 64, v150 bitop3:0x36\n"
    "v_bitop3_b32 v205, v154, 32, v150 bitop3:0x36\n"
    "v_and_or_b32 v150, v239, 48, s63\n"
    "s_mul_i32 s64, s66, 0x480\n"
    "v_lshl_or_b32 v150, s67, 7, v150\n"
    "v_add_u32_e32 v150, s64, v150\n"
    "s_movk_i32 s61, 0x1200\n"
    "v_add_u32_e32 v252, 0x8000, v150\n"
    "v_lshrrev_b32_e32 v150, 5, v239\n"
    "v_mov_b32_e32 v151, s73\n"
    "v_mad_u32_u24 v150, v150, s61, v151\n"
    "v_lshlrev_b32_e32 v151, 2, v153\n"
    "v_or3_b32 v150, v150, v151, s74\n"
    "v_mov_b32_e32 v151, 0x8000\n"
    "s_lshl_b32 s61, s65, 7\n"
    "v_lshl_add_u32 v253, v150, 2, v151\n"
    "v_lshl_or_b32 v150, v239, 10, s61\n"
    "s_lshl_b32 s61, s60, 8\n"
    "v_lshl_add_u32 v151, v239, 7, s61\n"
    "v_or_b32_e32 v218, v155, v152\n"
    "v_add_u32_e32 v151, 0x5700, v151\n"
    "v_mov_b32_e32 v152, 0x6000\n"
    "v_cmp_gt_u32_e32 vcc, 20, v239\n"
    "v_add_u32_e32 v150, 0x2a000, v150\n"
    "s_lshl_b32 s60, s60, 3\n"
    "v_cndmask_b32_e32 v151, v152, v151, vcc\n"
    "v_cmp_gt_u32_e32 vcc, 18, v239\n"
    "s_or_b32 s60, s60, s80\n"
    "s_mul_i32 s66, s66, 0x51000\n"
    "v_cndmask_b32_e32 v254, v151, v150, vcc\n"
    "v_min_u32_e32 v150, 20, v239\n"
    "v_mad_u64_u32 v[150:151], s[60:61], s60, 21, v[150:151]\n"
    "s_lshl_b32 s61, s62, 10\n"
    "s_lshl_b32 s62, s81, 7\n"
    "s_and_b32 s62, s62, 0x4000\n"
    "s_or_b32 s61, s61, s62\n"
    "s_mul_i32 s60, s80, 0x1c00\n"
    "s_xor_b32 s61, s61, 0x4000\n"
    "s_add_i32 s60, s60, 0x14c00\n"
    "s_add_i32 s61, s61, 0x22000\n"
    "s_cmp_eq_u32 s80, 0\n"
    "v_lshl_add_u32 v250, v150, 7, s66\n"
    "s_cselect_b32 s60, s61, s60\n"
    "v_min_u32_e32 v150, 55, v239\n"
    "v_lshl_add_u32 v249, v150, 7, s60\n"
    "s_mov_b32 s68, 0x00003f80\n"
    "s_mov_b32 s69, 0x3f800000\n"
    "s_waitcnt vmcnt(17)\n"
    "v_dot2_f32_bf16 v52, v64, s68, 0\n"
    "v_dot2_f32_bf16 v53, v64, s69, 0\n"
    "v_dot2_f32_bf16 v54, v65, s68, 0\n"
    "v_dot2_f32_bf16 v55, v65, s69, 0\n"
    "v_dot2_f32_bf16 v56, v66, s68, 0\n"
    "v_dot2_f32_bf16 v57, v66, s69, 0\n"
    "v_dot2_f32_bf16 v58, v67, s68, 0\n"
    "v_dot2_f32_bf16 v59, v67, s69, 0\n"
    "s_waitcnt vmcnt(16)\n"
    "v_dot2_f32_bf16 v52, v68, s68, v52\n"
    "v_dot2_f32_bf16 v53, v68, s69, v53\n"
    "v_dot2_f32_bf16 v54, v69, s68, v54\n"
    "v_dot2_f32_bf16 v55, v69, s69, v55\n"
    "v_dot2_f32_bf16 v56, v70, s68, v56\n"
    "v_dot2_f32_bf16 v57, v70, s69, v57\n"
    "v_dot2_f32_bf16 v58, v71, s68, v58\n"
    "v_dot2_f32_bf16 v59, v71, s69, v59\n"
    "s_waitcnt vmcnt(15)\n"
    "v_dot2_f32_bf16 v52, v72, s68, v52\n"
    "v_dot2_f32_bf16 v53, v72, s69, v53\n"
    "v_dot2_f32_bf16 v54, v73, s68, v54\n"
    "v_dot2_f32_bf16 v55, v73, s69, v55\n"
    "v_dot2_f32_bf16 v56, v74, s68, v56\n"
    "v_dot2_f32_bf16 v57, v74, s69, v57\n"
    "v_dot2_f32_bf16 v58, v75, s68, v58\n"
    "v_dot2_f32_bf16 v59, v75, s69, v59\n"
    "s_waitcnt vmcnt(14)\n"
    "v_dot2_f32_bf16 v52, v76, s68, v52\n"
    "v_dot2_f32_bf16 v53, v76, s69, v53\n"
    "v_dot2_f32_bf16 v54, v77, s68, v54\n"
    "v_dot2_f32_bf16 v55, v77, s69, v55\n"
    "v_dot2_f32_bf16 v56, v78, s68, v56\n"
    "v_dot2_f32_bf16 v57, v78, s69, v57\n"
    "v_dot2_f32_bf16 v58, v79, s68, v58\n"
    "v_dot2_f32_bf16 v59, v79, s69, v59\n"
    "s_waitcnt vmcnt(13)\n"
    "v_dot2_f32_bf16 v52, v80, s68, v52\n"
    "v_dot2_f32_bf16 v53, v80, s69, v53\n"
    "v_dot2_f32_bf16 v54, v81, s68, v54\n"
    "v_dot2_f32_bf16 v55, v81, s69, v55\n"
    "v_dot2_f32_bf16 v56, v82, s68, v56\n"
    "v_dot2_f32_bf16 v57, v82, s69, v57\n"
    "v_dot2_f32_bf16 v58, v83, s68, v58\n"
    "v_dot2_f32_bf16 v59, v83, s69, v59\n"
    "s_waitcnt vmcnt(12)\n"
    "v_dot2_f32_bf16 v52, v84, s68, v52\n"
    "v_dot2_f32_bf16 v53, v84, s69, v53\n"
    "v_dot2_f32_bf16 v54, v85, s68, v54\n"
    "v_dot2_f32_bf16 v55, v85, s69, v55\n"
    "v_dot2_f32_bf16 v56, v86, s68, v56\n"
    "v_dot2_f32_bf16 v57, v86, s69, v57\n"
    "v_dot2_f32_bf16 v58, v87, s68, v58\n"
    "v_dot2_f32_bf16 v59, v87, s69, v59\n"
    "s_waitcnt vmcnt(11)\n"
    "v_dot2_f32_bf16 v52, v88, s68, v52\n"
    "v_dot2_f32_bf16 v53, v88, s69, v53\n"
    "v_dot2_f32_bf16 v54, v89, s68, v54\n"
    "v_dot2_f32_bf16 v55, v89, s69, v55\n"
    "v_dot2_f32_bf16 v56, v90, s68, v56\n"
    "v_dot2_f32_bf16 v57, v90, s69, v57\n"
    "v_dot2_f32_bf16 v58, v91, s68, v58\n"
    "v_dot2_f32_bf16 v59, v91, s69, v59\n"
    "s_waitcnt vmcnt(10)\n"
    "v_dot2_f32_bf16 v52, v92, s68, v52\n"
    "v_dot2_f32_bf16 v53, v92, s69, v53\n"
    "v_dot2_f32_bf16 v54, v93, s68, v54\n"
    "v_dot2_f32_bf16 v55, v93, s69, v55\n"
    "v_dot2_f32_bf16 v56, v94, s68, v56\n"
    "v_dot2_f32_bf16 v57, v94, s69, v57\n"
    "v_dot2_f32_bf16 v58, v95, s68, v58\n"
    "v_dot2_f32_bf16 v59, v95, s69, v59\n"
    "s_waitcnt vmcnt(9)\n"
    "v_dot2_f32_bf16 v52, v96, s68, v52\n"
    "v_dot2_f32_bf16 v53, v96, s69, v53\n"
    "v_dot2_f32_bf16 v54, v97, s68, v54\n"
    "v_dot2_f32_bf16 v55, v97, s69, v55\n"
    "v_dot2_f32_bf16 v56, v98, s68, v56\n"
    "v_dot2_f32_bf16 v57, v98, s69, v57\n"
    "v_dot2_f32_bf16 v58, v99, s68, v58\n"
    "v_dot2_f32_bf16 v59, v99, s69, v59\n"
    "s_waitcnt vmcnt(8)\n"
    "v_dot2_f32_bf16 v52, v100, s68, v52\n"
    "v_dot2_f32_bf16 v53, v100, s69, v53\n"
    "v_dot2_f32_bf16 v54, v101, s68, v54\n"
    "v_dot2_f32_bf16 v55, v101, s69, v55\n"
    "v_dot2_f32_bf16 v56, v102, s68, v56\n"
    "v_dot2_f32_bf16 v57, v102, s69, v57\n"
    "v_dot2_f32_bf16 v58, v103, s68, v58\n"
    "v_dot2_f32_bf16 v59, v103, s69, v59\n"
    "s_waitcnt vmcnt(7)\n"
    "v_dot2_f32_bf16 v52, v104, s68, v52\n"
    "v_dot2_f32_bf16 v53, v104, s69, v53\n"
    "v_dot2_f32_bf16 v54, v105, s68, v54\n"
    "v_dot2_f32_bf16 v55, v105, s69, v55\n"
    "v_dot2_f32_bf16 v56, v106, s68, v56\n"
    "v_dot2_f32_bf16 v57, v106, s69, v57\n"
    "v_dot2_f32_bf16 v58, v107, s68, v58\n"
    "v_dot2_f32_bf16 v59, v107, s69, v59\n"
    "s_waitcnt vmcnt(6)\n"
    "v_dot2_f32_bf16 v52, v108, s68, v52\n"
    "v_dot2_f32_bf16 v53, v108, s69, v53\n"
    "v_dot2_f32_bf16 v54, v109, s68, v54\n"
    "v_dot2_f32_bf16 v55, v109, s69, v55\n"
    "v_dot2_f32_bf16 v56, v110, s68, v56\n"
    "v_dot2_f32_bf16 v57, v110, s69, v57\n"
    "v_dot2_f32_bf16 v58, v111, s68, v58\n"
    "v_dot2_f32_bf16 v59, v111, s69, v59\n"
    "s_waitcnt vmcnt(5)\n"
    "v_dot2_f32_bf16 v52, v112, s68, v52\n"
    "v_dot2_f32_bf16 v53, v112, s69, v53\n"
    "v_dot2_f32_bf16 v54, v113, s68, v54\n"
    "v_dot2_f32_bf16 v55, v113, s69, v55\n"
    "v_dot2_f32_bf16 v56, v114, s68, v56\n"
    "v_dot2_f32_bf16 v57, v114, s69, v57\n"
    "v_dot2_f32_bf16 v58, v115, s68, v58\n"
    "v_dot2_f32_bf16 v59, v115, s69, v59\n"
    "s_waitcnt vmcnt(4)\n"
    "v_dot2_f32_bf16 v52, v116, s68, v52\n"
    "v_dot2_f32_bf16 v53, v116, s69, v53\n"
    "v_dot2_f32_bf16 v54, v117, s68, v54\n"
    "v_dot2_f32_bf16 v55, v117, s69, v55\n"
    "v_dot2_f32_bf16 v56, v118, s68, v56\n"
    "v_dot2_f32_bf16 v57, v118, s69, v57\n"
    "v_dot2_f32_bf16 v58, v119, s68, v58\n"
    "v_dot2_f32_bf16 v59, v119, s69, v59\n"
    "s_waitcnt vmcnt(3)\n"
    "v_dot2_f32_bf16 v52, v120, s68, v52\n"
    "v_dot2_f32_bf16 v53, v120, s69, v53\n"
    "v_dot2_f32_bf16 v54, v121, s68, v54\n"
    "v_dot2_f32_bf16 v55, v121, s69, v55\n"
    "v_dot2_f32_bf16 v56, v122, s68, v56\n"
    "v_dot2_f32_bf16 v57, v122, s69, v57\n"
    "v_dot2_f32_bf16 v58, v123, s68, v58\n"
    "v_dot2_f32_bf16 v59, v123, s69, v59\n"
    "s_waitcnt vmcnt(2)\n"
    "v_dot2_f32_bf16 v52, v124, s68, v52\n"
    "v_dot2_f32_bf16 v53, v124, s69, v53\n"
    "v_dot2_f32_bf16 v54, v125, s68, v54\n"
    "v_dot2_f32_bf16 v55, v125, s69, v55\n"
    "v_dot2_f32_bf16 v56, v126, s68, v56\n"
    "v_dot2_f32_bf16 v57, v126, s69, v57\n"
    "v_dot2_f32_bf16 v58, v127, s68, v58\n"
    "v_dot2_f32_bf16 v59, v127, s69, v59\n"
    "s_waitcnt vmcnt(1)\n"
    "v_dot2_f32_bf16 v52, v128, s68, v52\n"
    "v_dot2_f32_bf16 v53, v128, s69, v53\n"
    "v_dot2_f32_bf16 v54, v129, s68, v54\n"
    "v_dot2_f32_bf16 v55, v129, s69, v55\n"
    "v_dot2_f32_bf16 v56, v130, s68, v56\n"
    "v_dot2_f32_bf16 v57, v130, s69, v57\n"
    "v_dot2_f32_bf16 v58, v131, s68, v58\n"
    "v_dot2_f32_bf16 v59, v131, s69, v59\n"
    "s_waitcnt vmcnt(0)\n"
    "v_dot2_f32_bf16 v52, v132, s68, v52\n"
    "v_dot2_f32_bf16 v53, v132, s69, v53\n"
    "v_dot2_f32_bf16 v54, v133, s68, v54\n"
    "v_dot2_f32_bf16 v55, v133, s69, v55\n"
    "v_dot2_f32_bf16 v56, v134, s68, v56\n"
    "v_dot2_f32_bf16 v57, v134, s69, v57\n"
    "v_dot2_f32_bf16 v58, v135, s68, v58\n"
    "v_dot2_f32_bf16 v59, v135, s69, v59\n"
    "v_add_f32_dpp v52, v52, v52 row_ror:8 row_mask:0xf bank_mask:0xf\n"
    "v_add_f32_dpp v53, v53, v53 row_ror:8 row_mask:0xf bank_mask:0xf\n"
    "v_add_f32_dpp v54, v54, v54 row_ror:8 row_mask:0xf bank_mask:0xf\n"
    "v_add_f32_dpp v55, v55, v55 row_ror:8 row_mask:0xf bank_mask:0xf\n"
    "v_add_f32_dpp v56, v56, v56 row_ror:8 row_mask:0xf bank_mask:0xf\n"
    "v_add_f32_dpp v57, v57, v57 row_ror:8 row_mask:0xf bank_mask:0xf\n"
    "v_add_f32_dpp v58, v58, v58 row_ror:8 row_mask:0xf bank_mask:0xf\n"
    "v_add_f32_dpp v59, v59, v59 row_ror:8 row_mask:0xf bank_mask:0xf\n"
    "ds_write_b128 v47, v[52:55]\n"
    "ds_write_b128 v47, v[56:59] offset:16\n"
    "s_waitcnt lgkmcnt(0)\n"
    "s_barrier\n"
    "s_cmp_ge_u32 s80, 4\n"
    "s_cbranch_scc1 Lrc121_%=\n"
    "s_cmp_lt_u32 s80, 2\n"
    "s_cbranch_scc1 Lrc121_%=\n"
    "ds_read_b32 v64, v48 offset:0\n"
    "ds_read_b32 v65, v48 offset:512\n"
    "ds_read_b32 v66, v48 offset:1024\n"
    "ds_read_b32 v67, v48 offset:1536\n"
    "ds_read_b32 v68, v48 offset:2048\n"
    "ds_read_b32 v69, v48 offset:2560\n"
    "ds_read_b32 v70, v48 offset:3072\n"
    "ds_read_b32 v71, v48 offset:3584\n"
    "ds_read_b32 v72, v48 offset:4096\n"
    "ds_read_b32 v73, v48 offset:4608\n"
    "ds_read_b32 v74, v48 offset:5120\n"
    "ds_read_b32 v75, v48 offset:5632\n"
    "ds_read_b32 v76, v48 offset:6144\n"
    "ds_read_b32 v77, v48 offset:6656\n"
    "ds_read_b32 v78, v48 offset:7168\n"
    "ds_read_b32 v79, v48 offset:7680\n"
    "s_waitcnt lgkmcnt(0)\n"
    "v_add_f32 v64, v64, v72\n"
    "v_add_f32 v65, v65, v73\n"
    "v_add_f32 v66, v66, v74\n"
    "v_add_f32 v67, v67, v75\n"
    "v_add_f32 v68, v68, v76\n"
    "v_add_f32 v69, v69, v77\n"
    "v_add_f32 v70, v70, v78\n"
    "v_add_f32 v71, v71, v79\n"
    "v_add_f32 v64, v64, v68\n"
    "v_add_f32 v65, v65, v69\n"
    "v_add_f32 v66, v66, v70\n"
    "v_add_f32 v67, v67, v71\n"
    "v_add_f32 v64, v64, v66\n"
    "v_add_f32 v65, v65, v67\n"
    "v_add_f32 v64, v64, v65\n"
    "buffer_store_dword v64, v50, s[36:39], 0 offen sc1\n"
    "s_mov_b32 s79, s70\n"
    "s_mov_b32 s82, 2\n"
    "s_branch Lrc121_%=\n"
    "Lw2125_%=:\n"
    "s_lshr_b32 s44, s81, 1\n"
    "s_and_b32 s44, s44, 0x7c\n"
    "s_lshr_b32 s45, s80, 1\n"
    "s_add_i32 s44, s44, s45\n"
    "v_min_u32_e32 v150, 62, v239\n"
    "v_mul_u32_u24_e32 v151, 57, v150\n"
    "s_mul_i32 s44, s44, 7\n"
    "v_lshrrev_b32_e32 v151, 9, v151\n"
    "s_addk_i32 s44, 0xfd0c\n"
    "v_add_u32_e32 v152, s44, v151\n"
    "s_movk_i32 s44, 0x2400\n"
    "v_mul_lo_u32 v152, v152, s44\n"
    "s_and_b32 s44, s81, 7\n"
    "s_mulk_i32 s44, 0x480\n"
    "v_mad_i32_i24 v150, v151, -9, v150\n"
    "v_lshl_add_u32 v150, v150, 7, s44\n"
    "s_mov_b32 s44, 0x3d5000\n"
    "v_add3_u32 v46, v152, v150, s44\n"
    "s_bitcmp0_b32 s80, 0\n"
    "s_cselect_b32 s20, s78, s28\n"
    "s_cselect_b32 s21, s79, s29\n"
    "s_and_b32 s21, s21, 0xffff\n"
    "s_mov_b32 s22, 5308416\n"
    "s_mov_b32 s23, 0x20000\n"
    "s_mov_b32 s41, 0\n"
    "buffer_load_dword v48, v46, s[20:23], s41 offen\n"
    "s_add_u32 s41, s41, 1290240\n"
    "s_cmp_lt_u32 s80, 4\n"
    "s_cselect_b32 s20, s78, s28\n"
    "s_cselect_b32 s21, s79, s29\n"
    "s_and_b32 s21, s21, 0xffff\n"
    "s_mov_b32 s22, 5308416\n"
    "s_mov_b32 s23, 0x20000\n"
    "s_mov_b32 s28, s30\n"
    "s_and_b32 s29, s31, 0xffff\n"
    "s_mov_b32 s30, 5308416\n"
    "s_mov_b32 s31, 0x20000\n"
    "s_mov_b32 s32, s34\n"
    "s_and_b32 s33, s35, 0xffff\n"
    "s_mov_b32 s34, 69468160\n"
    "s_mov_b32 s35, 0x20000\n"
    "s_cmp_lt_u32 s80, 4\n"
    "s_cselect_b32 s40, 884736, 0\n"
    "s_add_u32 s41, s40, 72704\n"
    "s_add_u32 s42, s40, 145408\n"
    "s_add_u32 s43, s40, 218112\n"
    "s_mov_b32 s44, 0\n"
    "s_mov_b32 s45, 0\n"
    "s_mov_b32 s46, 8192\n"
    "s_mov_b32 s47, 16384\n"
    "s_mov_b32 s48, 24576\n"
    "s_and_b32 s85, s85, 0xffff\n"
    "s_mov_b32 s86, 2359296\n"
    "s_mov_b32 s87, 0x20000\n"
    "v_mov_b32 v200, 0x3f803f80\n"
    "v_mov_b32 v201, 0x3f803f80\n"
    "v_mov_b32 v202, 0x3f803f80\n"
    "v_mov_b32 v203, 0x3f803f80\n"
    "s_lshl_b32 s60, s81, 1\n"
    "s_and_b32 s60, s60, 14\n"
    "s_bfe_u32 s61, s81, 0x10003\n"
    "s_or_b32 s60, s60, s61\n"
    "s_lshl_b32 s63, s61, 8\n"
    "v_lshrrev_b32_e32 v152, 3, v239\n"
    "v_and_b32_e32 v153, 7, v239\n"
    "s_and_b32 s54, s80, 3\n"
    "s_mul_i32 s62, s60, 0x120\n"
    "s_xor_b32 s63, s63, 0x100\n"
    "v_bitop3_b32 v153, v152, v153, 6 bitop3:0x6c\n"
    "v_mul_u32_u24_e32 v152, 0x2400, v152\n"
    "s_add_i32 s62, s62, s63\n"
    "s_add_i32 s63, s80, 2\n"
    "v_lshl_or_b32 v152, s54, 7, v152\n"
    "s_lshr_b32 s64, s63, 3\n"
    "s_mulk_i32 s60, 0x240\n"
    "v_lshlrev_b32_e32 v153, 4, v153\n"
    "v_lshl_or_b32 v152, s61, 6, v152\n"
    "v_and_b32_e32 v150, 3, v239\n"
    "v_lshrrev_b32_e32 v151, 2, v239\n"
    "s_and_b32 s65, s80, 1\n"
    "v_add3_u32 v213, v152, s60, v153\n"
    "v_bfe_u32 v152, v239, 4, 1\n"
    "s_lshl_b32 s61, s64, 1\n"
    "v_lshl_or_b32 v151, s65, 4, v151\n"
    "v_bitop3_b32 v150, s61, v150, v152 bitop3:0x36\n"
    "v_mul_u32_u24_e32 v151, 0x2400, v151\n"
    "s_lshl_b32 s60, s62, 1\n"
    "v_lshlrev_b32_e32 v150, 4, v150\n"
    "v_add3_u32 v214, s60, v151, v150\n"
    "s_lshl_b32 s60, s65, 11\n"
    "s_lshl_b32 s61, s64, 10\n"
    "s_not_b32 s63, s63\n"
    "s_or_b32 s50, s61, s60\n"
    "s_lshl_b32 s51, s80, 10\n"
    "s_mul_i32 s76, s81, 0x2400\n"
    "s_lshr_b32 s60, s80, 1\n"
    "s_bfe_u32 s61, s81, 0x20002\n"
    "s_lshr_b32 s55, s80, 2\n"
    "s_bfe_u32 s53, s63, 0x10002\n"
    "s_lshl_b32 s49, s80, 12\n"
    "s_bitset1_b32 s50, 15\n"
    "s_add_i32 s51, s51, 0x9000\n"
    "s_bitset1_b32 s76, 26\n"
    "s_xor_b32 s77, s61, s60\n"
    "s_lshl_b32 s60, s80, 1\n"
    "s_and_b32 s61, s60, 12\n"
    "v_lshrrev_b32_e32 v151, 2, v239\n"
    "s_and_b32 s62, s81, 15\n"
    "v_lshrrev_b32_e32 v150, 4, v239\n"
    "v_and_b32_e32 v152, 3, v239\n"
    "v_and_or_b32 v153, v151, 3, s61\n"
    "s_bitcmp1_b32 s80, 0\n"
    "s_cselect_b32 s63, 0x1200, 0\n"
    "v_lshlrev_b32_e32 v150, 6, v150\n"
    "v_lshlrev_b32_e32 v152, 4, v152\n"
    "v_cmp_eq_u32_e32 vcc, s62, v153\n"
    "v_bfe_u32 v153, v239, 1, 1\n"
    "v_or3_b32 v150, s63, v150, v152\n"
    "s_and_b32 s63, s80, 3\n"
    "v_and_or_b32 v154, v151, 6, v153\n"
    "s_and_b32 s65, s81, 7\n"
    "v_lshlrev_b32_e32 v154, 4, v154\n"
    "s_lshl_b32 s64, s63, 5\n"
    "s_lshl_b32 s66, s65, 1\n"
    "s_bfe_u32 s67, s81, 0x10003\n"
    "v_xor_b32_e32 v155, s64, v154\n"
    "s_add_i32 s60, s60, 4\n"
    "v_and_b32_e32 v156, 8, v151\n"
    "s_lshl_b32 s64, s80, 2\n"
    "s_or_b32 s66, s66, s67\n"
    "s_lshl_b32 s73, s67, 8\n"
    "s_and_b32 s60, s60, 16\n"
    "v_bfe_u32 v158, v239, 3, 2\n"
    "s_mul_i32 s72, s66, 0x120\n"
    "s_xor_b32 s73, s73, 0x100\n"
    "v_and_or_b32 v156, s64, 4, v156\n"
    "v_lshrrev_b32_e32 v157, 3, v239\n"
    "s_add_i32 s73, s72, s73\n"
    "v_or3_b32 v156, v156, v158, s60\n"
    "v_and_b32_e32 v158, 7, v239\n"
    "s_add_i32 s60, s67, s80\n"
    "v_or_b32_e32 v156, s73, v156\n"
    "v_bitop3_b32 v158, v157, v158, 6 bitop3:0x6c\n"
    "s_lshl_b32 s60, s60, 5\n"
    "v_mul_u32_u24_e32 v156, 0x480, v156\n"
    "v_lshlrev_b32_e32 v158, 4, v158\n"
    "s_add_i32 s60, s60, s72\n"
    "v_or_b32_e32 v217, v156, v158\n"
    "v_or_b32_e32 v156, s60, v157\n"
    "s_lshr_b32 s60, s81, 4\n"
    "s_lshl_b32 s64, s60, 1\n"
    "s_lshr_b32 s72, s80, 2\n"
    "s_or_b32 s64, s64, s72\n"
    "v_bfrev_b32_e32 v152, 1\n"
    "s_xor_b32 s64, s64, 30\n"
    "v_cndmask_b32_e32 v219, v152, v150, vcc\n"
    "v_lshlrev_b32_e32 v150, 3, v239\n"
    "s_mulk_i32 s64, 0x240\n"
    "s_lshl_b32 s74, s63, 3\n"
    "v_and_b32_e32 v150, 8, v150\n"
    "v_mul_u32_u24_e32 v156, 0x480, v156\n"
    "s_or_b32 s64, s64, s74\n"
    "v_lshl_or_b32 v152, v151, 7, v150\n"
    "v_or_b32_e32 v216, v156, v158\n"
    "v_or_b32_e32 v156, s64, v157\n"
    "v_lshlrev_b32_e32 v151, 6, v151\n"
    "v_lshl_or_b32 v215, v156, 7, v158\n"
    "v_lshl_or_b32 v151, v153, 10, v151\n"
    "v_bfe_u32 v153, v239, 4, 1\n"
    "v_and_b32_e32 v156, 2, v239\n"
    "v_bitop3_b32 v156, v153, s63, v156 bitop3:0x36\n"
    "v_lshl_or_b32 v151, v156, 4, v151\n"
    "s_mov_b32 s64, 0x8000\n"
    "v_or3_b32 v212, v151, v150, s64\n"
    "s_lshl_b32 s64, s72, 12\n"
    "s_add_i32 s64, s64, 0x9000\n"
    "v_or_b32_e32 v150, s64, v152\n"
    "s_movk_i32 s64, 0x60\n"
    "v_or_b32_e32 v208, v155, v150\n"
    "v_bitop3_b32 v211, v155, s64, v150 bitop3:0x36\n"
    "v_bitop3_b32 v210, v155, 64, v150 bitop3:0x36\n"
    "v_bitop3_b32 v209, v155, 32, v150 bitop3:0x36\n"
    "v_lshl_or_b32 v150, s63, 12, v152\n"
    "s_lshl_b32 s63, s63, 8\n"
    "v_or_b32_e32 v204, v154, v150\n"
    "v_bitop3_b32 v207, v154, s64, v150 bitop3:0x36\n"
    "v_bitop3_b32 v206, v154, 64, v150 bitop3:0x36\n"
    "v_bitop3_b32 v205, v154, 32, v150 bitop3:0x36\n"
    "v_and_or_b32 v150, v239, 48, s63\n"
    "s_mul_i32 s64, s66, 0x480\n"
    "v_lshl_or_b32 v150, s67, 7, v150\n"
    "v_add_u32_e32 v150, s64, v150\n"
    "s_movk_i32 s61, 0x1200\n"
    "v_add_u32_e32 v252, 0x8000, v150\n"
    "v_lshrrev_b32_e32 v150, 5, v239\n"
    "v_mov_b32_e32 v151, s73\n"
    "v_mad_u32_u24 v150, v150, s61, v151\n"
    "v_lshlrev_b32_e32 v151, 2, v153\n"
    "v_or3_b32 v150, v150, v151, s74\n"
    "v_mov_b32_e32 v151, 0x8000\n"
    "s_lshl_b32 s61, s65, 7\n"
    "v_lshl_add_u32 v253, v150, 2, v151\n"
    "v_lshl_or_b32 v150, v239, 10, s61\n"
    "s_lshl_b32 s61, s60, 8\n"
    "v_lshl_add_u32 v151, v239, 7, s61\n"
    "v_or_b32_e32 v218, v155, v152\n"
    "v_add_u32_e32 v151, 0x5700, v151\n"
    "v_mov_b32_e32 v152, 0x6000\n"
    "v_cmp_gt_u32_e32 vcc, 20, v239\n"
    "v_add_u32_e32 v150, 0x2a000, v150\n"
    "s_lshl_b32 s60, s60, 3\n"
    "v_cndmask_b32_e32 v151, v152, v151, vcc\n"
    "v_cmp_gt_u32_e32 vcc, 18, v239\n"
    "s_or_b32 s60, s60, s80\n"
    "s_mul_i32 s66, s66, 0x51000\n"
    "v_cndmask_b32_e32 v254, v151, v150, vcc\n"
    "v_min_u32_e32 v150, 20, v239\n"
    "v_mad_u64_u32 v[150:151], s[60:61], s60, 21, v[150:151]\n"
    "s_lshl_b32 s61, s62, 10\n"
    "s_lshl_b32 s62, s81, 7\n"
    "s_and_b32 s62, s62, 0x4000\n"
    "s_or_b32 s61, s61, s62\n"
    "s_mul_i32 s60, s80, 0x1c00\n"
    "s_xor_b32 s61, s61, 0x4000\n"
    "s_add_i32 s60, s60, 0x14c00\n"
    "s_add_i32 s61, s61, 0x22000\n"
    "s_cmp_eq_u32 s80, 0\n"
    "v_lshl_add_u32 v250, v150, 7, s66\n"
    "s_cselect_b32 s60, s61, s60\n"
    "v_min_u32_e32 v150, 55, v239\n"
    "v_lshl_add_u32 v249, v150, 7, s60\n"
    "Lrc121_%=:\n"
    "s_cmp_ge_u32 s80, 4\n"
    "s_cbranch_scc1 Lhs19_%=\n"
    "s_branch Lpo20_%=\n"
    "Lend2_%=:\n"
    : "+{v239}"(tid)
    : "{s[0:1]}"(__builtin_amdgcn_kernarg_segment_ptr()), "{s81}"(blockIdx.x), "s"((unsigned)(size_t)smem)
    : "memory", "scc", "vcc", "v0", "v1", "v2", "v3", "v4", "v5", "v6", "v7", "v8", "v9", "v10", "v11", "v12", "v13", "v14", "v15", "v16", "v17", "v18", "v19", "v20", "v21", "v22", "v23", "v24", "v25", "v26", "v27", "v28", "v29", "v30", "v31", "v32", "v33", "v34", "v35", "v36", "v37", "v38", "v39", "v40", "v41", "v42", "v43", "v44", "v45", "v46", "v47", "v48", "v49", "v50", "v51", "v52", "v53", "v54", "v55", "v56", "v57", "v58", "v59", "v60", "v61", "v62", "v63", "v64", "v65", "v66", "v67", "v68", "v69", "v70", "v71", "v72", "v73", "v74", "v75", "v76", "v77", "v78", "v79", "v80", "v81", "v82", "v83", "v84", "v85", "v86", "v87", "v88", "v89", "v90", "v91", "v92", "v93", "v94", "v95", "v96", "v97", "v98", "v99", "v100", "v101", "v102", "v103", "v104", "v105", "v106", "v107", "v108", "v109", "v110", "v111", "v112", "v113", "v114", "v115", "v116", "v117", "v118", "v119", "v120", "v121", "v122", "v123", "v124", "v125", "v126", "v127", "v128", "v129", "v130", "v131", "v132", "v133", "v134", "v135", "v136", "v137", "v138", "v139", "v140", "v141", "v142", "v143", "v144", "v145", "v146", "v147", "v148", "v149", "v150", "v151", "v152", "v153", "v154", "v155", "v156", "v157", "v158", "v159", "v160", "v161", "v162", "v163", "v164", "v165", "v166", "v167", "v168", "v169", "v170", "v171", "v172", "v173", "v174", "v175", "v176", "v177", "v178", "v179", "v180", "v181", "v182", "v183", "v184", "v185", "v186", "v187", "v188", "v189", "v190", "v191", "v192", "v193", "v194", "v195", "v196", "v197", "v198", "v199", "v200", "v201", "v202", "v203", "v204", "v205", "v206", "v207", "v208", "v209", "v210", "v211", "v212", "v213", "v214", "v215", "v216", "v217", "v218", "v219", "v220", "v221", "v222", "v223", "v224", "v225", "v226", "v227", "v228", "v229", "v230", "v231", "v232", "v233", "v234", "v235", "v236", "v237", "v238", "v240", "v241", "v242", "v243", "v244", "v245", "v246", "v247", "v248", "v249", "v250", "v251", "v252", "v253", "v254", "v255", "s20", "s21", "s22", "s23", "s24", "s25", "s26", "s27", "s28", "s29", "s30", "s31", "s33", "s34", "s35", "s36", "s37", "s38", "s39", "s40", "s41", "s42", "s43", "s44", "s45", "s46", "s47", "s48", "s49", "s50", "s51", "s52", "s53", "s54", "s55", "s56", "s57", "s58", "s59", "s60", "s61", "s62", "s63", "s64", "s65", "s66", "s67", "s68", "s69", "s70", "s71", "s72", "s73", "s74", "s75", "s76", "s77", "s78", "s79", "s80", "s82", "s83", "s84", "s85", "s86", "s87");

}

extern "C" void kernel_launch(void* const* d_in, const int* in_sizes, int n_in, void* d_out, int out_size, void* d_ws, size_t ws_size, hipStream_t stream) {
  Args A;
  A.x = (const u16*)d_in[0]; A.lar = (const u16*)d_in[1]; A.aim = (const u16*)d_in[2]; A.cre = (const u16*)d_in[3]; A.cim = (const u16*)d_in[4];
  A.ldt = (const u16*)d_in[5]; A.dsk = (const u16*)d_in[6]; A.wg = (const u16*)d_in[8]; A.wu = (const u16*)d_in[9]; A.wd = (const u16*)d_in[10];
  A.ws = (char*)d_ws; A.out = (u16*)d_out;
  k_fused<<<256, 512, 0, stream>>>(A);
}
```
